```python
import torch

def _softmax(x, axis=-1):
    return torch.softmax(x, dim=axis)
def _gelu(x):
    return torch.nn.functional.gelu(x, approximate="tanh")
def _silu(x):
    return torch.nn.functional.silu(x)
def _sigmoid(x):
    return torch.sigmoid(x)
def _layernorm(x, axis=-1, eps=1e-5):
    m = x.mean(dim=axis, keepdim=True)
    v = x.var(dim=axis, keepdim=True, unbiased=False)
    return (x - m) / torch.sqrt(v + eps)
def _scatter_add(tgt, idx, src, axis=0):
    return tgt.clone().index_add_(axis, idx, src)
def _segment_sum(src, seg, n):
    out = torch.zeros((n,) + src.shape[1:], dtype=src.dtype)
    return out.index_add_(0, seg, src)
def _pad(x, pw):
    flat = tuple(v for pair in reversed(pw) for v in pair)
    return torch.nn.functional.pad(x, flat)

kernel_name = 'gated_mix__grouped_kv_mqa__spectral_gfnet__prompt'

def setup_inputs():

    _w = (lambda a: (lambda t: t.to(torch.bfloat16) if t.is_floating_point() else t)(torch.as_tensor(a)))
    def _A_setup():
        B, S, D, H = 1, 2048, 640, 32
        Dh = D // H
        return {
            "x": _w((lambda *s: torch.randn(*s, dtype=torch.bfloat16) * 0.05)(B, S, D)),
            "wq": _w((lambda *s: torch.randn(*s, dtype=torch.bfloat16) * 0.8)(D, H * Dh)),
            "wk": _w((lambda *s: torch.randn(*s, dtype=torch.bfloat16) * 0.8)(D, Dh)),
            "wv": _w((lambda *s: torch.randn(*s, dtype=torch.bfloat16) * 0.05)(D, Dh)),
            "wo": _w((lambda *s: torch.randn(*s, dtype=torch.bfloat16) * 0.05)(H * Dh, D)),
        }
    _A_in = _A_setup()

    def _B_setup():
        B, S, D = 1, 2048, 640
        Df = D // 2 + 1
        return {
            "x": _w((lambda *s: torch.randn(*s, dtype=torch.bfloat16) * 0.05)(B, S, D)),
            "w_r": _w((lambda *s: torch.randn(*s, dtype=torch.bfloat16) * 0.8)(S, Df) * 0.02),
            "w_i": _w((lambda *s: torch.randn(*s, dtype=torch.bfloat16) * 0.8)(S, Df) * 0.02),
        }
    _B_in = _B_setup()

    return {
        "A_x": _A_in["x"],
        "A_wq": _A_in["wq"],
        "A_wk": _A_in["wk"],
        "A_wv": _A_in["wv"],
        "A_wo": _A_in["wo"],
        "B_x": _B_in["x"],
        "B_w_r": _B_in["w_r"],
        "B_w_i": _B_in["w_i"],
        "gate_w": _w((lambda *s: torch.randn(*s, dtype=torch.bfloat16) * 0.05)(640) * 16.0),
    }

def reference(A_x, A_wq, A_wk, A_wv, A_wo, B_x, B_w_r, B_w_i, gate_w):
    def _A_ref(x, wq, wk, wv, wo):
        B, S, D = x.shape
        H = 32
        Dh = D // H
        q = (lambda x, *d: x.permute(*d))((x @ wq).reshape(B, S, H, Dh), 0, 2, 1, 3)
        k = x @ wk
        v = x @ wv
        scores = torch.einsum("bhqd,bkd->bhqk", q, k) * (Dh ** -0.5)
        p = _softmax(scores, axis=-1)
        o = torch.einsum("bhqk,bkd->bhqd", p, v)
        return (lambda x, *d: x.permute(*d))(o, 0, 2, 1, 3).reshape(B, S, H * Dh) @ wo

    def _B_ref(x, w_r, w_i):
        B, S, D = x.shape
        X = (lambda x, axes=(-2,-1): torch.fft.rfft2(x.float(), dim=axes))(x, axes=(-2, -1))
        W = w_r + 1j * w_i
        X = X * W
        y = (lambda x, s, axes=(-2,-1): torch.fft.irfft2(x, s=s, dim=axes).to(torch.bfloat16))(X, (S, D), axes=(-2, -1))
        return y

    x = A_x
    g = _sigmoid((x * gate_w).sum(-1, keepdims=True))
    a = _A_ref(x, A_wq, A_wk, A_wv, A_wo)
    b = _B_ref(x, B_w_r, B_w_i)
    return g * a + (1 - g) * b
```

```cpp
#include <hip/hip_runtime.h>
#include <stdint.h>
#include <utility>

typedef unsigned short u16;
typedef unsigned int u32;
typedef __attribute__((ext_vector_type(8))) __bf16 bf8;
typedef __attribute__((ext_vector_type(4))) short s4;
typedef __attribute__((ext_vector_type(16))) float f16v;
typedef __attribute__((ext_vector_type(4))) float f4v;
typedef __attribute__((ext_vector_type(2))) float f2v;
typedef __attribute__((ext_vector_type(2))) __bf16 bf2;
typedef __attribute__((ext_vector_type(4))) u32 u4v;
typedef __attribute__((ext_vector_type(2))) u32 u2v;
#define LAS __attribute__((address_space(3)))
#define DEVI __device__ __forceinline__

constexpr int S_ = 2048, D_ = 640;

constexpr size_t WS_Q = 0x000000;
constexpr size_t WS_X1 = 0x280000;
constexpr size_t WS_X2 = 0x500000;
constexpr size_t WS_O = 0x780000;
constexpr size_t WS_WT = 0xA00000;
constexpr size_t WS_KV = 0xD00000;
constexpr size_t WS_KM = 0xD40000;
constexpr size_t WS_G = 0xD41000;
constexpr int KVT = 2688;

constexpr double kPi = 3.14159265358979323846;
constexpr double cx_sin(double x) { double t = x, s = x; for (int i = 1; i < 10; i++) { t *= -x * x / ((2 * i) * (2 * i + 1)); s += t; } return s; }
constexpr double cx_cos(double x) { double t = 1, s = 1; for (int i = 1; i < 10; i++) { t *= -x * x / ((2 * i - 1) * (2 * i)); s += t; } return s; }
constexpr double cos2pi(int m, int N) {
  m %= N; if (m < 0) m += N;
  if (2 * m > N) m = N - m;
  if (4 * m > N) return -cos2pi(N / 2 - m, N);
  if (8 * m > N) return cx_sin(2 * kPi * (N / 4 - m) / N);
  return cx_cos(2 * kPi * m / N);
}
constexpr double sin2pi(int m, int N) { return cos2pi(m + 3 * (N / 4), N); }
constexpr u16 to_bf16(double d) { float f = (float)d; u32 u = __builtin_bit_cast(u32, f); u += 0x7fffu + ((u >> 16) & 1); return (u16)(u >> 16); }

struct T2Rows { u16 v[16][640]; };
struct T2All { T2Rows c[40]; };
constexpr int crow_c(int r, int h) { return (r & 3) + 8 * (r >> 2) + 4 * h; }
constexpr int kperm_c(int ks, int h, int j) { return 16 * ks + 8 * (j >> 2) + 4 * h + (j & 3); }
struct ColTab {
  u16 a1[3][2][64][8];
  u16 a1c[3][2][64][8];
  u16 a2[2][4][3][64][8];
  float tw1[2][8][64][4];
  float tw2[2][8][64][4];
};

#if defined(__HIP_DEVICE_COMPILE__) && __HIP_DEVICE_COMPILE__
struct Base640 { u16 c[640]; u16 ns[640]; };
constexpr Base640 mk_base() { Base640 b{}; for (int i = 0; i < 640; i++) { b.c[i] = to_bf16(cos2pi(i, 640)); b.ns[i] = to_bf16(-sin2pi(i, 640)); } return b; }
constexpr Base640 kBase = mk_base();
constexpr T2Rows mk_t2(int c) {
  T2Rows r{};
  for (int i = 0; i < 16; i++) {
    int d = c * 16 + i; r.v[i][0] = kBase.c[0]; r.v[i][1] = kBase.c[(320 * d) % 640];
    int m = 0;
    for (int f = 1; f < 320; f++) { m += d; if (m >= 640) m -= 640; r.v[i][2 * f] = kBase.c[m]; r.v[i][2 * f + 1] = kBase.ns[m]; }
  }
  return r;
}
template <int C> constexpr T2Rows t2chunk = mk_t2(C);
template <int... I> constexpr T2All mk_t2all(std::integer_sequence<int, I...>) { return T2All{{t2chunk<I>...}}; }
__device__ const T2All gT2 = mk_t2all(std::make_integer_sequence<int, 40>{});

struct Trig2048 { double c[2048]; };
constexpr Trig2048 mk_trig() { Trig2048 t{}; for (int i = 0; i < 2048; i++) t.c[i] = cos2pi(i, 2048); return t; }
constexpr Trig2048 kTrig = mk_trig();
constexpr double C2K(int m) { return kTrig.c[m & 2047]; }
constexpr double S2K(int m) { return kTrig.c[(m + 1536) & 2047]; }
struct A1T { u16 a1[3][2][64][8]; u16 a1c[3][2][64][8]; };
constexpr A1T mk_a1() {
  A1T t{};
  for (int ks = 0; ks < 2; ks++) for (int l = 0; l < 64; l++) for (int j = 0; j < 8; j++) {
    int h = l >> 5, r = l & 31;
    int n1 = 16 * ks + 8 * h + j;
    double c = C2K(64 * n1 * r), s = S2K(64 * n1 * r);
    t.a1[0][ks][l][j] = to_bf16(c); t.a1[1][ks][l][j] = to_bf16(-s); t.a1[2][ks][l][j] = to_bf16(s);
    int k1 = kperm_c(ks, h, j);
    c = C2K(64 * k1 * r); s = S2K(64 * k1 * r);
    t.a1c[0][ks][l][j] = to_bf16(c); t.a1c[1][ks][l][j] = to_bf16(s); t.a1c[2][ks][l][j] = to_bf16(-s);
  }
  return t;
}
struct A2T { u16 v[4][3][64][8]; };
constexpr A2T mk_a2(int u) {
  A2T t{};
  for (int kk = 0; kk < 4; kk++) for (int l = 0; l < 64; l++) for (int j = 0; j < 8; j++) {
    int h = l >> 5, row = 32 * u + (l & 31), col = 32 * (kk >> 1) + kperm_c(kk & 1, h, j);
    double c = C2K(32 * row * col), s = S2K(32 * row * col);
    t.v[kk][0][l][j] = to_bf16(c); t.v[kk][1][l][j] = to_bf16(-s); t.v[kk][2][l][j] = to_bf16(s);
  }
  return t;
}
struct TWT { float v[8][64][4]; };
constexpr TWT mk_tw(int t, int inv) {
  TWT o{};
  for (int q = 0; q < 8; q++) for (int l = 0; l < 64; l++) for (int e = 0; e < 2; e++) {
    int h = l >> 5, r = 2 * q + e;
    int n2 = inv ? 32 * t + (l & 31) : 32 * t + crow_c(r, h);
    int k1 = inv ? crow_c(r, h) : (l & 31);
    o.v[q][l][2 * e] = (float)C2K(n2 * k1); o.v[q][l][2 * e + 1] = (float)S2K(n2 * k1);
  }
  return o;
}
struct ColTabD { A1T a1; A2T a2[2]; TWT tw1[2]; TWT tw2[2]; };
constexpr A1T cA1 = mk_a1();
template <int U> constexpr A2T cA2 = mk_a2(U);
template <int T, int I> constexpr TWT cTW = mk_tw(T, I);
__device__ const ColTabD gCT = {cA1, {cA2<0>, cA2<1>}, {cTW<0, 0>, cTW<1, 0>}, {cTW<0, 1>, cTW<1, 1>}};
#else
struct ColTabD { struct { u16 a1[3][2][64][8]; u16 a1c[3][2][64][8]; } a1; struct { u16 v[4][3][64][8]; } a2[2]; struct { float v[8][64][4]; } tw1[2], tw2[2]; };
extern __device__ const T2All gT2;
extern __device__ const ColTabD gCT;
#endif

DEVI f16v mfma32(u4v a, u4v b, f16v c) { return __builtin_amdgcn_mfma_f32_32x32x16_bf16(__builtin_bit_cast(bf8, a), __builtin_bit_cast(bf8, b), c, 0, 0, 0); }
DEVI f16v mfma32k8(u2v a, u2v b, f16v c) { return __builtin_amdgcn_mfma_f32_32x32x8bf16_1k(__builtin_bit_cast(s4, a), __builtin_bit_cast(s4, b), c, 0, 0, 0); }
DEVI f4v mfma16(u4v a, u4v b, f4v c) { return __builtin_amdgcn_mfma_f32_16x16x32_bf16(__builtin_bit_cast(bf8, a), __builtin_bit_cast(bf8, b), c, 0, 0, 0); }
DEVI u32 pk2(float a, float b) { f2v v = {a, b}; return __builtin_bit_cast(u32, __builtin_convertvector(v, bf2)); }
DEVI float bflo(u32 w) { return __builtin_bit_cast(float, w << 16); }
DEVI float bfhi(u32 w) { return __builtin_bit_cast(float, w & 0xffff0000u); }
DEVI u4v lds128(const char* p) { return *(const LAS u4v*)(const LAS char*)p; }
DEVI u2v lds64(const char* p) { return *(const LAS u2v*)(const LAS char*)p; }
DEVI u2v ldstr(const char* p) { return __builtin_bit_cast(u2v, __builtin_amdgcn_ds_read_tr16_b64_v4i16((LAS s4*)(LAS char*)p)); }
DEVI u4v cat(u2v a, u2v b) { u4v r = {a.x, a.y, b.x, b.y}; return r; }
DEVI void glds16(const void* g, u32 lds) {
  u32 sv;
  asm volatile("s_mov_b32 %0, m0\n\ts_mov_b32 m0, %2\n\ts_nop 0\n\tglobal_load_lds_dwordx4 %1, off\n\ts_mov_b32 m0, %0" : "=&s"(sv) : "v"(g), "s"(lds) : "memory");
}
template <int N> DEVI void wait_vm() { asm volatile("s_waitcnt vmcnt(%0)" ::"i"(N) : "memory"); }
template <int N> DEVI void wait_vm_bar() { asm volatile("s_waitcnt vmcnt(%0) lgkmcnt(0)\n\ts_barrier" ::"i"(N) : "memory"); }
DEVI u32 uni(u32 v) { return __builtin_amdgcn_readfirstlane(v); }
DEVI u4v swap_pack(u32 a0, u32 a1, u32 b0, u32 b1) {
  auto r0 = __builtin_amdgcn_permlane32_swap(a0, b0, false, false);
  auto r1 = __builtin_amdgcn_permlane32_swap(a1, b1, false, false);
  u4v o = {r0[0], r1[0], r0[1], r1[1]};
  return o;
}
DEVI int swap23(int k) { return (k & ~12) | ((k & 4) << 1) | ((k & 8) >> 1); }

struct Params {
  const u16 *x, *wq, *wk, *wv, *wo, *wr, *wi, *gw;
  u16* out;
  char* ws;
};

constexpr int K1_LDS = 153664;
template <int B0, int B1, int B2>
DEVI void k1_tile(const Params& P, char* smem, int mt, int nt) {
  constexpr bool SP = (B1 == 2);
  constexpr int WROW = SP ? 64 : 192;
  constexpr int WB = 64 * WROW, SLOT = 16384 + WB, NWI = WB / 4096, NL = 4 + NWI;
  constexpr int RAWK = 5 * SLOT, RAWV = RAWK + 25600;
  const int tid = threadIdx.x, lane = tid & 63, w = uni(tid >> 6), r = lane & 31, h = lane >> 5;
  const u32 lds0 = (u32)(uintptr_t)(LAS char*)smem;
  const u16* T2 = (const u16*)&gT2;
  const u16* xs[4];
#pragma unroll
  for (int i = 0; i < 4; i++) {
    int row = (w * 4 + i) * 8 + (lane >> 3), ch = (lane & 7) ^ ((row >> 1) & 7), p = mt * 128 + row;
    xs[i] = P.x + (64 * (p & 31) + (p >> 5)) * 640 + ch * 8;
  }
  const u16* wsrc[NWI];
#pragma unroll
  for (int i = 0; i < NWI; i++) {
    int o = (w * NWI + i) * 1024 + lane * 16, row = o / WROW, c = (o % WROW) >> 4, bl = 3 * nt + (c >> 2);
    wsrc[i] = (bl < 20 ? P.wq + bl * 32 : T2 + (bl - 20) * 32) + row * 640 + (c & 3) * 8;
  }
  if constexpr (SP) {
    for (int i = w; i < 50; i += 4) {
      const u16* src = (i < 25 ? P.wk + i * 512 : P.wv + (i - 25) * 512) + lane * 8;
      glds16(src, uni(lds0 + RAWK + i * 1024));
    }
  }
  auto issue = [&](int t) {
    const u32 sl = lds0 + (t % 5) * SLOT;
#pragma unroll
    for (int i = 0; i < 4; i++) glds16(xs[i] + t * 64, uni(sl + (w * 4 + i) * 1024));
#pragma unroll
    for (int i = 0; i < NWI; i++) glds16(wsrc[i] + t * 64 * 640, uni(sl + 16384 + (w * NWI + i) * 1024));
  };
#pragma unroll
  for (int t = 0; t < 4; t++) issue(t);
  f16v acc[3] = {};
  const int xrow = 32 * w + r;
  const int xoff = xrow * 128, xsw = (xrow >> 1) & 7;
  const int trq = (lane & 15) >> 2, trc = ((lane >> 4) & 1) * 32 + (lane & 3) * 8;
  const int woff = 16384 + (8 * h + trq) * WROW + trc;
  const int roff = (8 * h + trq) * 40 + trc;
#pragma unroll
  for (int t = 0; t < 10; t++) {
    if (t <= 6) wait_vm_bar<3 * NL>(); else if (t == 7) wait_vm_bar<2 * NL>(); else if (t == 8) wait_vm_bar<NL>(); else wait_vm_bar<0>();
    if (t + 4 < 10) issue(t + 4);
    const char* sl = smem + (t % 5) * SLOT;
#pragma unroll
    for (int ks = 0; ks < 4; ks++) {
      u4v xf = lds128(sl + xoff + (((2 * ks + h) ^ xsw) << 4));
#pragma unroll
      for (int j = 0; j < 3; j++) {
        const int ty = j == 0 ? B0 : j == 1 ? B1 : B2;
        u4v wf;
        if (ty <= 1) {
          const char* a = sl + woff + ks * 16 * WROW + j * 64;
          wf = cat(ldstr(a), ldstr(a + 4 * WROW));
        } else {
          const char* a = smem + (ty == 2 ? RAWK : RAWV) + roff + (t * 64 + ks * 16) * 40;
          wf = cat(ldstr(a), ldstr(a + 160));
        }
        acc[j] = (ty == 0 || ty == 2) ? mfma32(wf, xf, acc[j]) : mfma32(xf, wf, acc[j]);
      }
    }
  }
  const int p0 = mt * 128 + 32 * w;
#pragma unroll
  for (int j = 0; j < 3; j++) {
    const int ty = j == 0 ? B0 : j == 1 ? B1 : B2;
    const int bl = 3 * nt + j;
    f16v a = acc[j];
    if (ty == 0) {
      const float qs = 0.22360679774997896f * 1.4426950408889634f;
      u16* dst = (u16*)(P.ws + WS_Q) + (p0 + r) * 640 + bl * 32 + 8 * h;
#pragma unroll
      for (int g = 0; g < 4; g += 2)
        *(u4v*)(dst + 8 * g) = swap_pack(pk2(a[4 * g] * qs, a[4 * g + 1] * qs), pk2(a[4 * g + 2] * qs, a[4 * g + 3] * qs),
                                         pk2(a[4 * g + 4] * qs, a[4 * g + 5] * qs), pk2(a[4 * g + 6] * qs, a[4 * g + 7] * qs));
    } else if (ty == 1) {
      u16* dst = (u16*)(P.ws + WS_X1) + ((bl - 20) * 32 + r) * 2048 + p0 + 8 * h;
#pragma unroll
      for (int g = 0; g < 4; g += 2)
        *(u4v*)(dst + 8 * g) = swap_pack(pk2(a[4 * g], a[4 * g + 1]), pk2(a[4 * g + 2], a[4 * g + 3]),
                                         pk2(a[4 * g + 4], a[4 * g + 5]), pk2(a[4 * g + 6], a[4 * g + 7]));
    } else if (ty == 2) {
      char* tile = P.ws + WS_KV + (p0 >> 5) * KVT;
      *(u4v*)(tile + lane * 16) = swap_pack(pk2(a[0], a[1]), pk2(a[2], a[3]), pk2(a[4], a[5]), pk2(a[6], a[7]));
      u2v k1 = {pk2(a[8], a[9]), pk2(a[10], a[11])};
      if (h == 0) *(u2v*)(tile + 1024 + r * 8) = k1;
      float n2 = 0.f;
#pragma unroll
      for (int i = 0; i < 8; i++) n2 += a[i] * a[i];
      if (h == 0) {
#pragma unroll
        for (int i = 8; i < 12; i++) n2 += a[i] * a[i];
      }
      n2 += __shfl_xor(n2, 32);
      float bad = (n2 == n2) ? 0.f : 1.f;
#pragma unroll
      for (int o = 16; o > 0; o >>= 1) { n2 = fmaxf(n2, __shfl_xor(n2, o)); bad = fmaxf(bad, __shfl_xor(bad, o)); }
      if (lane == 0) ((float*)(P.ws + WS_KM))[p0 >> 5] = bad > 0.f ? __builtin_inff() : n2;
    } else {
      char* tile = P.ws + WS_KV + (p0 >> 5) * KVT + 1280;
#pragma unroll
      for (int s = 0; s < 2; s++) {
        u4v v = {pk2(a[8 * s], a[8 * s + 1]), pk2(a[8 * s + 2], a[8 * s + 3]), pk2(a[8 * s + 4], a[8 * s + 5]), pk2(a[8 * s + 6], a[8 * s + 7])};
        if (r == 20) v = u4v{0x3f803f80u, 0x3f803f80u, 0x3f803f80u, 0x3f803f80u};
        if (r <= 20) *(u4v*)(tile + (2 * s + h) * 352 + r * 16) = v;
      }
    }
  }
}

DEVI void k1_transpose(const Params& P, char* smem, int uu) {
  const int tid = threadIdx.x, m = uu >> 1, half = uu & 1, f0 = 160 * half;
  u32* img = (u32*)smem;
#pragma unroll 4
  for (int i = tid; i < 5120; i += 256) {
    int arr = i >= 2560, j = arr ? i - 2560 : i, row = j / 20, c = j % 20;
    const u16* src = (arr ? P.wi : P.wr) + (128 * m + row) * 321 + f0 + c * 8;
    u4v v;
    __builtin_memcpy(&v, src, 16);
    u32* d = img + arr * (128 * 81) + row * 81 + c * 4;
    d[0] = v.x; d[1] = v.y; d[2] = v.z; d[3] = v.w;
  }
  if (half) {
    int arr = tid >> 7, row = tid & 127;
    u16 v = (arr ? P.wi : P.wr)[(128 * m + row) * 321 + 320];
    ((u16*)(img + arr * (128 * 81) + row * 81))[160] = v;
  }
  __syncthreads();
  const int lane = tid & 63, w = tid >> 6, k1 = lane & 31;
  const int nf = half ? 161 : 160;
  const u16* re = (const u16*)img;
  const u16* im = (const u16*)(img + 128 * 81);
  const int u = m >> 3, q = (m >> 1) & 3, hh = m & 1;
  for (int fl = 2 * w + (lane >> 5); fl < nf; fl += 8) {
    u4v o;
#pragma unroll
    for (int e = 0; e < 4; e++) {
      int idx = (32 * e + k1) * 162 + fl;
      o[e] = (u32)re[idx] | ((u32)im[idx] << 16);
    }
    *(u4v*)(P.ws + WS_WT + (size_t)(f0 + fl) * 8192 + ((u * 4 + q) * 64 + hh * 32 + k1) * 16) = o;
  }
}

__global__ void __launch_bounds__(256, 1) k1_proj(Params P) {
  extern __shared__ __attribute__((aligned(16))) char smem[];
  const int xcd = blockIdx.x & 7, idx = blockIdx.x >> 3;
  if (idx >= 28) { k1_transpose(P, smem, xcd * 4 + idx - 28); return; }
  const int mt = (xcd >> 1) * 4 + idx / 7, nt = (xcd & 1) * 7 + idx % 7;
  if (nt < 6) k1_tile<0, 0, 0>(P, smem, mt, nt);
  else if (nt == 6) k1_tile<0, 0, 1>(P, smem, mt, nt);
  else if (nt < 13) k1_tile<1, 1, 1>(P, smem, mt, nt);
  else k1_tile<1, 2, 3>(P, smem, mt, nt);
}

constexpr int CHUNK = 8 * KVT;
constexpr int K2_LDS = 3 * CHUNK;
constexpr float kBound2 = 48.f * 48.f;

struct KF { u4v a; u2v b; };
struct VF { u4v s0, s1; };

template <bool FAST>
DEVI void attn_loop(const char* kvg, char* smem, u32 lds0, int w, int lane, u4v qa, u2v qb, f16v& o) {
  const int r = lane & 31, h = lane >> 5;
  const int koff = lane * 16, k1off = 1024 + r * 8;
  const int vrow = r <= 20 ? r : (r < 28 ? 20 : 16);
  const int voff = 1280 + h * 352 + vrow * 16;
  auto dma = [&](int c) {
    const char* src = kvg + c * CHUNK + lane * 16;
    const u32 dst = lds0 + (c % 3) * CHUNK;
    glds16(src + w * 1024, uni(dst + w * 1024));
    glds16(src + (w + 8) * 1024, uni(dst + (w + 8) * 1024));
    if (w < 5) glds16(src + (w + 16) * 1024, uni(dst + (w + 16) * 1024));
  };
  auto tptr = [&](int t) { return smem + ((t >> 3) % 3) * CHUNK + (t & 7) * KVT; };
  auto ldk = [&](int t) { KF k; const char* p = tptr(t); k.a = lds128(p + koff); k.b = lds64(p + k1off); return k; };
  auto ldv = [&](int t) { VF v; const char* p = tptr(t); v.s0 = lds128(p + voff); v.s1 = lds128(p + voff + 704); return v; };
  auto qk = [&](const KF& k) { f16v z = {}; z = mfma32(k.a, qa, z); return mfma32k8(k.b, qb, z); };
  float mrun = -__builtin_inff();
  auto soft_pv = [&](f16v s, const VF& v) {
    if constexpr (!FAST) {
      float mx = s[0];
#pragma unroll
      for (int i = 1; i < 16; i++) mx = fmaxf(mx, s[i]);
      mx = fmaxf(mx, __shfl_xor(mx, 32));
      const float mn = fmaxf(mrun, mx), al = __builtin_amdgcn_exp2f(mrun - mn);
      mrun = mn;
#pragma unroll
      for (int i = 0; i < 16; i++) { o[i] *= al; s[i] -= mn; }
    }
    u4v p0, p1;
#pragma unroll
    for (int i = 0; i < 4; i++) p0[i] = pk2(__builtin_amdgcn_exp2f(s[2 * i]), __builtin_amdgcn_exp2f(s[2 * i + 1]));
    o = mfma32(v.s0, p0, o);
#pragma unroll
    for (int i = 0; i < 4; i++) p1[i] = pk2(__builtin_amdgcn_exp2f(s[8 + 2 * i]), __builtin_amdgcn_exp2f(s[8 + 2 * i + 1]));
    o = mfma32(v.s1, p1, o);
  };
  wait_vm_bar<0>();
  dma(1);
  KF kA = ldk(0), kB = ldk(1);
  VF vA = ldv(0), vB;
  f16v sA = qk(kA), sB;
  for (int c = 0; c < 8; c++) {
#pragma unroll
    for (int i = 0; i < 8; i += 2) {
      const int t = 8 * c + i;
      sB = qk(kB);
      if (i == 6 && c < 7) { wait_vm_bar<0>(); if (c < 6) dma(c + 2); }
      const int t2 = t + 2 < 64 ? t + 2 : 63;
      kA = ldk(t2);
      vB = ldv(t + 1);
      soft_pv(sA, vA);
      sA = qk(kA);
      const int t3 = t + 3 < 64 ? t + 3 : 63;
      kB = ldk(t3);
      vA = ldv(t2);
      soft_pv(sB, vB);
    }
  }
}

DEVI void attn_wg(const Params& P, char* smem, int b) {
  const int tid = threadIdx.x, lane = tid & 63, w = uni(tid >> 6), r = lane & 31, h = lane >> 5;
  const u32 lds0 = (u32)(uintptr_t)(LAS char*)smem;
  const char* kvg = P.ws + WS_KV;
  {
    const char* src = kvg + lane * 16;
    glds16(src + w * 1024, uni(lds0 + w * 1024));
    glds16(src + (w + 8) * 1024, uni(lds0 + (w + 8) * 1024));
    if (w < 5) glds16(src + (w + 16) * 1024, uni(lds0 + (w + 16) * 1024));
  }
  const int p = 8 * b + w;
  const u16* qrow = (const u16*)(P.ws + WS_Q) + p * 640 + 20 * r;
  u4v qa; u2v qb;
  __builtin_memcpy(&qa, qrow + 8 * h, 16);
  __builtin_memcpy(&qb, qrow + 16, 8);
  if (h) qb = u2v{0u, 0u};
  float km = ((const float*)(P.ws + WS_KM))[lane];
  float g;
  {
    const int s = 64 * (p & 31) + (p >> 5);
    const u32* xr = (const u32*)(P.x + s * 640) + lane * 5;
    const u32* gr = (const u32*)P.gw + lane * 5;
    float z = 0.f;
#pragma unroll
    for (int i = 0; i < 5; i++) { u32 a = xr[i], c = gr[i]; z += bflo(a) * bflo(c) + bfhi(a) * bfhi(c); }
#pragma unroll
    for (int o = 32; o > 0; o >>= 1) z += __shfl_xor(z, o);
    g = 1.f / (1.f + __expf(-z));
    if (lane == 0) ((float*)(P.ws + WS_G))[p] = g;
  }
#pragma unroll
  for (int o = 32; o > 0; o >>= 1) km = fmaxf(km, __shfl_xor(km, o));
  float qn = 0.f;
#pragma unroll
  for (int i = 0; i < 4; i++) qn += bflo(qa[i]) * bflo(qa[i]) + bfhi(qa[i]) * bfhi(qa[i]);
#pragma unroll
  for (int i = 0; i < 2; i++) qn += bflo(qb[i]) * bflo(qb[i]) + bfhi(qb[i]) * bfhi(qb[i]);
  qn += __shfl_xor(qn, 32);
  const bool fast = __all(qn * km * 1.05f <= kBound2);
  f16v o = {};
  if (fast) attn_loop<true>(kvg, smem, lds0, w, lane, qa, qb, o);
  else attn_loop<false>(kvg, smem, lds0, w, lane, qa, qb, o);
  const float l = __shfl(o[8], r + 32);
  const float sc = g / l;
  u16* dst = (u16*)(P.ws + WS_O) + p * 640 + 20 * r + 4 * h;
  u2v v0 = {pk2(o[0] * sc, o[1] * sc), pk2(o[2] * sc, o[3] * sc)};
  u2v v1 = {pk2(o[4] * sc, o[5] * sc), pk2(o[6] * sc, o[7] * sc)};
  u2v v2 = {pk2(o[8] * sc, o[9] * sc), pk2(o[10] * sc, o[11] * sc)};
  *(u2v*)dst = v0;
  *(u2v*)(dst + 8) = v1;
  if (h == 0) *(u2v*)(dst + 16) = v2;
}

DEVI u4v pk8(const f16v& a, int s) {
  u4v v = {pk2(a[8 * s], a[8 * s + 1]), pk2(a[8 * s + 2], a[8 * s + 3]), pk2(a[8 * s + 4], a[8 * s + 5]), pk2(a[8 * s + 6], a[8 * s + 7])};
  return v;
}
DEVI void column_job(const Params& P, char* xch, int f, int t, int lane) {
  const int r = lane & 31, h = lane >> 5;
  const int cr = f == 0 ? 0 : f == 320 ? 1 : 2 * f;
  const bool real_in = (f == 0 || f == 320);
  const u16* zr = (const u16*)(P.ws + WS_X1) + cr * 2048 + (32 * t + r) * 32 + 8 * h;
  const u16* zi = zr + 2048;
  u4v Zr[2], Zi[2];
#pragma unroll
  for (int ks = 0; ks < 2; ks++) {
    Zr[ks] = *(const u4v*)(zr + 16 * ks);
    Zi[ks] = real_in ? u4v{0u, 0u, 0u, 0u} : *(const u4v*)(zi + 16 * ks);
  }
  const u4v* a1 = (const u4v*)gCT.a1.a1 + lane;
  const u4v* a1c = (const u4v*)gCT.a1.a1c + lane;
  const u4v* a2 = (const u4v*)gCT.a2[t].v + lane;
  const f4v* tw1 = (const f4v*)gCT.tw1[t].v + lane;
  const f4v* tw2 = (const f4v*)gCT.tw2[t].v + lane;
  const u4v* wt = (const u4v*)(P.ws + WS_WT + (size_t)f * 8192) + t * 256 + lane;
  f16v re = {}, im = {};
#pragma unroll
  for (int ks = 0; ks < 2; ks++) {
    u4v fr = a1[(0 * 2 + ks) * 64], fi = a1[(1 * 2 + ks) * 64], nfi = a1[(2 * 2 + ks) * 64];
    re = mfma32(Zr[ks], fr, re); re = mfma32(Zi[ks], nfi, re);
    im = mfma32(Zr[ks], fi, im); im = mfma32(Zi[ks], fr, im);
  }
#pragma unroll
  for (int q = 0; q < 8; q++) {
    f4v c = tw1[q * 64];
    float a0 = re[2 * q], b0 = im[2 * q], a1_ = re[2 * q + 1], b1 = im[2 * q + 1];
    re[2 * q] = a0 * c[0] + b0 * c[1]; im[2 * q] = b0 * c[0] - a0 * c[1];
    re[2 * q + 1] = a1_ * c[2] + b1 * c[3]; im[2 * q + 1] = b1 * c[2] - a1_ * c[3];
  }
  u4v yr[4], yi[4];
  auto exchange = [&](int phase) {
    u4v m0 = pk8(re, 0), m1 = pk8(re, 1), m2 = pk8(im, 0), m3 = pk8(im, 1);
    LAS u4v* mine = (LAS u4v*)(LAS char*)(xch + phase * 8192 + t * 4096) + lane;
    mine[0] = m0; mine[64] = m1; mine[128] = m2; mine[192] = m3;
    __syncthreads();
    const LAS u4v* oth = (const LAS u4v*)(LAS char*)(xch + phase * 8192 + (t ^ 1) * 4096) + lane;
    u4v o0 = oth[0], o1 = oth[64], o2 = oth[128], o3 = oth[192];
    yr[0] = t ? o0 : m0; yr[1] = t ? o1 : m1; yr[2] = t ? m0 : o0; yr[3] = t ? m1 : o1;
    yi[0] = t ? o2 : m2; yi[1] = t ? o3 : m3; yi[2] = t ? m2 : o2; yi[3] = t ? m3 : o3;
  };
  exchange(0);
  re = f16v{}; im = f16v{};
#pragma unroll
  for (int kk = 0; kk < 4; kk++) {
    u4v ar = a2[(kk * 3 + 0) * 64], ai = a2[(kk * 3 + 1) * 64], nai = a2[(kk * 3 + 2) * 64];
    re = mfma32(ar, yr[kk], re); re = mfma32(nai, yi[kk], re);
    im = mfma32(ai, yr[kk], im); im = mfma32(ar, yi[kk], im);
  }
  const float wsc = real_in ? 0.5f : 1.f;
#pragma unroll
  for (int q = 0; q < 4; q++) {
    u4v wv = wt[q * 64];
#pragma unroll
    for (int e = 0; e < 4; e++) {
      float wr_ = bflo(wv[e]) * wsc, wi_ = bfhi(wv[e]) * wsc, a = re[4 * q + e], b = im[4 * q + e];
      re[4 * q + e] = a * wr_ - b * wi_; im[4 * q + e] = a * wi_ + b * wr_;
    }
  }
  exchange(1);
  re = f16v{}; im = f16v{};
#pragma unroll
  for (int kk = 0; kk < 4; kk++) {
    u4v br = a2[(kk * 3 + 0) * 64], nbi = a2[(kk * 3 + 1) * 64], bi = a2[(kk * 3 + 2) * 64];
    re = mfma32(yr[kk], br, re); re = mfma32(yi[kk], nbi, re);
    im = mfma32(yr[kk], bi, im); im = mfma32(yi[kk], br, im);
  }
#pragma unroll
  for (int q = 0; q < 8; q++) {
    f4v c = tw2[q * 64];
    float a0 = re[2 * q], b0 = im[2 * q], a1_ = re[2 * q + 1], b1 = im[2 * q + 1];
    re[2 * q] = a0 * c[0] - b0 * c[1]; im[2 * q] = b0 * c[0] + a0 * c[1];
    re[2 * q + 1] = a1_ * c[2] - b1 * c[3]; im[2 * q + 1] = b1 * c[2] + a1_ * c[3];
  }
  u4v gr0 = pk8(re, 0), gr1 = pk8(re, 1), gi0 = pk8(im, 0), gi1 = pk8(im, 1);
  re = f16v{}; im = f16v{};
#pragma unroll
  for (int ks = 0; ks < 2; ks++) {
    u4v c = a1c[(0 * 2 + ks) * 64], ci = a1c[(1 * 2 + ks) * 64], nci = a1c[(2 * 2 + ks) * 64];
    u4v gr = ks ? gr1 : gr0, gi = ks ? gi1 : gi0;
    re = mfma32(c, gr, re); re = mfma32(nci, gi, re);
    im = mfma32(ci, gr, im); im = mfma32(c, gi, im);
  }
  u16* dst = (u16*)(P.ws + WS_X2) + cr * 2048 + (32 * t + r) * 32 + 8 * h;
#pragma unroll
  for (int g = 0; g < 4; g += 2) {
    *(u4v*)(dst + 8 * g) = swap_pack(pk2(re[4 * g], re[4 * g + 1]), pk2(re[4 * g + 2], re[4 * g + 3]),
                                     pk2(re[4 * g + 4], re[4 * g + 5]), pk2(re[4 * g + 6], re[4 * g + 7]));
    u4v vi = swap_pack(pk2(im[4 * g], im[4 * g + 1]), pk2(im[4 * g + 2], im[4 * g + 3]),
                       pk2(im[4 * g + 4], im[4 * g + 5]), pk2(im[4 * g + 6], im[4 * g + 7]));
    if (!real_in) *(u4v*)(dst + 2048 + 8 * g) = vi;
  }
}

__global__ void __launch_bounds__(512, 4) k2_mix(Params P) {
  extern __shared__ __attribute__((aligned(16))) char smem[];
  if (blockIdx.x < 256) { attn_wg(P, smem, blockIdx.x); return; }
  const int j = blockIdx.x - 256, w = uni(threadIdx.x >> 6), lane = threadIdx.x & 63;
  if (w >= 4) return;
  const int f = (w >> 1) ? 256 + j : j;
  if (f > 320) return;
  column_job(P, smem + (w >> 1) * 16384, f, w & 1, lane);
}

constexpr int K3_SLOT = 36864, K3_LDS = 4 * K3_SLOT;
__global__ void __launch_bounds__(512, 2) k3_out(Params P) {
  extern __shared__ __attribute__((aligned(16))) char smem[];
  const int xcd = blockIdx.x & 7, idx = blockIdx.x >> 3;
  const int mt = (xcd >> 1) * 8 + (idx >> 2), nt = (xcd & 1) * 4 + (idx & 3);
  const int p0 = mt * 64, n0 = nt * 80;
  const int tid = threadIdx.x, lane = tid & 63, w = uni(tid >> 6);
  const u32 lds0 = (u32)(uintptr_t)(LAS char*)smem;
  const u16* T2 = (const u16*)&gT2;
  const u16* X2 = (const u16*)(P.ws + WS_X2);
  const u16* O = (const u16*)(P.ws + WS_O);
  const u16 *srcb[5], *srca[5];
#pragma unroll
  for (int i = 0; i < 5; i++) {
    const int pc = w + 8 * i, o = pc * 1024 + lane * 16;
    if (i < 2) {
      int rho = o >> 7, s16 = (o >> 4) & 7, mb = (s16 >> 1) ^ ((rho >> 1) & 3);
      srcb[i] = X2 + swap23(rho) * 2048 + p0 + mb * 16 + (s16 & 1) * 8;
      int img = o >> 13, row = (o >> 7) & 63, ch = ((o >> 4) & 7) ^ ((row >> 1) & 7);
      srca[i] = O + (p0 + row) * 640 + img * 64 + ch * 8;
    } else {
      const int ow = (i < 4 || w < 4) ? o - 16384 : 0;
      int img = ow / 10240, rem = ow % 10240, row = rem >> 7, ch = ((rem >> 4) & 7) ^ ((row >> 1) & 7);
      srcb[i] = T2 + (n0 + row) * 640 + img * 64 + ch * 8;
      int rho = ow / 160, c = (ow % 160) >> 4;
      srca[i] = P.wo + swap23(rho) * 640 + n0 + c * 8;
    }
  }
  auto issue = [&](int st) {
    const u32 sl = lds0 + (st & 3) * K3_SLOT + w * 1024;
    if (st < 5) {
      glds16(srcb[0] + st * 128 * 2048, uni(sl)); glds16(srcb[1] + st * 128 * 2048, uni(sl + 8192));
      glds16(srcb[2] + st * 128, uni(sl + 16384)); glds16(srcb[3] + st * 128, uni(sl + 24576));
      if (w < 4) glds16(srcb[4] + st * 128, uni(sl + 32768));
    } else {
      const int k = st - 5;
      glds16(srca[0] + k * 128, uni(sl)); glds16(srca[1] + k * 128, uni(sl + 8192));
      glds16(srca[2] + k * 128 * 640, uni(sl + 16384)); glds16(srca[3] + k * 128 * 640, uni(sl + 24576));
      if (w < 4) glds16(srca[4] + k * 128 * 640, uni(sl + 32768));
    }
  };
  issue(0); issue(1); issue(2);
  const int mb = w & 3, kh = w >> 2, l15 = lane & 15, g = lane >> 4;
  const float gate = ((const float*)(P.ws + WS_G))[p0 + 16 * mb + l15];
  f4v acca[5] = {}, accb[5] = {};
  const int trq = l15 >> 2, trp = (lane & 3) * 8;
  const int rho0 = kh * 64 + 16 * (g >> 1) + 4 * (g & 1) + trq;
#pragma unroll
  for (int st = 0; st < 10; st++) {
    if (w < 4) { if (st <= 7) wait_vm_bar<10>(); else if (st == 8) wait_vm_bar<5>(); else wait_vm_bar<0>(); }
    else { if (st <= 7) wait_vm_bar<8>(); else if (st == 8) wait_vm_bar<4>(); else wait_vm_bar<0>(); }
    if (st + 3 < 10) issue(st + 3);
    const char* sl = smem + (st & 3) * K3_SLOT;
#pragma unroll
    for (int ks = 0; ks < 2; ks++) {
      if (st < 5) {
        const int r0 = rho0 + 32 * ks, r1 = r0 + 8;
        u4v act = cat(ldstr(sl + r0 * 128 + ((mb ^ ((r0 >> 1) & 3)) << 5) + trp), ldstr(sl + r1 * 128 + ((mb ^ ((r1 >> 1) & 3)) << 5) + trp));
#pragma unroll
        for (int nb = 0; nb < 5; nb++) {
          const int row = 16 * nb + l15;
          u4v wf = lds128(sl + 16384 + kh * 10240 + row * 128 + (((4 * ks + g) ^ ((row >> 1) & 7)) << 4));
          accb[nb] = mfma16(wf, act, accb[nb]);
        }
      } else {
        const int row = 16 * mb + l15;
        u4v act = lds128(sl + kh * 8192 + row * 128 + (((4 * ks + g) ^ ((row >> 1) & 7)) << 4));
        const char* wb = sl + 16384 + (rho0 + 32 * ks) * 160 + trp;
#pragma unroll
        for (int nb = 0; nb < 5; nb++) {
          u4v wf = cat(ldstr(wb + nb * 32), ldstr(wb + 8 * 160 + nb * 32));
          acca[nb] = mfma16(wf, act, acca[nb]);
        }
      }
    }
  }
  const float sb = (1.f - gate) * (2.f / (float)(S_ * D_));
  f4v v[5];
#pragma unroll
  for (int nb = 0; nb < 5; nb++) v[nb] = acca[nb] + accb[nb] * sb;
  __syncthreads();
  f4v* red = (f4v*)smem + mb * 320 + lane;
  if (kh == 1) {
#pragma unroll
    for (int nb = 0; nb < 5; nb++) red[nb * 64] = v[nb];
  }
  __syncthreads();
  if (kh == 0) {
    const int p = p0 + 16 * mb + l15, s = 64 * (p & 31) + (p >> 5);
    u16* dst = P.out + s * 640 + n0 + 4 * g;
#pragma unroll
    for (int nb = 0; nb < 5; nb++) {
      f4v t = v[nb] + red[nb * 64];
      u2v o = {pk2(t[0], t[1]), pk2(t[2], t[3])};
      *(u2v*)(dst + 16 * nb) = o;
    }
  }
}

extern "C" void kernel_launch(void* const* d_in, const int* in_sizes, int n_in, void* d_out, int out_size, void* d_ws,
                              size_t ws_size, hipStream_t stream) {
  Params P;
  P.x = (const u16*)d_in[0];
  P.wq = (const u16*)d_in[1];
  P.wk = (const u16*)d_in[2];
  P.wv = (const u16*)d_in[3];
  P.wo = (const u16*)d_in[4];
  P.wr = (const u16*)d_in[6];
  P.wi = (const u16*)d_in[7];
  P.gw = (const u16*)d_in[8];
  P.out = (u16*)d_out;
  P.ws = (char*)d_ws;
  hipFuncSetAttribute((const void*)k1_proj, hipFuncAttributeMaxDynamicSharedMemorySize, K1_LDS);
  hipFuncSetAttribute((const void*)k2_mix, hipFuncAttributeMaxDynamicSharedMemorySize, K2_LDS);
  hipFuncSetAttribute((const void*)k3_out, hipFuncAttributeMaxDynamicSharedMemorySize, K3_LDS);
  k1_proj<<<256, 256, K1_LDS, stream>>>(P);
  k2_mix<<<512, 512, K2_LDS, stream>>>(P);
  k3_out<<<256, 512, K3_LDS, stream>>>(P);
}
```

```cpp
#include <hip/hip_runtime.h>
#include <stdint.h>
#include <utility>

typedef unsigned short u16;
typedef unsigned int u32;
typedef __attribute__((ext_vector_type(8))) __bf16 bf8;
typedef __attribute__((ext_vector_type(4))) short s4;
typedef __attribute__((ext_vector_type(16))) float f16v;
typedef __attribute__((ext_vector_type(4))) float f4v;
typedef __attribute__((ext_vector_type(2))) float f2v;
typedef __attribute__((ext_vector_type(2))) __bf16 bf2;
typedef __attribute__((ext_vector_type(4))) u32 u4v;
typedef __attribute__((ext_vector_type(2))) u32 u2v;
#define LAS __attribute__((address_space(3)))
#define DEVI __device__ __forceinline__

constexpr int S_ = 2048, D_ = 640;

constexpr size_t WS_Q = 0x000000;
constexpr size_t WS_X1 = 0x280000;
constexpr size_t WS_X2 = 0x500000;
constexpr size_t WS_O = 0x780000;
constexpr size_t WS_WT = 0xA00000;
constexpr size_t WS_KV = 0xD00000;
constexpr size_t WS_KM = 0xD40000;
constexpr size_t WS_G = 0xD41000;
constexpr int KVT = 2688;

constexpr double kPi = 3.14159265358979323846;
constexpr double cx_sin(double x) { double t = x, s = x; for (int i = 1; i < 10; i++) { t *= -x * x / ((2 * i) * (2 * i + 1)); s += t; } return s; }
constexpr double cx_cos(double x) { double t = 1, s = 1; for (int i = 1; i < 10; i++) { t *= -x * x / ((2 * i - 1) * (2 * i)); s += t; } return s; }
constexpr double cos2pi(int m, int N) {
  m %= N; if (m < 0) m += N;
  if (2 * m > N) m = N - m;
  if (4 * m > N) return -cos2pi(N / 2 - m, N);
  if (8 * m > N) return cx_sin(2 * kPi * (N / 4 - m) / N);
  return cx_cos(2 * kPi * m / N);
}
constexpr double sin2pi(int m, int N) { return cos2pi(m + 3 * (N / 4), N); }
constexpr u16 to_bf16(double d) { float f = (float)d; u32 u = __builtin_bit_cast(u32, f); u += 0x7fffu + ((u >> 16) & 1); return (u16)(u >> 16); }

struct T2Rows { u16 v[16][640]; };
struct T2All { T2Rows c[40]; };
constexpr int crow_c(int r, int h) { return (r & 3) + 8 * (r >> 2) + 4 * h; }
constexpr int kperm_c(int ks, int h, int j) { return 16 * ks + 8 * (j >> 2) + 4 * h + (j & 3); }
struct ColTab {
  u16 a1[3][2][64][8];
  u16 a1c[3][2][64][8];
  u16 a2[2][4][3][64][8];
  float tw1[2][8][64][4];
  float tw2[2][8][64][4];
};

#if defined(__HIP_DEVICE_COMPILE__) && __HIP_DEVICE_COMPILE__
struct Base640 { u16 c[640]; u16 ns[640]; };
constexpr Base640 mk_base() { Base640 b{}; for (int i = 0; i < 640; i++) { b.c[i] = to_bf16(cos2pi(i, 640)); b.ns[i] = to_bf16(-sin2pi(i, 640)); } return b; }
constexpr Base640 kBase = mk_base();
constexpr T2Rows mk_t2(int c) {
  T2Rows r{};
  for (int i = 0; i < 16; i++) {
    int d = c * 16 + i; r.v[i][0] = kBase.c[0]; r.v[i][1] = kBase.c[(320 * d) % 640];
    int m = 0;
    for (int f = 1; f < 320; f++) { m += d; if (m >= 640) m -= 640; r.v[i][2 * f] = kBase.c[m]; r.v[i][2 * f + 1] = kBase.ns[m]; }
  }
  return r;
}
template <int C> constexpr T2Rows t2chunk = mk_t2(C);
template <int... I> constexpr T2All mk_t2all(std::integer_sequence<int, I...>) { return T2All{{t2chunk<I>...}}; }
__device__ const T2All gT2 = mk_t2all(std::make_integer_sequence<int, 40>{});

struct Trig2048 { double c[2048]; };
constexpr Trig2048 mk_trig() { Trig2048 t{}; for (int i = 0; i < 2048; i++) t.c[i] = cos2pi(i, 2048); return t; }
constexpr Trig2048 kTrig = mk_trig();
constexpr double C2K(int m) { return kTrig.c[m & 2047]; }
constexpr double S2K(int m) { return kTrig.c[(m + 1536) & 2047]; }
struct A1T { u16 a1[3][2][64][8]; u16 a1c[3][2][64][8]; };
constexpr A1T mk_a1() {
  A1T t{};
  for (int ks = 0; ks < 2; ks++) for (int l = 0; l < 64; l++) for (int j = 0; j < 8; j++) {
    int h = l >> 5, r = l & 31;
    int n1 = 16 * ks + 8 * h + j;
    double c = C2K(64 * n1 * r), s = S2K(64 * n1 * r);
    t.a1[0][ks][l][j] = to_bf16(c); t.a1[1][ks][l][j] = to_bf16(-s); t.a1[2][ks][l][j] = to_bf16(s);
    int k1 = kperm_c(ks, h, j);
    c = C2K(64 * k1 * r); s = S2K(64 * k1 * r);
    t.a1c[0][ks][l][j] = to_bf16(c); t.a1c[1][ks][l][j] = to_bf16(s); t.a1c[2][ks][l][j] = to_bf16(-s);
  }
  return t;
}
struct A2T { u16 v[4][3][64][8]; };
constexpr A2T mk_a2(int u) {
  A2T t{};
  for (int kk = 0; kk < 4; kk++) for (int l = 0; l < 64; l++) for (int j = 0; j < 8; j++) {
    int h = l >> 5, row = 32 * u + (l & 31), col = 32 * (kk >> 1) + kperm_c(kk & 1, h, j);
    double c = C2K(32 * row * col), s = S2K(32 * row * col);
    t.v[kk][0][l][j] = to_bf16(c); t.v[kk][1][l][j] = to_bf16(-s); t.v[kk][2][l][j] = to_bf16(s);
  }
  return t;
}
struct TWT { float v[8][64][4]; };
constexpr TWT mk_tw(int t, int inv) {
  TWT o{};
  for (int q = 0; q < 8; q++) for (int l = 0; l < 64; l++) for (int e = 0; e < 2; e++) {
    int h = l >> 5, r = 2 * q + e;
    int n2 = inv ? 32 * t + (l & 31) : 32 * t + crow_c(r, h);
    int k1 = inv ? crow_c(r, h) : (l & 31);
    o.v[q][l][2 * e] = (float)C2K(n2 * k1); o.v[q][l][2 * e + 1] = (float)S2K(n2 * k1);
  }
  return o;
}
struct ColTabD { A1T a1; A2T a2[2]; TWT tw1[2]; TWT tw2[2]; };
constexpr A1T cA1 = mk_a1();
template <int U> constexpr A2T cA2 = mk_a2(U);
template <int T, int I> constexpr TWT cTW = mk_tw(T, I);
__device__ const ColTabD gCT = {cA1, {cA2<0>, cA2<1>}, {cTW<0, 0>, cTW<1, 0>}, {cTW<0, 1>, cTW<1, 1>}};
#else
struct ColTabD { struct { u16 a1[3][2][64][8]; u16 a1c[3][2][64][8]; } a1; struct { u16 v[4][3][64][8]; } a2[2]; struct { float v[8][64][4]; } tw1[2], tw2[2]; };
extern __device__ const T2All gT2;
extern __device__ const ColTabD gCT;
#endif

DEVI f16v mfma32(u4v a, u4v b, f16v c) { return __builtin_amdgcn_mfma_f32_32x32x16_bf16(__builtin_bit_cast(bf8, a), __builtin_bit_cast(bf8, b), c, 0, 0, 0); }
DEVI f16v mfma32k8(u2v a, u2v b, f16v c) { return __builtin_amdgcn_mfma_f32_32x32x8bf16_1k(__builtin_bit_cast(s4, a), __builtin_bit_cast(s4, b), c, 0, 0, 0); }
DEVI f4v mfma16(u4v a, u4v b, f4v c) { return __builtin_amdgcn_mfma_f32_16x16x32_bf16(__builtin_bit_cast(bf8, a), __builtin_bit_cast(bf8, b), c, 0, 0, 0); }
DEVI u32 pk2(float a, float b) { f2v v = {a, b}; return __builtin_bit_cast(u32, __builtin_convertvector(v, bf2)); }
DEVI float bflo(u32 w) { return __builtin_bit_cast(float, w << 16); }
DEVI float bfhi(u32 w) { return __builtin_bit_cast(float, w & 0xffff0000u); }
DEVI u4v lds128(const char* p) { return *(const LAS u4v*)(const LAS char*)p; }
DEVI u2v lds64(const char* p) { return *(const LAS u2v*)(const LAS char*)p; }
DEVI u2v ldstr(const char* p) { return __builtin_bit_cast(u2v, __builtin_amdgcn_ds_read_tr16_b64_v4i16((LAS s4*)(LAS char*)p)); }
DEVI u4v cat(u2v a, u2v b) { u4v r = {a.x, a.y, b.x, b.y}; return r; }
DEVI void glds16(const void* g, u32 lds) {
  u32 sv;
  asm volatile("s_mov_b32 %0, m0\n\ts_mov_b32 m0, %2\n\ts_nop 0\n\tglobal_load_lds_dwordx4 %1, off\n\ts_mov_b32 m0, %0" : "=&s"(sv) : "v"(g), "s"(lds) : "memory");
}
template <int N> DEVI void wait_vm() { asm volatile("s_waitcnt vmcnt(%0)" ::"i"(N) : "memory"); }
template <int N> DEVI void wait_vm_bar() { asm volatile("s_waitcnt vmcnt(%0) lgkmcnt(0)\n\ts_barrier" ::"i"(N) : "memory"); }
DEVI u32 uni(u32 v) { return __builtin_amdgcn_readfirstlane(v); }
DEVI u4v swap_pack(u32 a0, u32 a1, u32 b0, u32 b1) {
  auto r0 = __builtin_amdgcn_permlane32_swap(a0, b0, false, false);
  auto r1 = __builtin_amdgcn_permlane32_swap(a1, b1, false, false);
  u4v o = {r0[0], r1[0], r0[1], r1[1]};
  return o;
}
DEVI int swap23(int k) { return (k & ~12) | ((k & 4) << 1) | ((k & 8) >> 1); }

#define STAMP_BEGIN()
#define STAMP_END(P, K)
#define STAMP_MID0()
#define STAMP_MID1()
#define STAMP_DECL()
#define STAMP_ARG
#define STAMP_PASS
struct Params {
  const u16 *x, *wq, *wk, *wv, *wo, *wr, *wi, *gw;
  u16* out;
  char* ws;
};

constexpr int K1_LDS = 153664;
template <int B0, int B1, int B2>
DEVI void k1_tile(const Params& P, char* smem, int mt, int nt) {
  constexpr bool SP = (B1 == 2);
  constexpr int WROW = SP ? 64 : 192;
  constexpr int WB = 64 * WROW, SLOT = 16384 + WB, NWI = WB / 4096, NL = 4 + NWI;
  constexpr int RAWK = 5 * SLOT, RAWV = RAWK + 25600;
  const int tid = threadIdx.x, lane = tid & 63, w = uni(tid >> 6), r = lane & 31, h = lane >> 5;
  const u32 lds0 = (u32)(uintptr_t)(LAS char*)smem;
  const u16* T2 = (const u16*)&gT2;
  const u16* xs[4];
#pragma unroll
  for (int i = 0; i < 4; i++) {
    int row = (w * 4 + i) * 8 + (lane >> 3), ch = (lane & 7) ^ ((row >> 1) & 7), p = mt * 128 + row;
    xs[i] = P.x + (64 * (p & 31) + (p >> 5)) * 640 + ch * 8;
  }
  const u16* wsrc[NWI];
#pragma unroll
  for (int i = 0; i < NWI; i++) {
    int o = (w * NWI + i) * 1024 + lane * 16, row = o / WROW, c = (o % WROW) >> 4, bl = 3 * nt + (c >> 2);
    wsrc[i] = (bl < 20 ? P.wq + bl * 32 : T2 + (bl - 20) * 32) + row * 640 + (c & 3) * 8;
  }
  if constexpr (SP) {
    for (int i = w; i < 50; i += 4) {
      const u16* src = (i < 25 ? P.wk + i * 512 : P.wv + (i - 25) * 512) + lane * 8;
      glds16(src, uni(lds0 + RAWK + i * 1024));
    }
  }
  auto issue = [&](int t) {
    const u32 sl = lds0 + (t % 5) * SLOT;
#pragma unroll
    for (int i = 0; i < 4; i++) glds16(xs[i] + t * 64, uni(sl + (w * 4 + i) * 1024));
#pragma unroll
    for (int i = 0; i < NWI; i++) glds16(wsrc[i] + t * 64 * 640, uni(sl + 16384 + (w * NWI + i) * 1024));
  };
#pragma unroll
  for (int t = 0; t < 4; t++) issue(t);
  f16v acc[3] = {};
  const int xrow = 32 * w + r;
  const int xoff = xrow * 128, xsw = (xrow >> 1) & 7;
  const int trq = (lane & 15) >> 2, trc = ((lane >> 4) & 1) * 32 + (lane & 3) * 8;
  const int woff = 16384 + (8 * h + trq) * WROW + trc;
  const int roff = (8 * h + trq) * 40 + trc;
#pragma unroll
  for (int t = 0; t < 10; t++) {
    if (t <= 6) wait_vm_bar<3 * NL>(); else if (t == 7) wait_vm_bar<2 * NL>(); else if (t == 8) wait_vm_bar<NL>(); else wait_vm_bar<0>();
    if (t + 4 < 10) issue(t + 4);
    const char* sl = smem + (t % 5) * SLOT;
#pragma unroll
    for (int ks = 0; ks < 4; ks++) {
      u4v xf = lds128(sl + xoff + (((2 * ks + h) ^ xsw) << 4));
#pragma unroll
      for (int j = 0; j < 3; j++) {
        const int ty = j == 0 ? B0 : j == 1 ? B1 : B2;
        u4v wf;
        if (ty <= 1) {
          const char* a = sl + woff + ks * 16 * WROW + j * 64;
          wf = cat(ldstr(a), ldstr(a + 4 * WROW));
        } else {
          const char* a = smem + (ty == 2 ? RAWK : RAWV) + roff + (t * 64 + ks * 16) * 40;
          wf = cat(ldstr(a), ldstr(a + 160));
        }
        acc[j] = (ty == 0 || ty == 2) ? mfma32(wf, xf, acc[j]) : mfma32(xf, wf, acc[j]);
      }
    }
  }
  const int p0 = mt * 128 + 32 * w;
#pragma unroll
  for (int j = 0; j < 3; j++) {
    const int ty = j == 0 ? B0 : j == 1 ? B1 : B2;
    const int bl = 3 * nt + j;
    f16v a = acc[j];
    if (ty == 0) {
      const float qs = 0.22360679774997896f * 1.4426950408889634f;
      u16* dst = (u16*)(P.ws + WS_Q) + (p0 + r) * 640 + bl * 32 + 8 * h;
#pragma unroll
      for (int g = 0; g < 4; g += 2)
        *(u4v*)(dst + 8 * g) = swap_pack(pk2(a[4 * g] * qs, a[4 * g + 1] * qs), pk2(a[4 * g + 2] * qs, a[4 * g + 3] * qs),
                                         pk2(a[4 * g + 4] * qs, a[4 * g + 5] * qs), pk2(a[4 * g + 6] * qs, a[4 * g + 7] * qs));
    } else if (ty == 1) {
      u16* dst = (u16*)(P.ws + WS_X1) + ((bl - 20) * 32 + r) * 2048 + p0 + 8 * h;
#pragma unroll
      for (int g = 0; g < 4; g += 2)
        *(u4v*)(dst + 8 * g) = swap_pack(pk2(a[4 * g], a[4 * g + 1]), pk2(a[4 * g + 2], a[4 * g + 3]),
                                         pk2(a[4 * g + 4], a[4 * g + 5]), pk2(a[4 * g + 6], a[4 * g + 7]));
    } else if (ty == 2) {
      char* tile = P.ws + WS_KV + (p0 >> 5) * KVT;
      *(u4v*)(tile + lane * 16) = swap_pack(pk2(a[0], a[1]), pk2(a[2], a[3]), pk2(a[4], a[5]), pk2(a[6], a[7]));
      u2v k1 = {pk2(a[8], a[9]), pk2(a[10], a[11])};
      if (h == 0) *(u2v*)(tile + 1024 + r * 8) = k1;
      float n2 = 0.f;
#pragma unroll
      for (int i = 0; i < 8; i++) n2 += a[i] * a[i];
      if (h == 0) {
#pragma unroll
        for (int i = 8; i < 12; i++) n2 += a[i] * a[i];
      }
      n2 += __shfl_xor(n2, 32);
      float bad = (n2 == n2) ? 0.f : 1.f;
#pragma unroll
      for (int o = 16; o > 0; o >>= 1) { n2 = fmaxf(n2, __shfl_xor(n2, o)); bad = fmaxf(bad, __shfl_xor(bad, o)); }
      if (lane == 0) ((float*)(P.ws + WS_KM))[p0 >> 5] = bad > 0.f ? __builtin_inff() : n2;
    } else {
      char* tile = P.ws + WS_KV + (p0 >> 5) * KVT + 1280;
#pragma unroll
      for (int s = 0; s < 2; s++) {
        u4v v = {pk2(a[8 * s], a[8 * s + 1]), pk2(a[8 * s + 2], a[8 * s + 3]), pk2(a[8 * s + 4], a[8 * s + 5]), pk2(a[8 * s + 6], a[8 * s + 7])};
        if (r == 20) v = u4v{0x3f803f80u, 0x3f803f80u, 0x3f803f80u, 0x3f803f80u};
        if (r == 21) v = u4v{0u, 0u, 0u, 0u};
        if (r <= 21) *(u4v*)(tile + (2 * s + h) * 352 + r * 16) = v;
      }
    }
  }
}

DEVI void k1_transpose(const Params& P, char* smem, int uu) {
  const int tid = threadIdx.x, m = uu >> 1, half = uu & 1, f0 = 160 * half;
  u32* img = (u32*)smem;
#pragma unroll 4
  for (int i = tid; i < 5120; i += 256) {
    int arr = i >= 2560, j = arr ? i - 2560 : i, row = j / 20, c = j % 20;
    const u16* src = (arr ? P.wi : P.wr) + (128 * m + row) * 321 + f0 + c * 8;
    u4v v;
    __builtin_memcpy(&v, src, 16);
    u32* d = img + arr * (128 * 81) + row * 81 + c * 4;
    d[0] = v.x; d[1] = v.y; d[2] = v.z; d[3] = v.w;
  }
  if (half) {
    int arr = tid >> 7, row = tid & 127;
    u16 v = (arr ? P.wi : P.wr)[(128 * m + row) * 321 + 320];
    ((u16*)(img + arr * (128 * 81) + row * 81))[160] = v;
  }
  __syncthreads();
  const int lane = tid & 63, w = tid >> 6, k1 = lane & 31;
  const int nf = half ? 161 : 160;
  const u16* re = (const u16*)img;
  const u16* im = (const u16*)(img + 128 * 81);
  const int u = m >> 3, q = (m >> 1) & 3, hh = m & 1;
  for (int fl = 2 * w + (lane >> 5); fl < nf; fl += 8) {
    u4v o;
#pragma unroll
    for (int e = 0; e < 4; e++) {
      int idx = (32 * e + k1) * 162 + fl;
      o[e] = (u32)re[idx] | ((u32)im[idx] << 16);
    }
    *(u4v*)(P.ws + WS_WT + (size_t)(f0 + fl) * 8192 + ((u * 4 + q) * 64 + hh * 32 + k1) * 16) = o;
  }
}

__global__ void __launch_bounds__(256, 1) k1_proj(Params P) {
  extern __shared__ __attribute__((aligned(16))) char smem[];
  STAMP_DECL() STAMP_BEGIN()
  const int xcd = blockIdx.x & 7, idx = blockIdx.x >> 3;
  if (idx >= 28) { k1_transpose(P, smem, xcd * 4 + idx - 28); STAMP_END(P, 0); return; }
  const int mt = (xcd >> 1) * 4 + idx / 7, nt = (xcd & 1) * 7 + idx % 7;
  if (nt < 6) k1_tile<0, 0, 0>(P, smem, mt, nt);
  else if (nt == 6) k1_tile<0, 0, 1>(P, smem, mt, nt);
  else if (nt < 13) k1_tile<1, 1, 1>(P, smem, mt, nt);
  else k1_tile<1, 2, 3>(P, smem, mt, nt);
  STAMP_END(P, 0);
}

constexpr int CHUNK = 8 * KVT;
constexpr int K2_LDS = 3 * CHUNK;
constexpr float kBound2 = 48.f * 48.f;

DEVI void kv_dma0(const char* kvg, u32 lds0, int w, int lane) {
  const char* src = kvg + lane * 16;
#pragma unroll
  for (int j = 0; j < 4; j++) glds16(src + (w + 5 * j) * 1024, uni(lds0 + (w + 5 * j) * 1024));
  if (w == 0) glds16(src + 20 * 1024, uni(lds0 + 20 * 1024));
}
DEVI void kv_loader(const char* kvg, u32 lds0, int lane) {
  wait_vm_bar<0>();
  for (int c = 0; c < 8; c++) {
    if (c < 7) {
      const char* src = kvg + (c + 1) * CHUNK + lane * 16;
      const u32 dst = lds0 + ((c + 1) % 3) * CHUNK;
#pragma unroll
      for (int j = 0; j < 21; j++) glds16(src + j * 1024, uni(dst + j * 1024));
    }
    wait_vm_bar<0>();
  }
}

DEVI void attn_loop_safe(char* smem, int lane, u4v qa0, u2v qb0, u4v qa1, u2v qb1, f16v& o0, f16v& o1) {
  const int r = lane & 31, h = lane >> 5;
  const int koff = lane * 16, k1off = 1024 + r * 8;
  const int voff = 1280 + h * 352 + (r <= 20 ? r : 21) * 16;
  float m0 = -__builtin_inff(), m1 = -__builtin_inff();
  auto one = [&](u4v ka, u2v kb, u4v v0, u4v v1, u4v qa, u2v qb, float& mrun, f16v& o) {
    f16v s = {};
    s = mfma32(ka, qa, s);
    s = mfma32k8(kb, qb, s);
    float mx = s[0];
#pragma unroll
    for (int i = 1; i < 16; i++) mx = fmaxf(mx, s[i]);
    mx = fmaxf(mx, __shfl_xor(mx, 32));
    const float mn = fmaxf(mrun, mx), al = __builtin_amdgcn_exp2f(mrun - mn);
    mrun = mn;
    u4v p0, p1;
#pragma unroll
    for (int i = 0; i < 16; i++) o[i] *= al;
#pragma unroll
    for (int i = 0; i < 4; i++) {
      p0[i] = pk2(__builtin_amdgcn_exp2f(s[2 * i] - mn), __builtin_amdgcn_exp2f(s[2 * i + 1] - mn));
      p1[i] = pk2(__builtin_amdgcn_exp2f(s[8 + 2 * i] - mn), __builtin_amdgcn_exp2f(s[9 + 2 * i] - mn));
    }
    o = mfma32(v0, p0, o);
    o = mfma32(v1, p1, o);
  };
  wait_vm_bar<0>();
  for (int c = 0; c < 8; c++) {
    for (int i = 0; i < 8; i++) {
      if (i == 7) wait_vm_bar<0>();
      const char* p = smem + (c % 3) * CHUNK + i * KVT;
      u4v ka = lds128(p + koff), v0 = lds128(p + voff), v1 = lds128(p + voff + 704);
      u2v kb = lds64(p + k1off);
      one(ka, kb, v0, v1, qa0, qb0, m0, o0);
      one(ka, kb, v0, v1, qa1, qb1, m1, o1);
    }
  }
}

#define XBAR "  s_barrier\n"
#define XLIM "7"
DEVI void attn_loop_fast(u32 lds0, int lane, u4v qa0, u2v qb0, u4v qa1, u2v qb1, f16v& o0, f16v& o1) {
  const int r = lane & 31, h = lane >> 5;
  const u32 kb = lds0 + lane * 16, k1b = lds0 + 1024 + r * 8, vb = lds0 + 1280 + h * 352 + (r <= 20 ? r : 21) * 16;
  u32 kc, k1c, vc, kn, k1n, vn;
  u32 s_c, s_nx;
  asm volatile(R"ASM(
.macro EXP r
  v_exp_f32 v[\r], v[\r]
.endm
.macro CVT d, a
  v_cvt_pk_bf16_f32 v[72+\d], v[\a], v[\a+1]
.endm
.macro STEPA ka, k1a, koff, va, voff, bar
  .if \bar
)ASM" XBAR R"ASM(
  .endif
  v_mfma_f32_32x32x16_bf16 v[56:71], v[80:83], %[qa1], 0
  ds_read_b128 v[80:83], \ka offset:\koff
  EXP 40
  EXP 41
  EXP 42
  EXP 43
  v_mfma_f32_32x32x16_bf16 %[o1], v[90:93], v[76:79], %[o1]
  ds_read_b128 v[90:93], \va offset:\voff+704
  EXP 44
  CVT 0, 40
  EXP 45
  CVT 1, 42
  EXP 46
  v_mfma_f32_32x32x8_bf16 v[56:71], v[84:85], %[qb1], v[56:71]
  ds_read_b64 v[84:85], \k1a offset:\koff
  EXP 47
  EXP 48
  CVT 2, 44
  EXP 49
  CVT 3, 46
  EXP 50
  EXP 51
  s_waitcnt lgkmcnt(3)
  v_mfma_f32_32x32x16_bf16 %[o0], v[86:89], v[72:75], %[o0]
  EXP 52
  EXP 53
  CVT 4, 48
  EXP 54
  CVT 5, 50
  EXP 55
  CVT 6, 52
  s_waitcnt lgkmcnt(0)
  CVT 7, 54
.endm
.macro STEPB va, voff
  v_mfma_f32_32x32x16_bf16 v[40:55], v[80:83], %[qa0], 0
  EXP 56
  EXP 57
  EXP 58
  EXP 59
  v_mfma_f32_32x32x16_bf16 %[o0], v[90:93], v[76:79], %[o0]
  EXP 60
  CVT 0, 56
  EXP 61
  CVT 1, 58
  EXP 62
  v_mfma_f32_32x32x8_bf16 v[40:55], v[84:85], %[qb0], v[40:55]
  EXP 63
  EXP 64
  CVT 2, 60
  EXP 65
  CVT 3, 62
  EXP 66
  EXP 67
  v_mfma_f32_32x32x16_bf16 %[o1], v[86:89], v[72:75], %[o1]
  ds_read_b128 v[86:89], \va offset:\voff
  EXP 68
  EXP 69
  CVT 4, 64
  EXP 70
  CVT 5, 66
  EXP 71
  CVT 6, 68
  s_nop 0
  CVT 7, 70
.endm
  ; chunk 0 was issued by the caller
  s_waitcnt vmcnt(0)
  s_barrier
  ds_read_b128 v[80:83], %[kb]
  ds_read_b64 v[84:85], %[k1b]
  ds_read_b128 v[86:89], %[vb]
  v_mov_b32 v90, 0
  v_mov_b32 v91, 0
  v_mov_b32 v92, 0
  v_mov_b32 v93, 0
  v_mov_b32 v76, 0
  v_mov_b32 v77, 0
  v_mov_b32 v78, 0
  v_mov_b32 v79, 0
  v_mov_b32 %[kc], %[kb]
  v_mov_b32 %[k1c], %[k1b]
  v_mov_b32 %[vc], %[vb]
  s_mov_b32 %[snx], 21504
  v_add_u32 %[kn], 21504, %[kb]
  v_add_u32 %[k1n], 21504, %[k1b]
  v_add_u32 %[vn], 21504, %[vb]
  s_mov_b32 %[sc_], 0
  s_waitcnt lgkmcnt(0)
  v_mfma_f32_32x32x16_bf16 v[40:55], v[80:83], %[qa0], 0
  v_mfma_f32_32x32x8_bf16 v[40:55], v[84:85], %[qb0], v[40:55]
  s_nop 15
  s_nop 7
3:
  .irp i, 0, 1, 2, 3, 4, 5, 6
  STEPA %[kc], %[k1c], (\i+1)*2688, %[vc], \i*2688, 0
  STEPB %[vc], (\i+1)*2688
  .endr
  STEPA %[kn], %[k1n], 0, %[vc], 7*2688, 1
  STEPB %[vn], 0
  v_mov_b32 %[kc], %[kn]
  v_mov_b32 %[k1c], %[k1n]
  v_mov_b32 %[vc], %[vn]
  s_add_u32 %[snx], %[snx], 21504
  s_cmp_eq_u32 %[snx], 64512
  s_cselect_b32 %[snx], 0, %[snx]
  v_add_u32 %[kn], %[snx], %[kb]
  v_add_u32 %[k1n], %[snx], %[k1b]
  v_add_u32 %[vn], %[snx], %[vb]
  s_add_u32 %[sc_], %[sc_], 1
  s_cmp_lt_u32 %[sc_], 8
  s_cbranch_scc1 3b
  v_mfma_f32_32x32x16_bf16 %[o1], v[90:93], v[76:79], %[o1]
  s_waitcnt lgkmcnt(0)
  s_nop 15
  s_nop 7
.purgem EXP
.purgem CVT
.purgem STEPA
.purgem STEPB
)ASM"
      : [o0] "+v"(o0), [o1] "+v"(o1), [kc] "=&v"(kc), [k1c] "=&v"(k1c), [vc] "=&v"(vc), [kn] "=&v"(kn), [k1n] "=&v"(k1n), [vn] "=&v"(vn),
        [sc_] "=&s"(s_c), [snx] "=&s"(s_nx)
      : [qa0] "v"(qa0), [qb0] "v"(qb0), [qa1] "v"(qa1), [qb1] "v"(qb1), [kb] "v"(kb), [k1b] "v"(k1b), [vb] "v"(vb)
      : "memory", "scc", "v40", "v41", "v42", "v43", "v44", "v45", "v46", "v47", "v48", "v49", "v50", "v51", "v52", "v53", "v54", "v55",
        "v56", "v57", "v58", "v59", "v60", "v61", "v62", "v63", "v64", "v65", "v66", "v67", "v68", "v69", "v70", "v71", "v72", "v73", "v74", "v75",
        "v76", "v77", "v78", "v79", "v80", "v81", "v82", "v83", "v84", "v85", "v86", "v87", "v88", "v89", "v90", "v91", "v92", "v93");
}

DEVI void attn_wg(const Params& P, char* smem, int b STAMP_ARG) {
  const int tid = threadIdx.x, lane = tid & 63, w = uni(tid >> 6), r = lane & 31, h = lane >> 5;
  const u32 lds0 = (u32)(uintptr_t)(LAS char*)smem;
  const char* kvg = P.ws + WS_KV;
  kv_dma0(kvg, lds0, w, lane);
  if (w == 4) { kv_loader(kvg, lds0, lane); return; }
  const int p = 8 * b + 2 * w;
  u4v qa[2]; u2v qb[2];
  u32 xr[2][5], gr[5];
#pragma unroll
  for (int i = 0; i < 2; i++) {
    const u16* qrow = (const u16*)__builtin_assume_aligned((const u16*)(P.ws + WS_Q) + (p + i) * 640 + 20 * r, 8);
    __builtin_memcpy(&qa[i], qrow + 8 * h, 16);
    __builtin_memcpy(&qb[i], qrow + 16, 8);
    const int s = 64 * ((p + i) & 31) + ((p + i) >> 5);
    const u32* xp = (const u32*)(P.x + s * 640) + lane * 5;
#pragma unroll
    for (int j = 0; j < 5; j++) xr[i][j] = xp[j];
  }
#pragma unroll
  for (int j = 0; j < 5; j++) gr[j] = ((const u32*)P.gw)[lane * 5 + j];
  float km = ((const float*)(P.ws + WS_KM))[lane];
  float g[2], qn = 0.f;
#pragma unroll
  for (int i = 0; i < 2; i++) {
    if (h) qb[i] = u2v{0u, 0u};
    float z = 0.f;
#pragma unroll
    for (int j = 0; j < 5; j++) z += bflo(xr[i][j]) * bflo(gr[j]) + bfhi(xr[i][j]) * bfhi(gr[j]);
#pragma unroll
    for (int o = 32; o > 0; o >>= 1) z += __shfl_xor(z, o);
    g[i] = 1.f / (1.f + __expf(-z));
    if (lane == 0) ((float*)(P.ws + WS_G))[p + i] = g[i];
    float n = 0.f;
#pragma unroll
    for (int j = 0; j < 4; j++) n += bflo(qa[i][j]) * bflo(qa[i][j]) + bfhi(qa[i][j]) * bfhi(qa[i][j]);
#pragma unroll
    for (int j = 0; j < 2; j++) n += bflo(qb[i][j]) * bflo(qb[i][j]) + bfhi(qb[i][j]) * bfhi(qb[i][j]);
    n += __shfl_xor(n, 32);
    qn = (n > qn || n != n) ? n : qn;
  }
#pragma unroll
  for (int o = 32; o > 0; o >>= 1) km = fmaxf(km, __shfl_xor(km, o));
  const bool fast = __all(qn * km * 1.05f <= kBound2);
  f16v o0 = {}, o1 = {};
  STAMP_MID0();
  if (fast) attn_loop_fast(lds0, lane, qa[0], qb[0], qa[1], qb[1], o0, o1);
  else attn_loop_safe(smem, lane, qa[0], qb[0], qa[1], qb[1], o0, o1);
  STAMP_MID1();
#pragma unroll
  for (int i = 0; i < 2; i++) {
    const f16v& o = i ? o1 : o0;
    const float sc = g[i] / __shfl(o[8], r + 32);
    u16* dst = (u16*)(P.ws + WS_O) + (p + i) * 640 + 20 * r + 4 * h;
    u2v v0 = {pk2(o[0] * sc, o[1] * sc), pk2(o[2] * sc, o[3] * sc)};
    u2v v1 = {pk2(o[4] * sc, o[5] * sc), pk2(o[6] * sc, o[7] * sc)};
    u2v v2 = {pk2(o[8] * sc, o[9] * sc), pk2(o[10] * sc, o[11] * sc)};
    *(u2v*)dst = v0;
    *(u2v*)(dst + 8) = v1;
    if (h == 0) *(u2v*)(dst + 16) = v2;
  }
}

DEVI u4v pk8(const f16v& a, int s) {
  u4v v = {pk2(a[8 * s], a[8 * s + 1]), pk2(a[8 * s + 2], a[8 * s + 3]), pk2(a[8 * s + 4], a[8 * s + 5]), pk2(a[8 * s + 6], a[8 * s + 7])};
  return v;
}
DEVI void column_job(const Params& P, char* xch, int f, int t, int lane) {
  const int r = lane & 31, h = lane >> 5;
  const int cr = f == 0 ? 0 : f == 320 ? 1 : 2 * f;
  const bool real_in = (f == 0 || f == 320);
  const u16* zr = (const u16*)(P.ws + WS_X1) + cr * 2048 + (32 * t + r) * 32 + 8 * h;
  const u16* zi = zr + 2048;
  u4v Zr[2], Zi[2];
#pragma unroll
  for (int ks = 0; ks < 2; ks++) {
    Zr[ks] = *(const u4v*)(zr + 16 * ks);
    Zi[ks] = real_in ? u4v{0u, 0u, 0u, 0u} : *(const u4v*)(zi + 16 * ks);
  }
  const u4v* a1 = (const u4v*)gCT.a1.a1 + lane;
  const u4v* a1c = (const u4v*)gCT.a1.a1c + lane;
  const u4v* a2 = (const u4v*)gCT.a2[t].v + lane;
  const f4v* tw1 = (const f4v*)gCT.tw1[t].v + lane;
  const f4v* tw2 = (const f4v*)gCT.tw2[t].v + lane;
  const u4v* wt = (const u4v*)(P.ws + WS_WT + (size_t)f * 8192) + t * 256 + lane;
  f16v re = {}, im = {};
#pragma unroll
  for (int ks = 0; ks < 2; ks++) {
    u4v fr = a1[(0 * 2 + ks) * 64], fi = a1[(1 * 2 + ks) * 64], nfi = a1[(2 * 2 + ks) * 64];
    re = mfma32(Zr[ks], fr, re); re = mfma32(Zi[ks], nfi, re);
    im = mfma32(Zr[ks], fi, im); im = mfma32(Zi[ks], fr, im);
  }
#pragma unroll
  for (int q = 0; q < 8; q++) {
    f4v c = tw1[q * 64];
    float a0 = re[2 * q], b0 = im[2 * q], a1_ = re[2 * q + 1], b1 = im[2 * q + 1];
    re[2 * q] = a0 * c[0] + b0 * c[1]; im[2 * q] = b0 * c[0] - a0 * c[1];
    re[2 * q + 1] = a1_ * c[2] + b1 * c[3]; im[2 * q + 1] = b1 * c[2] - a1_ * c[3];
  }
  u4v yr[4], yi[4];
  auto exchange = [&](int phase) {
    u4v m0 = pk8(re, 0), m1 = pk8(re, 1), m2 = pk8(im, 0), m3 = pk8(im, 1);
    LAS u4v* mine = (LAS u4v*)(LAS char*)(xch + phase * 8192 + t * 4096) + lane;
    mine[0] = m0; mine[64] = m1; mine[128] = m2; mine[192] = m3;
    __syncthreads();
    const LAS u4v* oth = (const LAS u4v*)(LAS char*)(xch + phase * 8192 + (t ^ 1) * 4096) + lane;
    u4v o0 = oth[0], o1 = oth[64], o2 = oth[128], o3 = oth[192];
    yr[0] = t ? o0 : m0; yr[1] = t ? o1 : m1; yr[2] = t ? m0 : o0; yr[3] = t ? m1 : o1;
    yi[0] = t ? o2 : m2; yi[1] = t ? o3 : m3; yi[2] = t ? m2 : o2; yi[3] = t ? m3 : o3;
  };
  exchange(0);
  re = f16v{}; im = f16v{};
#pragma unroll
  for (int kk = 0; kk < 4; kk++) {
    u4v ar = a2[(kk * 3 + 0) * 64], ai = a2[(kk * 3 + 1) * 64], nai = a2[(kk * 3 + 2) * 64];
    re = mfma32(ar, yr[kk], re); re = mfma32(nai, yi[kk], re);
    im = mfma32(ai, yr[kk], im); im = mfma32(ar, yi[kk], im);
  }
  const float wsc = real_in ? 0.5f : 1.f;
#pragma unroll
  for (int q = 0; q < 4; q++) {
    u4v wv = wt[q * 64];
#pragma unroll
    for (int e = 0; e < 4; e++) {
      float wr_ = bflo(wv[e]) * wsc, wi_ = bfhi(wv[e]) * wsc, a = re[4 * q + e], b = im[4 * q + e];
      re[4 * q + e] = a * wr_ - b * wi_; im[4 * q + e] = a * wi_ + b * wr_;
    }
  }
  exchange(1);
  re = f16v{}; im = f16v{};
#pragma unroll
  for (int kk = 0; kk < 4; kk++) {
    u4v br = a2[(kk * 3 + 0) * 64], nbi = a2[(kk * 3 + 1) * 64], bi = a2[(kk * 3 + 2) * 64];
    re = mfma32(yr[kk], br, re); re = mfma32(yi[kk], nbi, re);
    im = mfma32(yr[kk], bi, im); im = mfma32(yi[kk], br, im);
  }
#pragma unroll
  for (int q = 0; q < 8; q++) {
    f4v c = tw2[q * 64];
    float a0 = re[2 * q], b0 = im[2 * q], a1_ = re[2 * q + 1], b1 = im[2 * q + 1];
    re[2 * q] = a0 * c[0] - b0 * c[1]; im[2 * q] = b0 * c[0] + a0 * c[1];
    re[2 * q + 1] = a1_ * c[2] - b1 * c[3]; im[2 * q + 1] = b1 * c[2] + a1_ * c[3];
  }
  u4v gr0 = pk8(re, 0), gr1 = pk8(re, 1), gi0 = pk8(im, 0), gi1 = pk8(im, 1);
  re = f16v{}; im = f16v{};
#pragma unroll
  for (int ks = 0; ks < 2; ks++) {
    u4v c = a1c[(0 * 2 + ks) * 64], ci = a1c[(1 * 2 + ks) * 64], nci = a1c[(2 * 2 + ks) * 64];
    u4v gr = ks ? gr1 : gr0, gi = ks ? gi1 : gi0;
    re = mfma32(c, gr, re); re = mfma32(nci, gi, re);
    im = mfma32(ci, gr, im); im = mfma32(c, gi, im);
  }
  u16* dst = (u16*)(P.ws + WS_X2) + cr * 2048 + (32 * t + r) * 32 + 8 * h;
#pragma unroll
  for (int g = 0; g < 4; g += 2) {
    *(u4v*)(dst + 8 * g) = swap_pack(pk2(re[4 * g], re[4 * g + 1]), pk2(re[4 * g + 2], re[4 * g + 3]),
                                     pk2(re[4 * g + 4], re[4 * g + 5]), pk2(re[4 * g + 6], re[4 * g + 7]));
    u4v vi = swap_pack(pk2(im[4 * g], im[4 * g + 1]), pk2(im[4 * g + 2], im[4 * g + 3]),
                       pk2(im[4 * g + 4], im[4 * g + 5]), pk2(im[4 * g + 6], im[4 * g + 7]));
    if (!real_in) *(u4v*)(dst + 2048 + 8 * g) = vi;
  }
}

__global__ void __launch_bounds__(320, 3) k2_mix(Params P) {
  extern __shared__ __attribute__((aligned(16))) char smem[];
  STAMP_DECL() STAMP_BEGIN()
  if (blockIdx.x < 256) { attn_wg(P, smem, blockIdx.x STAMP_PASS); STAMP_END(P, 1); return; }
  const int j = blockIdx.x - 256, w = uni(threadIdx.x >> 6), lane = threadIdx.x & 63;
  if (w >= 4) return;
  const int f = (w >> 1) ? 256 + j : j;
  if (f > 320) return;
  column_job(P, smem + (w >> 1) * 16384, f, w & 1, lane);
  STAMP_END(P, 1);
}

constexpr int K3_SLOT = 36864, K3_LDS = 4 * K3_SLOT;
__global__ void __launch_bounds__(512, 2) k3_out(Params P) {
  extern __shared__ __attribute__((aligned(16))) char smem[];
  STAMP_DECL() STAMP_BEGIN()
  const int xcd = blockIdx.x & 7, idx = blockIdx.x >> 3;
  const int mt = (xcd >> 1) * 8 + (idx >> 2), nt = (xcd & 1) * 4 + (idx & 3);
  const int p0 = mt * 64, n0 = nt * 80;
  const int tid = threadIdx.x, lane = tid & 63, w = uni(tid >> 6);
  const u32 lds0 = (u32)(uintptr_t)(LAS char*)smem;
  const u16* T2 = (const u16*)&gT2;
  const u16* X2 = (const u16*)(P.ws + WS_X2);
  const u16* O = (const u16*)(P.ws + WS_O);
  const u16 *srcb[5], *srca[5];
#pragma unroll
  for (int i = 0; i < 5; i++) {
    const int pc = w + 8 * i, o = pc * 1024 + lane * 16;
    if (i < 2) {
      int rho = o >> 7, s16 = (o >> 4) & 7, mb = (s16 >> 1) ^ ((rho >> 1) & 3);
      srcb[i] = X2 + swap23(rho) * 2048 + p0 + mb * 16 + (s16 & 1) * 8;
      int img = o >> 13, row = (o >> 7) & 63, ch = ((o >> 4) & 7) ^ ((row >> 1) & 7);
      srca[i] = O + (p0 + row) * 640 + img * 64 + ch * 8;
    } else {
      const int ow = (i < 4 || w < 4) ? o - 16384 : 0;
      int img = ow / 10240, rem = ow % 10240, row = rem >> 7, ch = ((rem >> 4) & 7) ^ ((row >> 1) & 7);
      srcb[i] = T2 + (n0 + row) * 640 + img * 64 + ch * 8;
      int rho = ow / 160, c = (ow % 160) >> 4;
      srca[i] = P.wo + swap23(rho) * 640 + n0 + c * 8;
    }
  }
  auto issue = [&](int st) {
    const u32 sl = lds0 + (st & 3) * K3_SLOT + w * 1024;
    if (st < 5) {
      glds16(srcb[0] + st * 128 * 2048, uni(sl)); glds16(srcb[1] + st * 128 * 2048, uni(sl + 8192));
      glds16(srcb[2] + st * 128, uni(sl + 16384)); glds16(srcb[3] + st * 128, uni(sl + 24576));
      if (w < 4) glds16(srcb[4] + st * 128, uni(sl + 32768));
    } else {
      const int k = st - 5;
      glds16(srca[0] + k * 128, uni(sl)); glds16(srca[1] + k * 128, uni(sl + 8192));
      glds16(srca[2] + k * 128 * 640, uni(sl + 16384)); glds16(srca[3] + k * 128 * 640, uni(sl + 24576));
      if (w < 4) glds16(srca[4] + k * 128 * 640, uni(sl + 32768));
    }
  };
  issue(0); issue(1); issue(2);
  const int mb = w & 3, kh = w >> 2, l15 = lane & 15, g = lane >> 4;
  const float gate = ((const float*)(P.ws + WS_G))[p0 + 16 * mb + l15];
  f4v acca[5] = {}, accb[5] = {};
  const int trq = l15 >> 2, trp = (lane & 3) * 8;
  const int rho0 = kh * 64 + 16 * (g >> 1) + 4 * (g & 1) + trq;
#pragma unroll
  for (int st = 0; st < 10; st++) {
    if (w < 4) { if (st <= 7) wait_vm_bar<10>(); else if (st == 8) wait_vm_bar<5>(); else wait_vm_bar<0>(); }
    else { if (st <= 7) wait_vm_bar<8>(); else if (st == 8) wait_vm_bar<4>(); else wait_vm_bar<0>(); }
    if (st + 3 < 10) issue(st + 3);
    const char* sl = smem + (st & 3) * K3_SLOT;
#pragma unroll
    for (int ks = 0; ks < 2; ks++) {
      if (st < 5) {
        const int r0 = rho0 + 32 * ks, r1 = r0 + 8;
        u4v act = cat(ldstr(sl + r0 * 128 + ((mb ^ ((r0 >> 1) & 3)) << 5) + trp), ldstr(sl + r1 * 128 + ((mb ^ ((r1 >> 1) & 3)) << 5) + trp));
#pragma unroll
        for (int nb = 0; nb < 5; nb++) {
          const int row = 16 * nb + l15;
          u4v wf = lds128(sl + 16384 + kh * 10240 + row * 128 + (((4 * ks + g) ^ ((row >> 1) & 7)) << 4));
          accb[nb] = mfma16(wf, act, accb[nb]);
        }
      } else {
        const int row = 16 * mb + l15;
        u4v act = lds128(sl + kh * 8192 + row * 128 + (((4 * ks + g) ^ ((row >> 1) & 7)) << 4));
        const char* wb = sl + 16384 + (rho0 + 32 * ks) * 160 + trp;
#pragma unroll
        for (int nb = 0; nb < 5; nb++) {
          u4v wf = cat(ldstr(wb + nb * 32), ldstr(wb + 8 * 160 + nb * 32));
          acca[nb] = mfma16(wf, act, acca[nb]);
        }
      }
    }
  }
  const float sb = (1.f - gate) * (2.f / (float)(S_ * D_));
  f4v v[5];
#pragma unroll
  for (int nb = 0; nb < 5; nb++) v[nb] = acca[nb] + accb[nb] * sb;
  __syncthreads();
  f4v* red = (f4v*)smem + mb * 320 + lane;
  if (kh == 1) {
#pragma unroll
    for (int nb = 0; nb < 5; nb++) red[nb * 64] = v[nb];
  }
  __syncthreads();
  if (kh == 0) {
    const int p = p0 + 16 * mb + l15, s = 64 * (p & 31) + (p >> 5);
    u16* dst = P.out + s * 640 + n0 + 4 * g;
#pragma unroll
    for (int nb = 0; nb < 5; nb++) {
      f4v t = v[nb] + red[nb * 64];
      u2v o = {pk2(t[0], t[1]), pk2(t[2], t[3])};
      *(u2v*)(dst + 16 * nb) = o;
    }
  }
  STAMP_END(P, 2);
}

extern "C" void kernel_launch(void* const* d_in, const int* in_sizes, int n_in, void* d_out, int out_size, void* d_ws,
                              size_t ws_size, hipStream_t stream) {
  Params P;
  P.x = (const u16*)d_in[0];
  P.wq = (const u16*)d_in[1];
  P.wk = (const u16*)d_in[2];
  P.wv = (const u16*)d_in[3];
  P.wo = (const u16*)d_in[4];
  P.wr = (const u16*)d_in[6];
  P.wi = (const u16*)d_in[7];
  P.gw = (const u16*)d_in[8];
  P.out = (u16*)d_out;
  P.ws = (char*)d_ws;
  hipFuncSetAttribute((const void*)k1_proj, hipFuncAttributeMaxDynamicSharedMemorySize, K1_LDS);
  hipFuncSetAttribute((const void*)k2_mix, hipFuncAttributeMaxDynamicSharedMemorySize, K2_LDS);
  hipFuncSetAttribute((const void*)k3_out, hipFuncAttributeMaxDynamicSharedMemorySize, K3_LDS);
  k1_proj<<<256, 256, K1_LDS, stream>>>(P);
  k2_mix<<<512, 320, K2_LDS, stream>>>(P);
  k3_out<<<256, 512, K3_LDS, stream>>>(P);
}
```

```cpp
#include <hip/hip_runtime.h>
#include <stdint.h>
#include <type_traits>
#include <utility>

#define MODE_SPLIT 0

typedef unsigned short u16;
typedef unsigned int u32;
typedef unsigned long long u64;
typedef __attribute__((ext_vector_type(8))) __bf16 bf8;
typedef __attribute__((ext_vector_type(4))) short s4;
typedef __attribute__((ext_vector_type(16))) float f16v;
typedef __attribute__((ext_vector_type(4))) float f4v;
typedef __attribute__((ext_vector_type(2))) float f2v;
typedef __attribute__((ext_vector_type(2))) __bf16 bf2;
typedef __attribute__((ext_vector_type(4))) u32 u4v;
typedef __attribute__((ext_vector_type(2))) u32 u2v;
#define LAS __attribute__((address_space(3)))
#define GAS __attribute__((address_space(1)))
#define DEVI __device__ __forceinline__

constexpr int S_ = 2048, D_ = 640;
constexpr int NTHR = 512, LDS_BYTES = 81920;

constexpr u32 WS_X1 = 0x000000;
constexpr u32 WS_X2 = 0x280000;
constexpr u32 WS_O = 0x500000;
constexpr u32 WS_WT = 0x780000;
constexpr u32 WS_KV = 0xA10000;
constexpr u32 WS_KM = 0xA3A000;
constexpr u32 WS_G = 0xA3B000;
constexpr u32 WS_FL = 0xA40000;
constexpr u32 WS_WOT = 0xA50000;
constexpr u32 FL_E = 0, FL_S1 = 0x800, FL_S2 = 0x1000, FL_S3 = 0x1800, FL_S4 = 0x2000, FL_SA = 0x2800, FL_SB = 0x3000;
constexpr int KVT = 2688;

constexpr double kPi = 3.14159265358979323846;
constexpr double cx_sin(double x) { double t = x, s = x; for (int i = 1; i < 10; i++) { t *= -x * x / ((2 * i) * (2 * i + 1)); s += t; } return s; }
constexpr double cx_cos(double x) { double t = 1, s = 1; for (int i = 1; i < 10; i++) { t *= -x * x / ((2 * i - 1) * (2 * i)); s += t; } return s; }
constexpr double cos2pi(int m, int N) {
  m %= N; if (m < 0) m += N;
  if (2 * m > N) m = N - m;
  if (4 * m > N) return -cos2pi(N / 2 - m, N);
  if (8 * m > N) return cx_sin(2 * kPi * (N / 4 - m) / N);
  return cx_cos(2 * kPi * m / N);
}
constexpr double sin2pi(int m, int N) { return cos2pi(m + 3 * (N / 4), N); }
constexpr u16 to_bf16(double d) { float f = (float)d; u32 u = __builtin_bit_cast(u32, f); u += 0x7fffu + ((u >> 16) & 1); return (u16)(u >> 16); }
constexpr int crow_c(int r, int h) { return (r & 3) + 8 * (r >> 2) + 4 * h; }
constexpr int kperm_c(int ks, int h, int j) { return 16 * ks + 8 * (j >> 2) + 4 * h + (j & 3); }

struct FragB { u16 v[20][64][8]; };
struct FragAll { FragB c[40]; };
struct A1T { u16 a1[2][2][64][8]; u16 a1c[2][2][64][8]; };
struct A2T { u16 v[4][2][64][8]; };
struct TWT { float v[8][64][4]; };
struct ColTabD { A1T a1; A2T a2[2]; TWT tw1[2]; TWT tw2[2]; };

#if defined(__HIP_DEVICE_COMPILE__) && __HIP_DEVICE_COMPILE__
struct Base640 { u16 c[640]; u16 ns[640]; };
constexpr Base640 mk_base() { Base640 b{}; for (int i = 0; i < 640; i++) { b.c[i] = to_bf16(cos2pi(i, 640)); b.ns[i] = to_bf16(-sin2pi(i, 640)); } return b; }
constexpr Base640 kBase = mk_base();
constexpr u16 Fval(int d, int comp) {
  return comp == 0 ? kBase.c[0] : comp == 1 ? kBase.c[(320 * d) % 640] : (comp & 1) ? kBase.ns[((comp >> 1) * d) % 640] : kBase.c[((comp >> 1) * d) % 640];
}
constexpr FragB mk_frag(int cb, int inv) {
  FragB r{};
  for (int ks = 0; ks < 20; ks++) for (int l = 0; l < 64; l++) for (int j = 0; j < 8; j++) {
    int n = 16 * cb + (l & 15), k = 32 * ks + 8 * (l >> 4) + j;
    r.v[ks][l][j] = inv ? Fval(n, k) : Fval(k, n);
  }
  return r;
}
template <int C, int I> constexpr FragB cFrag = mk_frag(C, I);
template <int INV, int... I> constexpr FragAll mk_fragall(std::integer_sequence<int, I...>) { return FragAll{{cFrag<I, INV>...}}; }
__device__ const FragAll gTF = mk_fragall<0>(std::make_integer_sequence<int, 40>{});
__device__ const FragAll gTI = mk_fragall<1>(std::make_integer_sequence<int, 40>{});

struct Trig2048 { double c[2048]; };
constexpr Trig2048 mk_trig() { Trig2048 t{}; for (int i = 0; i < 2048; i++) t.c[i] = cos2pi(i, 2048); return t; }
constexpr Trig2048 kTrig = mk_trig();
constexpr double C2K(int m) { return kTrig.c[m & 2047]; }
constexpr double S2K(int m) { return kTrig.c[(m + 1536) & 2047]; }
constexpr A1T mk_a1() {
  A1T t{};
  for (int ks = 0; ks < 2; ks++) for (int l = 0; l < 64; l++) for (int j = 0; j < 8; j++) {
    int h = l >> 5, r = l & 31;
    int n1 = 16 * ks + 8 * h + j;
    double c = C2K(64 * n1 * r), s = S2K(64 * n1 * r);
    t.a1[0][ks][l][j] = to_bf16(c); t.a1[1][ks][l][j] = to_bf16(-s);
    int k1 = kperm_c(ks, h, j);
    c = C2K(64 * k1 * r); s = S2K(64 * k1 * r);
    t.a1c[0][ks][l][j] = to_bf16(c); t.a1c[1][ks][l][j] = to_bf16(-s);
  }
  return t;
}
constexpr A2T mk_a2(int u) {
  A2T t{};
  for (int kk = 0; kk < 4; kk++) for (int l = 0; l < 64; l++) for (int j = 0; j < 8; j++) {
    int h = l >> 5, row = 32 * u + (l & 31), col = 32 * (kk >> 1) + kperm_c(kk & 1, h, j);
    double c = C2K(32 * row * col), s = S2K(32 * row * col);
    t.v[kk][0][l][j] = to_bf16(c); t.v[kk][1][l][j] = to_bf16(-s);
  }
  return t;
}
constexpr TWT mk_tw(int t, int inv) {
  TWT o{};
  for (int q = 0; q < 8; q++) for (int l = 0; l < 64; l++) for (int e = 0; e < 2; e++) {
    int h = l >> 5, r = 2 * q + e;
    int n2 = inv ? 32 * t + (l & 31) : 32 * t + crow_c(r, h);
    int k1 = inv ? crow_c(r, h) : (l & 31);
    o.v[q][l][e] = (float)C2K(n2 * k1); o.v[q][l][2 + e] = (float)S2K(n2 * k1);
  }
  return o;
}
constexpr A1T cA1 = mk_a1();
template <int U> constexpr A2T cA2 = mk_a2(U);
template <int T, int I> constexpr TWT cTW = mk_tw(T, I);
__device__ const ColTabD gCT = {cA1, {cA2<0>, cA2<1>}, {cTW<0, 0>, cTW<1, 0>}, {cTW<0, 1>, cTW<1, 1>}};
#else
extern __device__ const FragAll gTF;
extern __device__ const FragAll gTI;
extern __device__ const ColTabD gCT;
#endif

DEVI f16v mfma32(u4v a, u4v b, f16v c) { return __builtin_amdgcn_mfma_f32_32x32x16_bf16(__builtin_bit_cast(bf8, a), __builtin_bit_cast(bf8, b), c, 0, 0, 0); }
DEVI f16v mfma32k8(u2v a, u2v b, f16v c) { return __builtin_amdgcn_mfma_f32_32x32x8bf16_1k(__builtin_bit_cast(s4, a), __builtin_bit_cast(s4, b), c, 0, 0, 0); }
DEVI f4v mfma16(u4v a, u4v b, f4v c) { return __builtin_amdgcn_mfma_f32_16x16x32_bf16(__builtin_bit_cast(bf8, a), __builtin_bit_cast(bf8, b), c, 0, 0, 0); }
DEVI u32 pk2(float a, float b) { f2v v = {a, b}; return __builtin_bit_cast(u32, __builtin_convertvector(v, bf2)); }
DEVI float bflo(u32 w) { return __builtin_bit_cast(float, w << 16); }
DEVI float bfhi(u32 w) { return __builtin_bit_cast(float, w & 0xffff0000u); }
DEVI u4v lds128(const char* p) { return *(const LAS u4v*)(const LAS char*)p; }
DEVI u2v lds64(const char* p) { return *(const LAS u2v*)(const LAS char*)p; }
DEVI void sts128(char* p, u4v v) { *(LAS u4v*)(LAS char*)p = v; }
DEVI void sts64(char* p, u2v v) { *(LAS u2v*)(LAS char*)p = v; }
DEVI u2v ldstr(const char* p) { return __builtin_bit_cast(u2v, __builtin_amdgcn_ds_read_tr16_b64_v4i16((LAS s4*)(LAS char*)p)); }
DEVI u4v cat(u2v a, u2v b) { u4v r = {a.x, a.y, b.x, b.y}; return r; }
DEVI void glds16(const void* base, u32 voff, u32 lds) {
  u32 sv;
  asm volatile("s_mov_b32 %0, m0\n\ts_mov_b32 m0, %3\n\ts_nop 0\n\tglobal_load_lds_dwordx4 %1, %2\n\ts_mov_b32 m0, %0" : "=&s"(sv) : "v"(voff), "s"(base), "s"(lds) : "memory");
}
DEVI void glds16c(const void* base, u32 voff, u32 lds) {
  u32 sv;
  asm volatile("s_mov_b32 %0, m0\n\ts_mov_b32 m0, %3\n\ts_nop 0\n\tglobal_load_lds_dwordx4 %1, %2 sc1\n\ts_mov_b32 m0, %0" : "=&s"(sv) : "v"(voff), "s"(base), "s"(lds) : "memory");
}
template <int N> DEVI void wait_vm() { asm volatile("s_waitcnt vmcnt(%0)" ::"i"(N) : "memory"); }
template <int N> DEVI void wait_vm_bar() { asm volatile("s_waitcnt vmcnt(%0) lgkmcnt(0)\n\ts_barrier" ::"i"(N) : "memory"); }
template <int N> DEVI void vm_bar() { asm volatile("s_waitcnt vmcnt(%0)\n\ts_barrier" ::"i"(N) : "memory"); }
DEVI void raw_bar() { asm volatile("s_barrier" ::: "memory"); }
DEVI void lds_bar() { asm volatile("s_waitcnt lgkmcnt(0)\n\ts_barrier" ::: "memory"); }
DEVI u32 uni(u32 v) { return __builtin_amdgcn_readfirstlane(v); }
DEVI int fresh_tid() { int t = threadIdx.x; asm volatile("" : "+v"(t)); return t; }
DEVI u4v swap_pack(u32 a0, u32 a1, u32 b0, u32 b1) {
  auto r0 = __builtin_amdgcn_permlane32_swap(a0, b0, false, false);
  auto r1 = __builtin_amdgcn_permlane32_swap(a1, b1, false, false);
  u4v o = {r0[0], r1[0], r0[1], r1[1]};
  return o;
}
template <int CTRL> DEVI float dpp(float v) { return __builtin_bit_cast(float, __builtin_amdgcn_update_dpp(0, __builtin_bit_cast(int, v), CTRL, 0xf, 0xf, true)); }
DEVI float xor16(float v) { auto r = __builtin_amdgcn_permlane16_swap(__builtin_bit_cast(u32, v), __builtin_bit_cast(u32, v), false, false); return __builtin_bit_cast(float, (threadIdx.x & 16) ? r[0] : r[1]); }
DEVI float xor32(float v) { auto r = __builtin_amdgcn_permlane32_swap(__builtin_bit_cast(u32, v), __builtin_bit_cast(u32, v), false, false); return __builtin_bit_cast(float, (threadIdx.x & 32) ? r[0] : r[1]); }
DEVI float nmax(float a, float b) { return (a > b || a != a) ? a : b; }
DEVI float max16(float v) {
  v = nmax(v, dpp<0xB1>(v)); v = nmax(v, dpp<0x4E>(v)); v = nmax(v, dpp<0x141>(v)); return nmax(v, dpp<0x140>(v));
}
DEVI float max64(float v) { v = max16(v); v = nmax(v, xor16(v)); return nmax(v, xor32(v)); }
DEVI int swap23(int k) { return (k & ~12) | ((k & 4) << 1) | ((k & 8) >> 1); }
DEVI int s_of_p(int p) { return 64 * (p & 31) + (p >> 5); }

typedef __amdgpu_buffer_rsrc_t rsrc_t;
constexpr int SC1 = 16;
DEVI rsrc_t ws_rsrc(const char* ws) { return __builtin_amdgcn_make_buffer_rsrc((void*)ws, 0, 0x1000000, 0x00020000); }
DEVI void st128c(rsrc_t r, u32 off, u4v v) { __builtin_amdgcn_raw_buffer_store_b128(v, r, off, 0, SC1); }
DEVI void st64c(rsrc_t r, u32 off, u2v v) { __builtin_amdgcn_raw_buffer_store_b64(v, r, off, 0, SC1); }
DEVI void st32c(rsrc_t r, u32 off, u32 v) { __builtin_amdgcn_raw_buffer_store_b32(v, r, off, 0, SC1); }
DEVI u4v ld128c(rsrc_t r, u32 off) { return __builtin_amdgcn_raw_buffer_load_b128(r, off, 0, SC1); }
DEVI u32 ld32c(rsrc_t r, u32 off) { return __builtin_amdgcn_raw_buffer_load_b32(r, off, 0, SC1); }

struct Params {
  const u16 *x, *wq, *wk, *wv, *wo, *wr, *wi, *gw;
  u16* out;
  char* ws;
};

#define STAMP_DECL()
#define STMP(n)
#define STAMP_ARG
#define STAMP_PASS
#define STAMP_END(P, K)

DEVI u64 read_epoch(const Params& P) { return __hip_atomic_load((GAS u64*)(P.ws + WS_FL + FL_E), __ATOMIC_RELAXED, __HIP_MEMORY_SCOPE_AGENT); }
DEVI u64 tag_of(u64 e) { return (((u64)uni((u32)(e >> 32)) << 32) | uni((u32)e)) + 1; }
DEVI void put_flag(const Params& P, u32 off, u64 tag) {
  __hip_atomic_store((GAS u64*)(P.ws + WS_FL + off), tag, __ATOMIC_RELAXED, __HIP_MEMORY_SCOPE_AGENT);
}
DEVI void publish(const Params& P, u32 off, u64 tag) {
  asm volatile("s_waitcnt vmcnt(0) lgkmcnt(0)\n\ts_barrier" ::: "memory");
  if (threadIdx.x == 0) put_flag(P, off, tag);
}
template <int N> DEVI bool test_flags(const Params& P, u32 off, u64 tag, int lane) {
  const GAS u64* f = (const GAS u64*)(P.ws + WS_FL + off);
  bool ok = true;
#pragma unroll
  for (int i = 0; i < (N + 63) / 64; i++) {
    const int idx = N >= 64 ? lane + 64 * i : (lane & (N - 1));
    ok &= __hip_atomic_load(f + idx, __ATOMIC_RELAXED, __HIP_MEMORY_SCOPE_AGENT) == tag;
  }
  return __all(ok);
}
#define PSLEEP 4
template <int N, int SLEEP = PSLEEP> DEVI void spin_flags(const Params& P, u32 off, u64 tag, int lane) {
  for (int it = 0; it < (1 << 20); it++) {
    if (test_flags<N>(P, off, tag, lane)) break;
    __builtin_amdgcn_s_sleep(SLEEP);
  }
}
template <int N, int SLEEP = PSLEEP> DEVI void wait_flags(const Params& P, u32 off, u64 tag) {
  if (uni(threadIdx.x >> 6) == 0) spin_flags<N, SLEEP>(P, off, tag, threadIdx.x & 63);
  asm volatile("s_waitcnt vmcnt(0) lgkmcnt(0)\n\ts_barrier" ::: "memory");
}

constexpr int CHUNK = 8 * KVT;
constexpr int KV_BASE = LDS_BYTES - 3 * CHUNK;
#define SA_T 7
#define HINT_CHUNK 7
#define HINT_SLEEP 40
constexpr float kBound2 = 48.f * 48.f;

DEVI void kv_fetch(const char* src, u32 dst, int lw, int lane) {
#pragma unroll
  for (int j = 0; j < 5; j++) glds16c(src + (lw + 4 * j) * 1024, lane * 16, dst + (lw + 4 * j) * 1024);
  if (lw == 0) glds16c(src + 20 * 1024, lane * 16, dst + 20 * 1024);
}
DEVI void kv_chunk0_4(const char* kvg, u32 lds0, int lw, int lane) { kv_fetch(kvg, lds0 + KV_BASE + 2 * CHUNK, lw, lane); }
template <bool FUSED>
DEVI void kv_loader(const Params& P, const char* kvg, u32 lds0, int lw, int lane, int wgi, u64 tag STAMP_ARG) {
  for (int c = 0; c < 8; c++) {
    if (c < 7) kv_fetch(kvg + (c + 1) * CHUNK, lds0 + KV_BASE + (c % 3) * CHUNK, lw, lane);
    if (FUSED && c == HINT_CHUNK && lw == 0) {
      __builtin_amdgcn_s_sleep(HINT_SLEEP);
      if (lane == 0) put_flag(P, FL_SB + wgi * 8, tag);
    }
    wait_vm_bar<0>();
  }
  if (FUSED && HINT_CHUNK == 8 && lw == 0 && lane == 0) put_flag(P, FL_SB + wgi * 8, tag);
}

DEVI void attn_loop_safe(char* smem, int lane, u4v qa0, u2v qb0, u4v qa1, u2v qb1, f16v& o0, f16v& o1) {
  const int r = lane & 31, h = lane >> 5;
  const int koff = lane * 16, k1off = 1024 + r * 8;
  const int voff = 1280 + h * 352 + (r <= 20 ? r : 21) * 16;
  float m0 = -__builtin_inff(), m1 = -__builtin_inff();
  auto one = [&](u4v ka, u2v kb, u4v v0, u4v v1, u4v qa, u2v qb, float& mrun, f16v& o) {
    f16v s = {};
    s = mfma32(ka, qa, s);
    s = mfma32k8(kb, qb, s);
    float mx = s[0];
#pragma unroll
    for (int i = 1; i < 16; i++) mx = fmaxf(mx, s[i]);
    mx = fmaxf(mx, __shfl_xor(mx, 32));
    const float mn = fmaxf(mrun, mx), al = __builtin_amdgcn_exp2f(mrun - mn);
    mrun = mn;
    u4v p0, p1;
#pragma unroll
    for (int i = 0; i < 16; i++) o[i] *= al;
#pragma unroll
    for (int i = 0; i < 4; i++) {
      p0[i] = pk2(__builtin_amdgcn_exp2f(s[2 * i] - mn), __builtin_amdgcn_exp2f(s[2 * i + 1] - mn));
      p1[i] = pk2(__builtin_amdgcn_exp2f(s[8 + 2 * i] - mn), __builtin_amdgcn_exp2f(s[9 + 2 * i] - mn));
    }
    o = mfma32(v0, p0, o);
    o = mfma32(v1, p1, o);
  };
  for (int c = 0; c < 8; c++) {
    for (int i = 0; i < 8; i++) {
      if (i == 7) lds_bar();
      const char* p = smem + KV_BASE + ((c + 2) % 3) * CHUNK + i * KVT;
      u4v ka = lds128(p + koff), v0 = lds128(p + voff), v1 = lds128(p + voff + 704);
      u2v kb = lds64(p + k1off);
      one(ka, kb, v0, v1, qa0, qb0, m0, o0);
      one(ka, kb, v0, v1, qa1, qb1, m1, o1);
    }
  }
}

DEVI void attn_loop_fast(u32 lds0, int lane, u4v qa0, u2v qb0, u4v qa1, u2v qb1, f16v& o0, f16v& o1) {
  const int r = lane & 31, h = lane >> 5;
  const u32 base = lds0 + KV_BASE;
  const u32 kb = base + lane * 16, k1b = base + 1024 + r * 8, vb = base + 1280 + h * 352 + (r <= 20 ? r : 21) * 16;
  u32 kc, k1c, vc, kn, k1n, vn;
  u32 s_c, s_nx;
  asm volatile(R"ASM(
.macro EXP r
  v_exp_f32 v[\r], v[\r]
.endm
.macro CVT d, a
  v_cvt_pk_bf16_f32 v[72+\d], v[\a], v[\a+1]
.endm
.macro STEPA ka, k1a, koff, va, voff, bar
  .if \bar
  s_barrier
  .endif
  v_mfma_f32_32x32x16_bf16 v[56:71], v[80:83], %[qa1], 0
  ds_read_b128 v[80:83], \ka offset:\koff
  EXP 40
  EXP 41
  EXP 42
  EXP 43
  v_mfma_f32_32x32x16_bf16 %[o1], v[90:93], v[76:79], %[o1]
  ds_read_b128 v[90:93], \va offset:\voff+704
  EXP 44
  CVT 0, 40
  EXP 45
  CVT 1, 42
  EXP 46
  v_mfma_f32_32x32x8_bf16 v[56:71], v[84:85], %[qb1], v[56:71]
  ds_read_b64 v[84:85], \k1a offset:\koff
  EXP 47
  EXP 48
  CVT 2, 44
  EXP 49
  CVT 3, 46
  EXP 50
  EXP 51
  s_waitcnt lgkmcnt(3)
  v_mfma_f32_32x32x16_bf16 %[o0], v[86:89], v[72:75], %[o0]
  EXP 52
  EXP 53
  CVT 4, 48
  EXP 54
  CVT 5, 50
  EXP 55
  CVT 6, 52
  s_waitcnt lgkmcnt(0)
  CVT 7, 54
.endm
.macro STEPB va, voff
  v_mfma_f32_32x32x16_bf16 v[40:55], v[80:83], %[qa0], 0
  EXP 56
  EXP 57
  EXP 58
  EXP 59
  v_mfma_f32_32x32x16_bf16 %[o0], v[90:93], v[76:79], %[o0]
  EXP 60
  CVT 0, 56
  EXP 61
  CVT 1, 58
  EXP 62
  v_mfma_f32_32x32x8_bf16 v[40:55], v[84:85], %[qb0], v[40:55]
  EXP 63
  EXP 64
  CVT 2, 60
  EXP 65
  CVT 3, 62
  EXP 66
  EXP 67
  v_mfma_f32_32x32x16_bf16 %[o1], v[86:89], v[72:75], %[o1]
  ds_read_b128 v[86:89], \va offset:\voff
  EXP 68
  EXP 69
  CVT 4, 64
  EXP 70
  CVT 5, 66
  EXP 71
  CVT 6, 68
  s_nop 0
  CVT 7, 70
.endm
  ; chunk 0 sits in slot 2, chunk 1 goes to slot 0
  v_add_u32 %[kc], 43008, %[kb]
  v_add_u32 %[k1c], 43008, %[k1b]
  v_add_u32 %[vc], 43008, %[vb]
  s_nop 0
  ds_read_b128 v[80:83], %[kc]
  ds_read_b64 v[84:85], %[k1c]
  ds_read_b128 v[86:89], %[vc]
  v_mov_b32 v90, 0
  v_mov_b32 v91, 0
  v_mov_b32 v92, 0
  v_mov_b32 v93, 0
  v_mov_b32 v76, 0
  v_mov_b32 v77, 0
  v_mov_b32 v78, 0
  v_mov_b32 v79, 0
  s_mov_b32 %[snx], 0
  v_mov_b32 %[kn], %[kb]
  v_mov_b32 %[k1n], %[k1b]
  v_mov_b32 %[vn], %[vb]
  s_mov_b32 %[sc_], 0
  s_waitcnt lgkmcnt(0)
  v_mfma_f32_32x32x16_bf16 v[40:55], v[80:83], %[qa0], 0
  v_mfma_f32_32x32x8_bf16 v[40:55], v[84:85], %[qb0], v[40:55]
  s_nop 15
  s_nop 7
3:
  .irp i, 0, 1, 2, 3, 4, 5, 6
  STEPA %[kc], %[k1c], (\i+1)*2688, %[vc], \i*2688, 0
  STEPB %[vc], (\i+1)*2688
  .endr
  STEPA %[kn], %[k1n], 0, %[vc], 7*2688, 1
  STEPB %[vn], 0
  v_mov_b32 %[kc], %[kn]
  v_mov_b32 %[k1c], %[k1n]
  v_mov_b32 %[vc], %[vn]
  s_add_u32 %[snx], %[snx], 21504
  s_cmp_eq_u32 %[snx], 64512
  s_cselect_b32 %[snx], 0, %[snx]
  v_add_u32 %[kn], %[snx], %[kb]
  v_add_u32 %[k1n], %[snx], %[k1b]
  v_add_u32 %[vn], %[snx], %[vb]
  s_add_u32 %[sc_], %[sc_], 1
  s_cmp_lt_u32 %[sc_], 8
  s_cbranch_scc1 3b
  v_mfma_f32_32x32x16_bf16 %[o1], v[90:93], v[76:79], %[o1]
  s_waitcnt lgkmcnt(0)
  s_nop 15
  s_nop 7
.purgem EXP
.purgem CVT
.purgem STEPA
.purgem STEPB
)ASM"
      : [o0] "+v"(o0), [o1] "+v"(o1), [kc] "=&v"(kc), [k1c] "=&v"(k1c), [vc] "=&v"(vc), [kn] "=&v"(kn), [k1n] "=&v"(k1n), [vn] "=&v"(vn),
        [sc_] "=&s"(s_c), [snx] "=&s"(s_nx)
      : [qa0] "v"(qa0), [qb0] "v"(qb0), [qa1] "v"(qa1), [qb1] "v"(qb1), [kb] "v"(kb), [k1b] "v"(k1b), [vb] "v"(vb)
      : "memory", "scc", "v40", "v41", "v42", "v43", "v44", "v45", "v46", "v47", "v48", "v49", "v50", "v51", "v52", "v53", "v54", "v55",
        "v56", "v57", "v58", "v59", "v60", "v61", "v62", "v63", "v64", "v65", "v66", "v67", "v68", "v69", "v70", "v71", "v72", "v73", "v74", "v75",
        "v76", "v77", "v78", "v79", "v80", "v81", "v82", "v83", "v84", "v85", "v86", "v87", "v88", "v89", "v90", "v91", "v92", "v93");
}

template <bool FUSED>
DEVI void a_wg(const Params& P, char* smem, int mt, int nt, int wgi STAMP_ARG) {
  const int tid = fresh_tid(), lane = tid & 63, w = uni(tid >> 6), r = lane & 31, h = lane >> 5;
  const u32 lds0 = (u32)(uintptr_t)(LAS char*)smem;
  const int p0 = mt * 64;
#define APRIO0 0
  __builtin_amdgcn_s_setprio(APRIO0);
  u64 epoch = 0;
  if (FUSED) epoch = read_epoch(P);
  const char* kvg = P.ws + WS_KV;
  u64 tag = 0;
  u4v qa[2]; u2v qb[2];
  float qn;
  auto qproj = [&](auto NPc) {
    constexpr int NP = NPc;
    u32 xoff, voff[NP - 1];
    {
      const int row = w * 8 + (lane >> 3), ch = (lane & 7) ^ ((row >> 1) & 7);
      xoff = (s_of_p(p0 + row) * 640 + ch * 8) * 2;
    }
    const int wp0 = NP == 2 ? w : 4 + 2 * (w - 4);
#pragma unroll
    for (int j = 0; j < NP - 1; j++) {
      const int o = (wp0 + j) * 1024 + lane * 16, row = o / 192, c = (o % 192) >> 4, col = 80 * nt + 8 * c;
      voff[j] = (row * 640 + (col > 632 ? 632 : col)) * 2;
    }
    auto xpiece = [&](int t) { glds16((const char*)P.x + t * 128, xoff, lds0 + (t & 3) * 20480 + w * 1024); };
    auto wpiece = [&](int t, int j) { glds16((const char*)P.wq + t * (64 * 1280), voff[j], lds0 + (t & 3) * 20480 + 8192 + (wp0 + j) * 1024); };
#pragma unroll
    for (int t = 0; t < 3; t++) {
      xpiece(t);
#pragma unroll
      for (int j = 0; j < NP - 1; j++) wpiece(t, j);
    }
    int xo[4];
#pragma unroll
    for (int ks = 0; ks < 4; ks++) xo[ks] = (w >> 2) * 4096 + r * 128 + (((2 * ks + h) ^ ((r >> 1) & 7)) << 4);
    const int woff = 8192 + (8 * h + ((lane & 15) >> 2)) * 192 + 40 * (w & 3) + ((lane >> 4) & 1) * 32 + (lane & 3) * 8;
    struct Fr { u4v x, wf; };
    auto rd = [&](const char* sl, int ks) {
      Fr f;
      f.x = lds128(sl + xo[ks]);
      const char* a = sl + woff + ks * 16 * 192;
      f.wf = cat(ldstr(a), ldstr(a + 4 * 192));
      return f;
    };
    f16v q = {};
    STMP(8);
    vm_bar<2 * NP>();
    STMP(9);
    if (FUSED) tag = tag_of(epoch);
    Fr fa = rd(smem, 0), fb = rd(smem, 1);
    auto stage = [&](int t, auto DMA, auto&& wait) {
      const char* sl = smem + (t & 3) * 20480;
      const char* sl1 = smem + ((t + 1) & 3) * 20480;
      q = mfma32(fa.wf, fa.x, q); fa = rd(sl, 2);
      if constexpr (DMA) xpiece(t + 3);
      __builtin_amdgcn_sched_barrier(0);
      q = mfma32(fb.wf, fb.x, q); fb = rd(sl, 3);
      if constexpr (DMA) wpiece(t + 3, 0);
      __builtin_amdgcn_sched_barrier(0);
      q = mfma32(fa.wf, fa.x, q);
      if constexpr (DMA && NP == 3) wpiece(t + 3, NP - 2);
      wait();
      fa = rd(sl1, 0);
      __builtin_amdgcn_sched_barrier(0);
      q = mfma32(fb.wf, fb.x, q); fb = rd(sl1, 1);
      __builtin_amdgcn_sched_barrier(0);
    };
#pragma unroll 1
    for (int t = 0; t < 7; t++) {
      stage(t, std::true_type{}, [&] { wait_vm_bar<2 * NP>(); });
      if (FUSED && t == SA_T && w == 4 && lane == 0) put_flag(P, FL_SA + wgi * 8, tag);
    }
    STMP(10);
    u64 fl[4];
    if (FUSED) {
#pragma unroll
      for (int k = 0; k < 4; k++) fl[k] = __hip_atomic_load((const GAS u64*)(P.ws + WS_FL + FL_S1) + lane + 64 * k, __ATOMIC_RELAXED, __HIP_MEMORY_SCOPE_AGENT);
    }
    stage(7, std::false_type{}, [&] { wait_vm_bar<NP + (FUSED ? 4 : 0)>(); });
    const bool got = !FUSED || __all(fl[0] == tag && fl[1] == tag && fl[2] == tag && fl[3] == tag);
    STMP(5);
    auto chunk0 = [&] {
#pragma unroll
      for (int j = 0; j < 2; j++) glds16c(kvg + (w + 8 * j) * 1024, lane * 16, lds0 + KV_BASE + 2 * CHUNK + (w + 8 * j) * 1024);
      if (w < 5) glds16c(kvg + (w + 16) * 1024, lane * 16, lds0 + KV_BASE + 2 * CHUNK + (w + 16) * 1024);
    };
    u32 kmr[4] = {};
    auto km_load = [&] {
      if (w == 5) {
        const rsrc_t r5 = ws_rsrc(P.ws);
#pragma unroll
        for (int k = 0; k < 4; k++) kmr[k] = ld32c(r5, WS_KM + k * 256 + lane * 4);
      }
    };
    if (got) { chunk0(); km_load(); }
    stage(8, std::false_type{}, [&] { if (FUSED) lds_bar(); else wait_vm_bar<2>(); });
    stage(9, std::false_type{}, [&] { lds_bar(); });
    STMP(6);
    if (FUSED && SA_T > 6 && w == 4 && lane == 0) put_flag(P, FL_SA + wgi * 8, tag);
    if (!got) { spin_flags<256, 1>(P, FL_S1, tag, lane); chunk0(); km_load(); }
    const float qs = 0.22360679774997896f * 1.4426950408889634f;
    float n = 0.f;
#pragma unroll
    for (int j = 0; j < 12; j++) { q[j] *= qs; if (j < 8 || h == 0) n += q[j] * q[j]; }
    qa[0] = u4v{pk2(q[0], q[1]), pk2(q[2], q[3]), pk2(q[4], q[5]), pk2(q[6], q[7])};
    qb[0] = h ? u2v{0u, 0u} : u2v{pk2(q[8], q[9]), pk2(q[10], q[11])};
    qn = n + xor32(n);
    if (NP == 3) {
      char* qx = smem + 1024 + (w & 3) * (7 * 256) + lane * 4;
#pragma unroll
      for (int j = 0; j < 4; j++) *(LAS u32*)(LAS char*)(qx + j * 256) = qa[0][j];
#pragma unroll
      for (int j = 0; j < 2; j++) *(LAS u32*)(LAS char*)(qx + (4 + j) * 256) = qb[0][j];
      *(LAS float*)(LAS char*)(qx + 6 * 256) = qn;
      if (w == 5) {
        const float km0 = nmax(__builtin_bit_cast(float, kmr[0]), __builtin_bit_cast(float, kmr[1]));
        const float km1 = nmax(__builtin_bit_cast(float, kmr[2]), __builtin_bit_cast(float, kmr[3]));
        const float kmx = max64(km0) + max64(km1);
        if (lane == 0) *(LAS float*)(LAS char*)smem = kmx;
      }
    }
  };
  if (w < 4) qproj(std::integral_constant<int, 2>{}); else qproj(std::integral_constant<int, 3>{});
  STMP(1);
  lds_bar();
  STMP(11);
  if (w >= 4) { wait_vm_bar<0>(); STMP(2); kv_loader<FUSED>(P, kvg, lds0, w - 4, lane, wgi, tag STAMP_PASS); return; }
  const rsrc_t rs = ws_rsrc(P.ws);
  const float km = *(const LAS float*)(const LAS char*)smem;
  {
    const char* qx = smem + 1024 + w * (7 * 256) + lane * 4;
#pragma unroll
    for (int j = 0; j < 4; j++) qa[1][j] = *(const LAS u32*)(const LAS char*)(qx + j * 256);
#pragma unroll
    for (int j = 0; j < 2; j++) qb[1][j] = *(const LAS u32*)(const LAS char*)(qx + (4 + j) * 256);
    qn = nmax(qn, *(const LAS float*)(const LAS char*)(qx + 6 * 256));
  }
  const bool fast = __all(qn * km * 1.05f <= kBound2);
#define APRIO 0
  __builtin_amdgcn_s_setprio(APRIO);
  f16v o0 = {}, o1 = {};
  wait_vm_bar<0>();
  STMP(2);
  if (fast) attn_loop_fast(lds0, lane, qa[0], qb[0], qa[1], qb[1], o0, o1);
  else attn_loop_safe(smem, lane, qa[0], qb[0], qa[1], qb[1], o0, o1);
  STMP(3);
  __builtin_amdgcn_s_setprio(3);
#pragma unroll
  for (int i = 0; i < 2; i++) {
    const f16v& o = i ? o1 : o0;
    const float up = xor32(o[8]);
    const float sc = __builtin_amdgcn_rcpf(h ? o[8] : up);
    char* dst = smem + (32 * i + r) * 176 + (20 * w + 4 * h) * 2;
    sts64(dst, u2v{pk2(o[0] * sc, o[1] * sc), pk2(o[2] * sc, o[3] * sc)});
    sts64(dst + 16, u2v{pk2(o[4] * sc, o[5] * sc), pk2(o[6] * sc, o[7] * sc)});
    if (h == 0) sts64(dst + 32, u2v{pk2(o[8] * sc, o[9] * sc), pk2(o[10] * sc, o[11] * sc)});
  }
  lds_bar();
  {
    u4v v[3];
    u32 o[3];
#pragma unroll
    for (int k = 0; k < 3; k++) {
      const int ch = tid + 256 * k < 640 ? tid + 256 * k : tid, row = ch / 10, c = ch % 10;
      v[k] = lds128(smem + row * 176 + c * 16);
      o[k] = WS_O + ((p0 + row) * 640 + 80 * nt + 8 * c) * 2;
    }
    st128c(rs, o[0], v[0]); st128c(rs, o[1], v[1]);
    if (w < 2) st128c(rs, o[2], v[2]);
  }
  if (FUSED) publish(P, FL_S4 + (mt * 8 + nt) * 8, tag_of(epoch));
  STMP(4);
}

DEVI void b1_kv(const Params& P, char* smem, int kg, int part STAMP_ARG) {
  const int tid = fresh_tid(), lane = tid & 63, w = uni(tid >> 6), l15 = lane & 15, g = lane >> 4;
  const u32 lds0 = (u32)(uintptr_t)(LAS char*)smem;
  constexpr int RW = 20480, RG = RW + 25600;
  const char* wsrc = (const char*)(part ? P.wv : P.wk);
  {
    const int row = (w & 1) * 8 + (lane >> 3), ch = (lane & 7) ^ ((row >> 1) & 7);
    const u32 xo = (s_of_p(16 * kg + row) * 640 + ch * 8) * 2;
    glds16((const char*)P.x + (w >> 1) * 128, xo, lds0 + w * 1024);
    glds16((const char*)P.x + ((w + 8) >> 1) * 128, xo, lds0 + (w + 8) * 1024);
    if (w < 4) glds16((const char*)P.x + ((w + 16) >> 1) * 128, xo, lds0 + (w + 16) * 1024);
    else glds16(wsrc + (w - 4) * 1024, lane * 16, lds0 + (w + 16) * 1024);
    glds16(wsrc + (w + 4) * 1024, lane * 16, lds0 + (w + 24) * 1024);
    glds16(wsrc + (w + 12) * 1024, lane * 16, lds0 + (w + 32) * 1024);
    if (w < 5) glds16(wsrc + (w + 20) * 1024, lane * 16, lds0 + (w + 40) * 1024);
    else if (part && w < 7) { const int e = (w - 5) * 512 + lane * 8; glds16(P.gw, (e > 632 ? 632 : e) * 2, lds0 + (w + 40) * 1024); }
  }
  STMP(13);
  wait_vm_bar<0>();
  STMP(14);
  const rsrc_t rs = ws_rsrc(P.ws);
  const int s = kg & 1;
  const u32 tile = WS_KV + (kg >> 1) * KVT;
  if (w < 2 + part) {
    const char* xa = smem + l15 * 128;
    const int sw = (l15 >> 1) & 7;
    const char* aw = smem + RW + (8 * g + (l15 >> 2)) * 40 + (lane & 3) * 8 + w * 32;
    f4v a0 = {}, a1 = {};
    auto run = [&](auto GATE, auto KPART) {
      u4v xf[2][4], wf[2][4];
      auto rd = [&](int b) {
#pragma unroll
        for (int e = 0; e < 4; e++) {
          const int ks = 4 * b + e;
          xf[b & 1][e] = lds128(xa + (ks >> 1) * 2048 + (((4 * (ks & 1) + g) ^ sw) << 4));
          if constexpr (GATE) wf[b & 1][e] = lds128(smem + RG + (32 * ks + 8 * g) * 2);
          else wf[b & 1][e] = cat(ldstr(aw + ks * 32 * 40), ldstr(aw + ks * 32 * 40 + 160));
        }
      };
      rd(0);
#pragma unroll
      for (int b = 0; b < 5; b++) {
        if (b < 4) rd(b + 1);
        __builtin_amdgcn_sched_barrier(0);
#pragma unroll
        for (int e = 0; e < 4; e++) {
          f4v& a = (e & 1) ? a1 : a0;
          a = KPART ? mfma16(wf[b & 1][e], xf[b & 1][e], a) : mfma16(xf[b & 1][e], wf[b & 1][e], a);
        }
        __builtin_amdgcn_sched_barrier(0);
      }
    };
    if (part == 0) run(std::false_type{}, std::true_type{});
    else if (w < 2) run(std::false_type{}, std::false_type{});
    else run(std::true_type{}, std::false_type{});
    const f4v a = a0 + a1;
    STMP(15);
    if (part == 0) {
      const u2v v = {pk2(a[0], a[1]), pk2(a[2], a[3])};
      float n2 = a[0] * a[0] + a[1] * a[1] + a[2] * a[2] + a[3] * a[3];
      if (w == 0) {
        st64c(rs, tile + ((g & 1) * 32 + 16 * s + l15) * 16 + (g >> 1) * 8, v);
        n2 += xor16(n2); n2 += xor32(n2);
      } else if (g == 0) st64c(rs, tile + 1024 + (16 * s + l15) * 8, v);
      n2 = max16(n2);
      if (lane == 0) st32c(rs, WS_KM + w * 512 + kg * 4, __builtin_bit_cast(u32, n2));
    } else if (w < 2) {
      u2v v = {pk2(a[0], a[1]), pk2(a[2], a[3])};
      if (w == 1 && l15 == 4) v = u2v{0x3f803f80u, 0x3f803f80u};
      if (w == 1 && l15 == 5) v = u2v{0u, 0u};
      if (w == 0 || l15 <= 5) st64c(rs, tile + 1280 + (2 * s + (g & 1)) * 352 + (g >> 1) * 8 + (16 * w + l15) * 16, v);
    } else if (l15 == 0) {
      u4v o;
#pragma unroll
      for (int i = 0; i < 4; i++) o[i] = __builtin_bit_cast(u32, 1.f / (1.f + __expf(-a[i])));
      st128c(rs, WS_G + (16 * kg + 4 * g) * 4, o);
    }
  }
}

struct WtUnit { u4v re, im; };
DEVI WtUnit wt_load(const Params& P, int u, int lane) {
  const int k2 = u / 20, f0 = 16 * (u % 20) + 8 * (lane >> 5), row = 32 * k2 + (lane & 31);
  WtUnit t;
  __builtin_memcpy(&t.re, P.wr + row * 321 + f0, 16);
  __builtin_memcpy(&t.im, P.wi + row * 321 + f0, 16);
  return t;
}
DEVI void wt_store(const Params& P, int u, int lane, const WtUnit& t) {
  const int k2 = u / 20, f0 = 16 * (u % 20) + 8 * (lane >> 5);
  const rsrc_t rs = ws_rsrc(P.ws);
  const u32 o = WS_WT + f0 * 8192 + k2 * 128 + (lane & 31) * 4;
#pragma unroll
  for (int j = 0; j < 4; j++) {
    st32c(rs, o + (2 * j) * 8192, (t.re[j] & 0xffffu) | (t.im[j] << 16));
    st32c(rs, o + (2 * j + 1) * 8192, (t.re[j] >> 16) | (t.im[j] & 0xffff0000u));
  }
}

template <bool COH> DEVI void b_piece(const char* base, u32 vo, int adv, u32 lds0, int w, int t) {
  if (COH) glds16c(base + t * adv, vo, lds0 + (t & 7) * 8192 + w * 1024);
  else glds16(base + t * adv, vo, lds0 + (t & 7) * 8192 + w * 1024);
}
template <int KIND> DEVI u4v b_frag(const char* smem, const int (&ao)[4], int n, int mb) {
  if (KIND == 2) { const char* p = smem + ao[mb] + (n & 15) * 4096; return cat(ldstr(p), ldstr(p + 1024)); }
  return lds128(smem + ao[n & 1] + ((n >> 1) & 7) * 8192 + mb * 2048);
}
template <int N, class F> DEVI void forN(F&& f) { if constexpr (N > 0) { forN<N - 1>(f); f(std::integral_constant<int, N - 1>{}); } }
template <class F> DEVI void call0(F& f) { if constexpr (std::is_invocable_v<F&>) f(); }
template <int J, class F> DEVI void call1(F& f) { if constexpr (std::is_invocable_v<F&, std::integral_constant<int, J>>) f(std::integral_constant<int, J>{}); }
constexpr int b_landed(int n, int npre) {
  const int m1 = npre == 20 ? 3 : -1;
  const int la = n < m1 ? 1 : 2, lap = (n - 1) < m1 ? 1 : 2;
  const int need = (n + la) >> 1, had = n == 0 ? 0 : (n - 1 + lap) >> 1;
  return need > had ? need : had;
}
template <int KIND, bool COH, bool MAIN, int NPRE, int NSIDE, class SIDE>
DEVI void b_gemm_w(const char* smem, u4v (&W)[20], const u4v* tab, f4v (&acc)[4], const int (&ao)[4],
                   const char* base, u32 vo, int adv, u32 lds0, int w, SIDE&& side STAMP_ARG) {
  STMP(16 + 4 * KIND);
#pragma unroll
  for (int t = 0; t < 5; t++) b_piece<COH>(base, vo, adv, lds0, w, t);
  STMP(17 + 4 * KIND);
  vm_bar<4>();
  STMP(18 + 4 * KIND);
  constexpr int N1 = (MAIN && NPRE == 20) ? 3 : -1;
  const int kq = w & 3;
  u4v a[2][4];
  if constexpr (MAIN) {
#pragma unroll
    for (int i = 0; i < (NPRE == 20 ? 1 : 2); i++)
#pragma unroll
      for (int mb = 0; mb < 4; mb++) a[i][mb] = b_frag<KIND>(smem, ao, i, mb);
  } else if (kq < 2) {
#pragma unroll
    for (int mb = 0; mb < 4; mb++) a[0][mb] = kq ? b_frag<KIND>(smem, ao, 1, mb) : b_frag<KIND>(smem, ao, 0, mb);
  }
  forN<20>([&](auto NN) {
    constexpr int n = NN, T = n >> 1;
    constexpr int M1 = NPRE == 20 ? 3 : -1;
    constexpr int LA = n < M1 ? 1 : 2, LAP = (n - 1) < M1 ? 1 : 2;
    constexpr int need = (n + LA) >> 1, had = n == 0 ? 0 : (n - 1 + LAP) >> 1;
    if constexpr (n == 10) { STMP(19 + 4 * KIND); }
    if constexpr (MAIN && NPRE == 12 && (n == 2 || n == 6)) {
#pragma unroll
      for (int i = 0; i < 4; i++) W[(n == 2 ? 12 : 16) + i] = tab[((n == 2 ? 12 : 16) + i) * 64];
    }
    if constexpr (!(n & 1) && n < 10) b_piece<COH>(base, vo, adv, lds0, w, 5 + T);
    if constexpr (!MAIN && n == 8) call0(side);
    if constexpr (MAIN && NSIDE >= 0 && n == 12) call0(side);
    if constexpr (MAIN && NSIDE < 0 && n >= 10 && n <= 15) call1<n - 10>(side);
    if constexpr (need > had && need <= 9) {
      constexpr int issued = 5 + (T + 1 < 5 ? T + 1 : 5);
      constexpr int wy = MAIN ? (NPRE == 12 ? ((n >= 2 && need <= 5) ? 4 : 0) + ((n >= 6 && need <= 7) ? 4 : 0) : 0) + (NSIDE >= 0 ? (n >= 12 ? NSIDE : 0) : (n >= 10 ? 2 * ((n < 15 ? n : 15) - 9) : 0)) : (n >= 8 ? NSIDE : 0);
      vm_bar<(issued - need - 1 + wy)>();
    }
    if constexpr (MAIN) {
      constexpr int cur = n <= N1 ? 0 : (n & 1);
#pragma unroll
      for (int mb = 0; mb < 4; mb++) {
        const u4v x = a[cur][mb];
        acc[mb] = KIND == 0 ? mfma16(x, W[n], acc[mb]) : mfma16(W[n], x, acc[mb]);
        if constexpr (n == N1) { a[(n + 1) & 1][mb] = b_frag<KIND>(smem, ao, n + 1, mb); a[n & 1][mb] = b_frag<KIND>(smem, ao, n + 2, mb); }
        else if constexpr (n + LA < 20) a[LA == 1 ? 0 : (n & 1)][mb] = b_frag<KIND>(smem, ao, n + LA, mb);
      }
    } else {
      if ((n & 3) == kq) {
#pragma unroll
        for (int mb = 0; mb < 4; mb++) acc[mb] = KIND == 0 ? mfma16(a[0][mb], W[n >> 2], acc[mb]) : mfma16(W[n >> 2], a[0][mb], acc[mb]);
      }
      constexpr bool now2 = n + 2 < 20 && ((n + 2) >> 1) <= b_landed(n, NPRE);
      constexpr bool late1 = n >= 1 && ((n + 1) >> 1) > b_landed(n - 1, NPRE);
      if constexpr (late1) {
        if (((n + 1) & 3) == kq) {
#pragma unroll
          for (int mb = 0; mb < 4; mb++) a[0][mb] = b_frag<KIND>(smem, ao, n + 1, mb);
        }
      }
      if constexpr (now2) {
        if (((n + 2) & 3) == kq) {
#pragma unroll
          for (int mb = 0; mb < 4; mb++) a[0][mb] = b_frag<KIND>(smem, ao, n + 2, mb);
        }
      }
    }
    __builtin_amdgcn_sched_barrier(0);
  });
}
template <bool MAIN>
DEVI void b_gemm_res(const char* smem, const u4v (&W)[20], f4v (&acc)[4], const int (&ao)[4], int w) {
  auto frag = [&](int n, int mb) { const char* p = smem + ao[mb] + n * 4096; return cat(ldstr(p), ldstr(p + 1024)); };
  if constexpr (MAIN) {
    u4v a[2][4];
#pragma unroll
    for (int mb = 0; mb < 4; mb++) a[0][mb] = frag(0, mb);
    forN<20>([&](auto NN) {
      constexpr int n = NN, cur = n <= 3 ? 0 : (n & 1);
#pragma unroll
      for (int mb = 0; mb < 4; mb++) {
        acc[mb] = mfma16(W[n], a[cur][mb], acc[mb]);
        if constexpr (n < 3) a[0][mb] = frag(n + 1, mb);
        else if constexpr (n == 3) { a[0][mb] = frag(4, mb); a[1][mb] = frag(5, mb); }
        else if constexpr (n + 2 < 20) a[n & 1][mb] = frag(n + 2, mb);
      }
      __builtin_amdgcn_sched_barrier(0);
    });
  } else {
    const int kq = w & 3;
    const char* sm = smem + kq * 4096;
    auto fr = [&](int j, int mb) { const char* p = sm + ao[mb] + j * 16384; return cat(ldstr(p), ldstr(p + 1024)); };
    u4v a[2][4];
#pragma unroll
    for (int mb = 0; mb < 4; mb++) a[0][mb] = fr(0, mb);
#pragma unroll
    for (int j = 0; j < 5; j++) {
      if (j < 4) {
#pragma unroll
        for (int mb = 0; mb < 4; mb++) a[(j + 1) & 1][mb] = fr(j + 1, mb);
      }
#pragma unroll
      for (int mb = 0; mb < 4; mb++) acc[mb] = mfma16(W[j], a[j & 1][mb], acc[mb]);
      __builtin_amdgcn_sched_barrier(0);
    }
  }
}
template <bool MAIN> DEVI f4v b_reduce(char* xa, const f4v (&acc)[4], int w, int lane) {
  lds_bar();
  const int kq = w & 3;
  if constexpr (!MAIN) {
#pragma unroll
    for (int mb = 0; mb < 4; mb++) *(LAS f4v*)(LAS char*)(xa + (kq * 4 + mb) * 1024 + lane * 16) = acc[mb];
  }
  lds_bar();
  f4v r = {};
  if constexpr (!MAIN) {
#pragma unroll
    for (int q = 0; q < 4; q++) r += *(const LAS f4v*)(const LAS char*)(xa + (q * 4 + kq) * 1024 + lane * 16);
  }
  return r;
}
DEVI u32 rows_off(int p0, bool perm, int w, int lane) {
  const int row = w * 8 + (lane >> 3), ch = (lane & 7) ^ ((row >> 1) & 7);
  return ((perm ? s_of_p(p0 + row) : p0 + row) * 640 + ch * 8) * 2;
}
DEVI void mk_ro(int (&ao)[4], int l15, int g) {
  ao[0] = l15 * 128 + ((g ^ ((l15 >> 1) & 7)) << 4);
  ao[1] = l15 * 128 + (((4 + g) ^ ((l15 >> 1) & 7)) << 4);
  ao[2] = ao[3] = 0;
}

DEVI void b2_fwd(const Params& P, char* smem, int mt, int nt, int i STAMP_ARG) {
  const int tid = fresh_tid(), lane = tid & 63, w = uni(tid >> 6), l15 = lane & 15, g = lane >> 4;
  const u32 lds0 = (u32)(uintptr_t)(LAS char*)smem;
  const u32 vo = rows_off(mt * 64, true, w, lane);
  f4v acc[4] = {};
  u4v W[20];
  int ao[4];
  mk_ro(ao, l15, g);
  char* stg = smem + 16384;
  auto put = [&](int blk, int mb, const f4v& v) {
    sts64(stg + blk * 2304 + l15 * 144 + (16 * mb + 4 * g) * 2, u2v{pk2(v[0], v[1]), pk2(v[2], v[3])});
  };
  if (w < 4) {
    const u4v* tf = (const u4v*)&gTF + (nt * 5 + w) * 1280 + lane;
#pragma unroll
    for (int k = 0; k < 12; k++) W[k] = tf[k * 64];
    b_gemm_w<0, false, true, 12, 0>(smem, W, tf, acc, ao, (const char*)P.x, vo, 128, lds0, w, [] {} STAMP_PASS);
    STMP(11);
    b_reduce<true>(smem, acc, w, lane);
#pragma unroll
    for (int mb = 0; mb < 4; mb++) put(w, mb, acc[mb]);
    lds_bar();
  } else {
    const u4v* tf = (const u4v*)&gTF + (nt * 5 + 4) * 1280 + (w & 3) * 64 + lane;
#pragma unroll
    for (int k = 0; k < 5; k++) W[k] = tf[k * 256];
    const int u0 = 5 * i + 2 * (w > 4 ? w - 5 : 0), u1 = w < 7 ? u0 + 1 : u0;
    WtUnit t0, t1;
    u32 ny = 0;
    b_gemm_w<0, false, false, 12, 5>(smem, W, nullptr, acc, ao, (const char*)P.x, vo, 128, lds0, w, [&] {
      t0 = wt_load(P, u0, lane);
      t1 = wt_load(P, u1, lane);
      const int row = 32 * (i & 63) + (lane & 31);
      ny = (lane >> 5) ? P.wi[row * 321 + 320] : P.wr[row * 321 + 320];
    } STAMP_PASS);
    const f4v r = b_reduce<false>(smem, acc, w, lane);
    put(4, w & 3, r);
    lds_bar();
    if (w > 4) {
      wt_store(P, u0, lane, t0);
      if (w < 7) wt_store(P, u1, lane, t1);
      else if (i < 64) {
        const u32 o = __shfl_xor(ny, 32);
        if (lane < 32) st32c(ws_rsrc(P.ws), WS_WT + 320 * 8192 + i * 128 + lane * 4, ny | (o << 16));
      }
    }
  }
  if (w < 5) {
    const rsrc_t rs = ws_rsrc(P.ws);
#pragma unroll
    for (int k = 0; k < 2; k++) {
      const int row = 8 * k + (lane >> 3), c = lane & 7;
      st128c(rs, WS_X1 + ((80 * nt + 16 * w + row) * 2048 + mt * 64 + 8 * c) * 2, lds128(stg + w * 2304 + row * 144 + c * 16));
    }
  }
}

constexpr int CT_A1 = 0, CT_A1C = 4096, CT_A2 = 8192, CT_TW1 = 24576, CT_TW2 = 40960, CT_XCH = 57344;
static_assert(sizeof(ColTabD) == CT_XCH, "table image");
DEVI u4v pk8(const f16v& a, int s) {
  u4v v = {pk2(a[8 * s], a[8 * s + 1]), pk2(a[8 * s + 2], a[8 * s + 3]), pk2(a[8 * s + 4], a[8 * s + 5]), pk2(a[8 * s + 6], a[8 * s + 7])};
  return v;
}
DEVI u4v neg8(u4v v) { return v ^ u4v{0x80008000u, 0x80008000u, 0x80008000u, 0x80008000u}; }
DEVI void column_tables(char* smem) {
  const int tid = fresh_tid(), lane = tid & 63, w = uni(tid >> 6);
  const u32 lds0 = (u32)(uintptr_t)(LAS char*)smem;
#pragma unroll
  for (int j = 0; j < 7; j++) glds16((const char*)&gCT + (w + 8 * j) * 1024, lane * 16, lds0 + (w + 8 * j) * 1024);
}
DEVI f2v lo2(f4v c) { return f2v{c[0], c[1]}; }
DEVI f2v hi2(f4v c) { return f2v{c[2], c[3]}; }
template <int SG> DEVI void twiddle(f16v& re, f16v& im, const char* tab) {
#pragma unroll
  for (int hq = 0; hq < 2; hq++) {
    f4v c[4];
#pragma unroll
    for (int q = 0; q < 4; q++) c[q] = __builtin_bit_cast(f4v, lds128(tab + (4 * hq + q) * 1024));
#pragma unroll
    for (int q = 0; q < 4; q++) {
      const int k = 2 * (4 * hq + q);
      const f2v a = {re[k], re[k + 1]}, b = {im[k], im[k + 1]}, cc = lo2(c[q]), ss = hi2(c[q]);
      const f2v nr = SG > 0 ? a * cc - b * ss : a * cc + b * ss;
      const f2v ni = SG > 0 ? b * cc + a * ss : b * cc - a * ss;
      re[k] = nr[0]; re[k + 1] = nr[1]; im[k] = ni[0]; im[k + 1] = ni[1];
    }
  }
}
DEVI void column_job(const Params& P, char* smem, int col, int f, int t, int lane) {
  const int r = lane & 31, h = lane >> 5;
  const rsrc_t rs = ws_rsrc(P.ws);
  const int cr = f == 0 ? 0 : f == 320 ? 1 : 2 * f;
  const bool real_in = (f == 0 || f == 320);
  const u32 zo = WS_X1 + (cr * 2048 + (32 * t + r) * 32 + 8 * h) * 2;
  u4v Zr[2], Zi[2];
#pragma unroll
  for (int ks = 0; ks < 2; ks++) {
    Zr[ks] = ld128c(rs, zo + 32 * ks);
    Zi[ks] = ld128c(rs, zo + 4096 + 32 * ks);
  }
  u32 wv[16];
#pragma unroll
  for (int i = 0; i < 16; i++) wv[i] = ld32c(rs, WS_WT + f * 8192 + (32 * t + (i & 3) + 8 * (i >> 2) + 4 * h) * 128 + r * 4);
  if (real_in) { Zi[0] = u4v{0u, 0u, 0u, 0u}; Zi[1] = Zi[0]; }
  const char* tb = smem + lane * 16;
  char* xch = smem + CT_XCH + col * 8192 + lane * 16;
  f16v re = {}, im = {};
#pragma unroll
  for (int ks = 0; ks < 2; ks++) {
    u4v c = lds128(tb + CT_A1 + ks * 1024), ns = lds128(tb + CT_A1 + 2048 + ks * 1024);
    re = mfma32(Zr[ks], c, re); re = mfma32(neg8(Zi[ks]), ns, re);
    im = mfma32(Zr[ks], ns, im); im = mfma32(Zi[ks], c, im);
  }
  twiddle<-1>(re, im, tb + CT_TW1 + t * 8192);
  u4v yr[4], yi[4];
  auto exchange = [&]() {
    yr[0] = pk8(re, 0); yr[1] = pk8(re, 1); yi[0] = pk8(im, 0); yi[1] = pk8(im, 1);
    char* mine = xch + t * 4096;
    sts128(mine, yr[0]); sts128(mine + 1024, yr[1]); sts128(mine + 2048, yi[0]); sts128(mine + 3072, yi[1]);
    lds_bar();
    const char* oth = xch + (t ^ 1) * 4096;
    yr[2] = lds128(oth); yr[3] = lds128(oth + 1024); yi[2] = lds128(oth + 2048); yi[3] = lds128(oth + 3072);
  };
  const char* a2 = tb + CT_A2 + t * 8192;
  const int ko = t * 4096;
  exchange();
  re = f16v{}; im = f16v{};
#pragma unroll
  for (int kk = 0; kk < 4; kk++) {
    u4v c = lds128(a2 + ((kk * 2048) ^ ko)), ns = lds128(a2 + ((kk * 2048) ^ ko) + 1024);
    re = mfma32(c, yr[kk], re); re = mfma32(ns, neg8(yi[kk]), re);
    im = mfma32(ns, yr[kk], im); im = mfma32(c, yi[kk], im);
  }
  const float wsc = real_in ? 0.5f : 1.f;
#pragma unroll
  for (int i = 0; i < 16; i += 2) {
    const f2v wr_ = f2v{bflo(wv[i]), bflo(wv[i + 1])} * wsc, wi_ = f2v{bfhi(wv[i]), bfhi(wv[i + 1])} * wsc;
    const f2v a = {re[i], re[i + 1]}, b = {im[i], im[i + 1]};
    const f2v nr = a * wr_ - b * wi_, ni = a * wi_ + b * wr_;
    re[i] = nr[0]; re[i + 1] = nr[1]; im[i] = ni[0]; im[i + 1] = ni[1];
  }
  lds_bar();
  exchange();
  re = f16v{}; im = f16v{};
#pragma unroll
  for (int kk = 0; kk < 4; kk++) {
    u4v c = lds128(a2 + ((kk * 2048) ^ ko)), ns = lds128(a2 + ((kk * 2048) ^ ko) + 1024);
    re = mfma32(yr[kk], c, re); re = mfma32(yi[kk], ns, re);
    im = mfma32(neg8(yr[kk]), ns, im); im = mfma32(yi[kk], c, im);
  }
  twiddle<1>(re, im, tb + CT_TW2 + t * 8192);
  u4v gr0 = pk8(re, 0), gr1 = pk8(re, 1), gi0 = pk8(im, 0), gi1 = pk8(im, 1);
  re = f16v{}; im = f16v{};
#pragma unroll
  for (int ks = 0; ks < 2; ks++) {
    u4v c = lds128(tb + CT_A1C + ks * 1024), ns = lds128(tb + CT_A1C + 2048 + ks * 1024);
    u4v gr = ks ? gr1 : gr0, gi = ks ? gi1 : gi0;
    re = mfma32(c, gr, re); re = mfma32(ns, gi, re);
    im = mfma32(ns, neg8(gr), im); im = mfma32(c, gi, im);
  }
  const u32 dst = WS_X2 + (cr * 2048 + (32 * t + r) * 32 + 8 * h) * 2;
#pragma unroll
  for (int g = 0; g < 4; g += 2) {
    st128c(rs, dst + 16 * g, swap_pack(pk2(re[4 * g], re[4 * g + 1]), pk2(re[4 * g + 2], re[4 * g + 3]),
                                       pk2(re[4 * g + 4], re[4 * g + 5]), pk2(re[4 * g + 6], re[4 * g + 7])));
    u4v vi = swap_pack(pk2(im[4 * g], im[4 * g + 1]), pk2(im[4 * g + 2], im[4 * g + 3]),
                       pk2(im[4 * g + 4], im[4 * g + 5]), pk2(im[4 * g + 6], im[4 * g + 7]));
    if (!real_in) st128c(rs, dst + 4096 + 16 * g, vi);
  }
}
DEVI void wot_job(const Params& P, char* img, int fr, int lane) {
  const int cb = fr / 20, ks = fr % 20;
  glds16((const char*)P.wo + (32 * ks * 640 + 16 * cb) * 2, (lane >> 1) * 1280 + (lane & 1) * 16, (u32)(uintptr_t)(LAS char*)img);
  wait_vm<0>();
  const char* a = img + (8 * (lane >> 4) + ((lane & 15) >> 2)) * 32 + (lane & 3) * 8;
  st128c(ws_rsrc(P.ws), WS_WOT + fr * 1024 + lane * 16, cat(ldstr(a), ldstr(a + 128)));
}
template <bool FUSED>
DEVI void b3_columns(const Params& P, char* smem, int i, u64 tag STAMP_ARG) {
  lds_bar();
  column_tables(smem);
  if (FUSED) wait_flags<256>(P, FL_S2, tag); else wait_vm_bar<0>();
  STMP(12);
  const int tid = fresh_tid(), w = uni(tid >> 6), lane = tid & 63;
  const int f = (w >> 1) == 0 ? i : 256 + i;
  if (w < 4 && f <= 320) column_job(P, smem, w >> 1, f, w & 1, lane);
  else {
    if (w >= 4 && i + 256 * (w - 4) < 800) wot_job(P, smem + CT_XCH + 16384 + (w - 4) * 1024, i + 256 * (w - 4), lane);
    lds_bar(); lds_bar(); lds_bar();
  }
}

template <bool FUSED>
DEVI void b45_out(const Params& P, char* smem, int mt, int nt, int wgi, u64 tag STAMP_ARG) {
  const u32 lds0 = (u32)(uintptr_t)(LAS char*)smem;
  const int p0 = mt * 64, n0 = nt * 80;
  constexpr int STG = 65536;
  const int tid = fresh_tid(), lane = tid & 63, w = uni(tid >> 6), l15 = lane & 15, g = lane >> 4;
  auto synth_off = [&] { const int o = w * 1024 + lane * 16, rho = o >> 7, s16 = (o >> 4) & 7, mbs = (s16 >> 1) ^ ((rho >> 1) & 3);
                         return (u32)((swap23(rho) * 2048 + p0 + mbs * 16 + (s16 & 1) * 8) * 2); };
  auto fetch_b = [&] {
    if (FUSED) {
      wait_flags<256>(P, FL_S3, tag);
    } else lds_bar();
    const u32 vo = synth_off();
#pragma unroll
    for (int t = 0; t < 10; t++) glds16c(P.ws + WS_X2 + t * (64 * 4096), vo, lds0 + t * 8192 + w * 1024);
    if (FUSED) {
      wait_flags<256>(P, FL_S1, tag);
      wait_flags<1>(P, FL_SB + wgi * 8, tag);
    }
    wait_vm_bar<0>();
  };
  auto mk_co = [&](int (&ao)[4]) {
    const int rho0 = 16 * (g >> 1) + 4 * (g & 1) + (l15 >> 2);
#pragma unroll
    for (int mb = 0; mb < 4; mb++) ao[mb] = rho0 * 128 + ((mb ^ ((rho0 >> 1) & 3)) << 5) + (lane & 3) * 8;
  };
  const float sb = 2.f / (float)(S_ * D_);
  const int blk = w < 4 ? w : 4;
  auto stg = [&](int mb) { return smem + STG + (16 * mb + l15) * 176 + (16 * blk + 4 * g) * 2; };
  auto park = [&](int mb, const f4v& v) { sts64(stg(mb), u2v{pk2(v[0] * sb, v[1] * sb), pk2(v[2] * sb, v[3] * sb)}); };
  auto mix = [&](int mb, const f4v& v, float gg) {
    const float og = 1.f - gg;
    const u2v b = lds64(stg(mb));
    sts64(stg(mb), u2v{pk2(gg * v[0] + og * bflo(b[0]), gg * v[1] + og * bfhi(b[0])), pk2(gg * v[2] + og * bflo(b[1]), gg * v[3] + og * bfhi(b[1]))});
  };
  const rsrc_t rs = ws_rsrc(P.ws);
  if (w < 4) {
    u4v W[20], WO[20];
    f4v accb[4] = {}, acca[4] = {};
    int ao[4];
    mk_co(ao);
    const u4v* ti = (const u4v*)&gTI + (nt * 5 + w) * 1280 + lane;
#pragma unroll
    for (int i = 0; i < 20; i++) W[i] = ti[i * 64];
    fetch_b();
    STMP(4);
    __builtin_amdgcn_s_waitcnt(0x0F70);
    b_gemm_res<true>(smem, W, accb, ao, w);
    STMP(27);
    lds_bar();
#pragma unroll
    for (int mb = 0; mb < 4; mb++) park(mb, accb[mb]);
    const u32 woff = WS_WOT + (nt * 5 + w) * 20480 + lane * 16;
#pragma unroll
    for (int i = 0; i < 20; i++) WO[i] = ld128c(rs, woff + i * 1024);
    lds_bar();
    STMP(5);
    mk_ro(ao, l15, g);
    const u32 vo = rows_off(p0, false, w, lane);
    if (FUSED) wait_flags<8, 1>(P, FL_S4 + mt * 64, tag); else lds_bar();
    STMP(7);
    __builtin_amdgcn_s_waitcnt(0x0F70);
    float gate[4];
    b_gemm_w<1, true, true, 20, 4>(smem, WO, nullptr, acca, ao, P.ws + WS_O, vo, 128, lds0, w, [&] {
#pragma unroll
      for (int mb = 0; mb < 4; mb++) gate[mb] = __builtin_bit_cast(float, ld32c(rs, WS_G + (p0 + 16 * mb + l15) * 4));
    } STAMP_PASS);
#pragma unroll
    for (int mb = 0; mb < 4; mb++) mix(mb, acca[mb], gate[mb]);
    b_reduce<true>(smem, acca, w, lane);
  } else {
    const int kq = w & 3;
    u4v W[20];
    f4v acc[4] = {};
    int ao[4];
    mk_co(ao);
    const u4v* ti = (const u4v*)&gTI + (nt * 5 + 4) * 1280 + kq * 64 + lane;
#pragma unroll
    for (int i = 0; i < 5; i++) W[i] = ti[i * 256];
    fetch_b();
    __builtin_amdgcn_s_waitcnt(0x0F70);
    b_gemm_res<false>(smem, W, acc, ao, w);
    const u32 woff = WS_WOT + (nt * 5 + 4) * 20480 + kq * 1024 + lane * 16;
#pragma unroll
    for (int i = 0; i < 5; i++) W[i] = ld128c(rs, woff + i * 4096);
    const float gate = __builtin_bit_cast(float, ld32c(rs, WS_G + (p0 + 16 * kq + l15) * 4));
    park(kq, b_reduce<false>(smem, acc, w, lane));
#pragma unroll
    for (int mb = 0; mb < 4; mb++) acc[mb] = f4v{};
    mk_ro(ao, l15, g);
    const u32 vo = rows_off(p0, false, w, lane);
    if (FUSED) wait_flags<8, 1>(P, FL_S4 + mt * 64, tag); else lds_bar();
    __builtin_amdgcn_s_waitcnt(0x0F70);
    b_gemm_w<1, true, false, 20, 0>(smem, W, nullptr, acc, ao, P.ws + WS_O, vo, 128, lds0, w, [] {} STAMP_PASS);
    mix(kq, b_reduce<false>(smem, acc, w, lane), gate);
  }
  lds_bar();
  {
    const int t2 = fresh_tid();
    u4v v[2];
    u16* d[2];
#pragma unroll
    for (int k = 0; k < 2; k++) {
      const int ch = t2 + NTHR * k < 640 ? t2 + NTHR * k : t2, row = ch / 10, c = ch % 10;
      v[k] = lds128(smem + STG + row * 176 + c * 16);
      d[k] = P.out + s_of_p(p0 + row) * 640 + n0 + 8 * c;
    }
    *(u4v*)d[0] = v[0];
    if (t2 < 128) *(u4v*)d[1] = v[1];
  }
}

template <int PH>
__global__ void __launch_bounds__(NTHR, 4) mega(Params P) {
  extern __shared__ __attribute__((aligned(16))) char smem[];
  STAMP_DECL()
  asm volatile("" ::"s"(P.x), "s"(P.wq), "s"(P.wk), "s"(P.wv), "s"(P.wo), "s"(P.wr), "s"(P.wi), "s"(P.gw), "s"(P.out), "s"(P.ws));
  constexpr bool FUSED = PH == 0;
  const int i = blockIdx.x & 255, xcd = i & 7, idx = i >> 3;
  const int mt = (xcd >> 1) * 8 + (idx >> 2), nt = (xcd & 1) * 4 + (idx & 3);
  if (blockIdx.x >= 256) {
    if (PH == 0 || PH == 2) a_wg<FUSED>(P, smem, mt, nt, i STAMP_PASS);
    STAMP_END(P, PH);
    return;
  }
#define BPRIO1 3
  __builtin_amdgcn_s_setprio(BPRIO1);
  u64 tag = 0, epoch = 0;
  if (FUSED) epoch = read_epoch(P);
  if (PH == 0 || PH == 1) {
    b1_kv(P, smem, i >> 1, i & 1 STAMP_PASS);
    STMP(8);
    if (FUSED) tag = tag_of(epoch);
    if (FUSED) publish(P, FL_S1 + i * 8, tag); else lds_bar();
    STMP(1);
#define BPRIO 1
#define BPRIO2 3
#define BPRIO4 2
    __builtin_amdgcn_s_setprio(BPRIO2);
    if (FUSED) wait_flags<1>(P, FL_SA + i * 8, tag);
    STMP(9);
    b2_fwd(P, smem, mt, nt, i STAMP_PASS);
    if (FUSED) publish(P, FL_S2 + i * 8, tag);
    STMP(2);
  }
  __builtin_amdgcn_s_setprio(BPRIO);
  if (PH == 0 || PH == 2) {
    b3_columns<FUSED>(P, smem, i, tag STAMP_PASS);
    if (FUSED) publish(P, FL_S3 + i * 8, tag);
    STMP(3);
  }
  __builtin_amdgcn_s_setprio(BPRIO4);
  if (FUSED && i == 0 && uni(threadIdx.x >> 6) == 7) {
    spin_flags<256>(P, FL_SA, tag, threadIdx.x & 63);
    if ((threadIdx.x & 63) == 0) put_flag(P, FL_E, tag);
  }
  if (PH == 0 || PH == 3) b45_out<FUSED>(P, smem, mt, nt, i, tag STAMP_PASS);
  STAMP_END(P, PH);
}

extern "C" void kernel_launch(void* const* d_in, const int* in_sizes, int n_in, void* d_out, int out_size, void* d_ws,
                              size_t ws_size, hipStream_t stream) {
  Params P;
  P.x = (const u16*)d_in[0];
  P.wq = (const u16*)d_in[1];
  P.wk = (const u16*)d_in[2];
  P.wv = (const u16*)d_in[3];
  P.wo = (const u16*)d_in[4];
  P.wr = (const u16*)d_in[6];
  P.wi = (const u16*)d_in[7];
  P.gw = (const u16*)d_in[8];
  P.out = (u16*)d_out;
  P.ws = (char*)d_ws;
  (void)hipFuncSetAttribute((const void*)mega<0>, hipFuncAttributeMaxDynamicSharedMemorySize, LDS_BYTES);
  mega<0><<<512, NTHR, LDS_BYTES, stream>>>(P);
}
```

```cpp
#include <hip/hip_runtime.h>
#include <stdint.h>
#include <type_traits>
#include <utility>

#define MODE_SPLIT 0

typedef unsigned short u16;
typedef unsigned int u32;
typedef unsigned long long u64;
typedef __attribute__((ext_vector_type(8))) __bf16 bf8;
typedef __attribute__((ext_vector_type(4))) short s4;
typedef __attribute__((ext_vector_type(16))) float f16v;
typedef __attribute__((ext_vector_type(4))) float f4v;
typedef __attribute__((ext_vector_type(2))) float f2v;
typedef __attribute__((ext_vector_type(2))) __bf16 bf2;
typedef __attribute__((ext_vector_type(4))) u32 u4v;
typedef __attribute__((ext_vector_type(2))) u32 u2v;
#define LAS __attribute__((address_space(3)))
#define GAS __attribute__((address_space(1)))
#define DEVI __device__ __forceinline__

constexpr int S_ = 2048, D_ = 640;
constexpr int NTHR = 512, LDS_BYTES = 81920;

constexpr u32 WS_X1 = 0x000000;
constexpr u32 WS_X2 = 0x280000;
constexpr u32 WS_O = 0x500000;
constexpr u32 WS_WT = 0x780000;
constexpr u32 WS_KV = 0xA10000;
constexpr u32 WS_KM = 0xA3A000;
constexpr u32 WS_G = 0xA3B000;
constexpr u32 WS_FL = 0xA40000;
constexpr u32 WS_WOT = 0xA50000;
constexpr u32 FL_E = 0, FL_S1 = 0x800, FL_S2 = 0x1000, FL_S3 = 0x1800, FL_S4 = 0x2000, FL_SA = 0x2800, FL_SB = 0x3000;
constexpr int KVT = 2688;

constexpr double kPi = 3.14159265358979323846;
constexpr double cx_sin(double x) { double t = x, s = x; for (int i = 1; i < 10; i++) { t *= -x * x / ((2 * i) * (2 * i + 1)); s += t; } return s; }
constexpr double cx_cos(double x) { double t = 1, s = 1; for (int i = 1; i < 10; i++) { t *= -x * x / ((2 * i - 1) * (2 * i)); s += t; } return s; }
constexpr double cos2pi(int m, int N) {
  m %= N; if (m < 0) m += N;
  if (2 * m > N) m = N - m;
  if (4 * m > N) return -cos2pi(N / 2 - m, N);
  if (8 * m > N) return cx_sin(2 * kPi * (N / 4 - m) / N);
  return cx_cos(2 * kPi * m / N);
}
constexpr double sin2pi(int m, int N) { return cos2pi(m + 3 * (N / 4), N); }
constexpr u16 to_bf16(double d) { float f = (float)d; u32 u = __builtin_bit_cast(u32, f); u += 0x7fffu + ((u >> 16) & 1); return (u16)(u >> 16); }
constexpr int crow_c(int r, int h) { return (r & 3) + 8 * (r >> 2) + 4 * h; }
constexpr int kperm_c(int ks, int h, int j) { return 16 * ks + 8 * (j >> 2) + 4 * h + (j & 3); }

struct FragB { u16 v[20][64][8]; };
struct FragAll { FragB c[40]; };
struct A1T { u16 a1[2][2][64][8]; u16 a1c[2][2][64][8]; };
struct A2T { u16 v[4][2][64][8]; };
struct TWT { float v[8][64][4]; };
struct ColTabD { A1T a1; A2T a2[2]; TWT tw1[2]; TWT tw2[2]; };

#if defined(__HIP_DEVICE_COMPILE__) && __HIP_DEVICE_COMPILE__
struct Base640 { u16 c[640]; u16 ns[640]; };
constexpr Base640 mk_base() { Base640 b{}; for (int i = 0; i < 640; i++) { b.c[i] = to_bf16(cos2pi(i, 640)); b.ns[i] = to_bf16(-sin2pi(i, 640)); } return b; }
constexpr Base640 kBase = mk_base();
constexpr u16 Fval(int d, int comp) {
  return comp == 0 ? kBase.c[0] : comp == 1 ? kBase.c[(320 * d) % 640] : (comp & 1) ? kBase.ns[((comp >> 1) * d) % 640] : kBase.c[((comp >> 1) * d) % 640];
}
constexpr FragB mk_frag(int cb, int inv) {
  FragB r{};
  for (int ks = 0; ks < 20; ks++) for (int l = 0; l < 64; l++) for (int j = 0; j < 8; j++) {
    int n = 16 * cb + (l & 15), k = 32 * ks + 8 * (l >> 4) + j;
    r.v[ks][l][j] = inv ? Fval(n, k) : Fval(k, n);
  }
  return r;
}
template <int C, int I> constexpr FragB cFrag = mk_frag(C, I);
template <int INV, int... I> constexpr FragAll mk_fragall(std::integer_sequence<int, I...>) { return FragAll{{cFrag<I, INV>...}}; }
__device__ const FragAll gTF = mk_fragall<0>(std::make_integer_sequence<int, 40>{});
__device__ const FragAll gTI = mk_fragall<1>(std::make_integer_sequence<int, 40>{});

struct Trig2048 { double c[2048]; };
constexpr Trig2048 mk_trig() { Trig2048 t{}; for (int i = 0; i < 2048; i++) t.c[i] = cos2pi(i, 2048); return t; }
constexpr Trig2048 kTrig = mk_trig();
constexpr double C2K(int m) { return kTrig.c[m & 2047]; }
constexpr double S2K(int m) { return kTrig.c[(m + 1536) & 2047]; }
constexpr A1T mk_a1() {
  A1T t{};
  for (int ks = 0; ks < 2; ks++) for (int l = 0; l < 64; l++) for (int j = 0; j < 8; j++) {
    int h = l >> 5, r = l & 31;
    int n1 = 16 * ks + 8 * h + j;
    double c = C2K(64 * n1 * r), s = S2K(64 * n1 * r);
    t.a1[0][ks][l][j] = to_bf16(c); t.a1[1][ks][l][j] = to_bf16(-s);
    int k1 = kperm_c(ks, h, j);
    c = C2K(64 * k1 * r); s = S2K(64 * k1 * r);
    t.a1c[0][ks][l][j] = to_bf16(c); t.a1c[1][ks][l][j] = to_bf16(-s);
  }
  return t;
}
constexpr A2T mk_a2(int u) {
  A2T t{};
  for (int kk = 0; kk < 4; kk++) for (int l = 0; l < 64; l++) for (int j = 0; j < 8; j++) {
    int h = l >> 5, row = 32 * u + (l & 31), col = 32 * (kk >> 1) + kperm_c(kk & 1, h, j);
    double c = C2K(32 * row * col), s = S2K(32 * row * col);
    t.v[kk][0][l][j] = to_bf16(c); t.v[kk][1][l][j] = to_bf16(-s);
  }
  return t;
}
constexpr TWT mk_tw(int t, int inv) {
  TWT o{};
  for (int q = 0; q < 8; q++) for (int l = 0; l < 64; l++) for (int e = 0; e < 2; e++) {
    int h = l >> 5, r = 2 * q + e;
    int n2 = inv ? 32 * t + (l & 31) : 32 * t + crow_c(r, h);
    int k1 = inv ? crow_c(r, h) : (l & 31);
    o.v[q][l][e] = (float)C2K(n2 * k1); o.v[q][l][2 + e] = (float)S2K(n2 * k1);
  }
  return o;
}
constexpr A1T cA1 = mk_a1();
template <int U> constexpr A2T cA2 = mk_a2(U);
template <int T, int I> constexpr TWT cTW = mk_tw(T, I);
__device__ const ColTabD gCT = {cA1, {cA2<0>, cA2<1>}, {cTW<0, 0>, cTW<1, 0>}, {cTW<0, 1>, cTW<1, 1>}};
#else
extern __device__ const FragAll gTF;
extern __device__ const FragAll gTI;
extern __device__ const ColTabD gCT;
#endif

DEVI f16v mfma32(u4v a, u4v b, f16v c) { return __builtin_amdgcn_mfma_f32_32x32x16_bf16(__builtin_bit_cast(bf8, a), __builtin_bit_cast(bf8, b), c, 0, 0, 0); }
DEVI f16v mfma32k8(u2v a, u2v b, f16v c) { return __builtin_amdgcn_mfma_f32_32x32x8bf16_1k(__builtin_bit_cast(s4, a), __builtin_bit_cast(s4, b), c, 0, 0, 0); }
DEVI f4v mfma16(u4v a, u4v b, f4v c) { return __builtin_amdgcn_mfma_f32_16x16x32_bf16(__builtin_bit_cast(bf8, a), __builtin_bit_cast(bf8, b), c, 0, 0, 0); }
DEVI u32 pk2(float a, float b) { f2v v = {a, b}; return __builtin_bit_cast(u32, __builtin_convertvector(v, bf2)); }
DEVI float bflo(u32 w) { return __builtin_bit_cast(float, w << 16); }
DEVI float bfhi(u32 w) { return __builtin_bit_cast(float, w & 0xffff0000u); }
DEVI u4v lds128(const char* p) { return *(const LAS u4v*)(const LAS char*)p; }
DEVI u2v lds64(const char* p) { return *(const LAS u2v*)(const LAS char*)p; }
DEVI void sts128(char* p, u4v v) { *(LAS u4v*)(LAS char*)p = v; }
DEVI void sts64(char* p, u2v v) { *(LAS u2v*)(LAS char*)p = v; }
DEVI u2v ldstr(const char* p) { return __builtin_bit_cast(u2v, __builtin_amdgcn_ds_read_tr16_b64_v4i16((LAS s4*)(LAS char*)p)); }
DEVI u4v cat(u2v a, u2v b) { u4v r = {a.x, a.y, b.x, b.y}; return r; }
DEVI void glds16(const void* base, u32 voff, u32 lds) {
  u32 sv;
  asm volatile("s_mov_b32 %0, m0\n\ts_mov_b32 m0, %3\n\ts_nop 0\n\tglobal_load_lds_dwordx4 %1, %2\n\ts_mov_b32 m0, %0" : "=&s"(sv) : "v"(voff), "s"(base), "s"(lds) : "memory");
}
DEVI void glds16c(const void* base, u32 voff, u32 lds) {
  u32 sv;
  asm volatile("s_mov_b32 %0, m0\n\ts_mov_b32 m0, %3\n\ts_nop 0\n\tglobal_load_lds_dwordx4 %1, %2 sc1\n\ts_mov_b32 m0, %0" : "=&s"(sv) : "v"(voff), "s"(base), "s"(lds) : "memory");
}
template <int N> DEVI void wait_vm() { asm volatile("s_waitcnt vmcnt(%0)" ::"i"(N) : "memory"); }
template <int N> DEVI void wait_vm_bar() { asm volatile("s_waitcnt vmcnt(%0) lgkmcnt(0)\n\ts_barrier" ::"i"(N) : "memory"); }
template <int N> DEVI void vm_bar() { asm volatile("s_waitcnt vmcnt(%0)\n\ts_barrier" ::"i"(N) : "memory"); }
DEVI void raw_bar() { asm volatile("s_barrier" ::: "memory"); }
DEVI void lds_bar() { asm volatile("s_waitcnt lgkmcnt(0)\n\ts_barrier" ::: "memory"); }
DEVI u32 uni(u32 v) { return __builtin_amdgcn_readfirstlane(v); }
DEVI int fresh_tid() { int t = threadIdx.x; asm volatile("" : "+v"(t)); return t; }
DEVI u4v swap_pack(u32 a0, u32 a1, u32 b0, u32 b1) {
  auto r0 = __builtin_amdgcn_permlane32_swap(a0, b0, false, false);
  auto r1 = __builtin_amdgcn_permlane32_swap(a1, b1, false, false);
  u4v o = {r0[0], r1[0], r0[1], r1[1]};
  return o;
}
template <int CTRL> DEVI float dpp(float v) { return __builtin_bit_cast(float, __builtin_amdgcn_update_dpp(0, __builtin_bit_cast(int, v), CTRL, 0xf, 0xf, true)); }
DEVI float xor16(float v) { auto r = __builtin_amdgcn_permlane16_swap(__builtin_bit_cast(u32, v), __builtin_bit_cast(u32, v), false, false); return __builtin_bit_cast(float, (threadIdx.x & 16) ? r[0] : r[1]); }
DEVI float xor32(float v) { auto r = __builtin_amdgcn_permlane32_swap(__builtin_bit_cast(u32, v), __builtin_bit_cast(u32, v), false, false); return __builtin_bit_cast(float, (threadIdx.x & 32) ? r[0] : r[1]); }
DEVI float nmax(float a, float b) { return (a > b || a != a) ? a : b; }
DEVI float max16(float v) {
  v = nmax(v, dpp<0xB1>(v)); v = nmax(v, dpp<0x4E>(v)); v = nmax(v, dpp<0x141>(v)); return nmax(v, dpp<0x140>(v));
}
DEVI float max64(float v) { v = max16(v); v = nmax(v, xor16(v)); return nmax(v, xor32(v)); }
DEVI int swap23(int k) { return (k & ~12) | ((k & 4) << 1) | ((k & 8) >> 1); }
DEVI int s_of_p(int p) { return 64 * (p & 31) + (p >> 5); }

typedef __amdgpu_buffer_rsrc_t rsrc_t;
constexpr int SC1 = 16;
DEVI rsrc_t ws_rsrc(const char* ws) { return __builtin_amdgcn_make_buffer_rsrc((void*)ws, 0, 0x1000000, 0x00020000); }
DEVI void st128c(rsrc_t r, u32 off, u4v v) { __builtin_amdgcn_raw_buffer_store_b128(v, r, off, 0, SC1); }
DEVI void st64c(rsrc_t r, u32 off, u2v v) { __builtin_amdgcn_raw_buffer_store_b64(v, r, off, 0, SC1); }
DEVI void st32c(rsrc_t r, u32 off, u32 v) { __builtin_amdgcn_raw_buffer_store_b32(v, r, off, 0, SC1); }
DEVI u4v ld128c(rsrc_t r, u32 off) { return __builtin_amdgcn_raw_buffer_load_b128(r, off, 0, SC1); }
DEVI u32 ld32c(rsrc_t r, u32 off) { return __builtin_amdgcn_raw_buffer_load_b32(r, off, 0, SC1); }

struct Params {
  const u16 *x, *wq, *wk, *wv, *wo, *wr, *wi, *gw;
  u16* out;
  char* ws;
};

#define STAMP_DECL()
#define STMP(n)
#define STAMP_ARG
#define STAMP_PASS
#define STAMP_END(P, K)

DEVI u64 read_epoch(const Params& P) { return __hip_atomic_load((GAS u64*)(P.ws + WS_FL + FL_E), __ATOMIC_RELAXED, __HIP_MEMORY_SCOPE_AGENT); }
DEVI u64 tag_of(u64 e) { return (((u64)uni((u32)(e >> 32)) << 32) | uni((u32)e)) + 1; }
DEVI void put_flag(const Params& P, u32 off, u64 tag) {
  __hip_atomic_store((GAS u64*)(P.ws + WS_FL + off), tag, __ATOMIC_RELAXED, __HIP_MEMORY_SCOPE_AGENT);
}
DEVI void publish(const Params& P, u32 off, u64 tag) {
  asm volatile("s_waitcnt vmcnt(0) lgkmcnt(0)\n\ts_barrier" ::: "memory");
  if (threadIdx.x == 0) put_flag(P, off, tag);
}
template <int N> DEVI bool test_flags(const Params& P, u32 off, u64 tag, int lane) {
  const GAS u64* f = (const GAS u64*)(P.ws + WS_FL + off);
  bool ok = true;
#pragma unroll
  for (int i = 0; i < (N + 63) / 64; i++) {
    const int idx = N >= 64 ? lane + 64 * i : (lane & (N - 1));
    ok &= __hip_atomic_load(f + idx, __ATOMIC_RELAXED, __HIP_MEMORY_SCOPE_AGENT) == tag;
  }
  return __all(ok);
}
#define PSLEEP 4
template <int N, int SLEEP = PSLEEP> DEVI void spin_flags(const Params& P, u32 off, u64 tag, int lane) {
  for (int it = 0; it < (1 << 20); it++) {
    if (test_flags<N>(P, off, tag, lane)) break;
    __builtin_amdgcn_s_sleep(SLEEP);
  }
}
template <int N, int SLEEP = PSLEEP> DEVI void wait_flags(const Params& P, u32 off, u64 tag) {
  if (uni(threadIdx.x >> 6) == 0) spin_flags<N, SLEEP>(P, off, tag, threadIdx.x & 63);
  asm volatile("s_waitcnt vmcnt(0) lgkmcnt(0)\n\ts_barrier" ::: "memory");
}

constexpr int CHUNK = 8 * KVT;
constexpr int KV_BASE = LDS_BYTES - 3 * CHUNK;
#define SA_T 7
#define HINT_CHUNK 7
#define HINT_SLEEP 40
constexpr float kBound2 = 48.f * 48.f;

DEVI void kv_fetch(const char* src, u32 dst, int lw, int lane) {
#pragma unroll
  for (int j = 0; j < 5; j++) glds16c(src + (lw + 4 * j) * 1024, lane * 16, dst + (lw + 4 * j) * 1024);
  if (lw == 0) glds16c(src + 20 * 1024, lane * 16, dst + 20 * 1024);
}
DEVI void kv_chunk0_4(const char* kvg, u32 lds0, int lw, int lane) { kv_fetch(kvg, lds0 + KV_BASE + 2 * CHUNK, lw, lane); }
template <bool FUSED>
DEVI void kv_loader(const Params& P, const char* kvg, u32 lds0, int lw, int lane, int wgi, u64 tag STAMP_ARG) {
  for (int c = 0; c < 8; c++) {
    if (c < 7) kv_fetch(kvg + (c + 1) * CHUNK, lds0 + KV_BASE + (c % 3) * CHUNK, lw, lane);
    if (FUSED && c == HINT_CHUNK && lw == 0) {
      __builtin_amdgcn_s_sleep(HINT_SLEEP);
      if (lane == 0) put_flag(P, FL_SB + wgi * 8, tag);
    }
    wait_vm_bar<0>();
  }
  if (FUSED && HINT_CHUNK == 8 && lw == 0 && lane == 0) put_flag(P, FL_SB + wgi * 8, tag);
}

DEVI void attn_loop_safe(char* smem, int lane, u4v qa0, u2v qb0, u4v qa1, u2v qb1, f16v& o0, f16v& o1) {
  const int r = lane & 31, h = lane >> 5;
  const int koff = lane * 16, k1off = 1024 + r * 8;
  const int voff = 1280 + h * 352 + (r <= 20 ? r : 21) * 16;
  float m0 = -__builtin_inff(), m1 = -__builtin_inff();
  auto one = [&](u4v ka, u2v kb, u4v v0, u4v v1, u4v qa, u2v qb, float& mrun, f16v& o) {
    f16v s = {};
    s = mfma32(ka, qa, s);
    s = mfma32k8(kb, qb, s);
    float mx = s[0];
#pragma unroll
    for (int i = 1; i < 16; i++) mx = fmaxf(mx, s[i]);
    mx = fmaxf(mx, __shfl_xor(mx, 32));
    const float mn = fmaxf(mrun, mx), al = __builtin_amdgcn_exp2f(mrun - mn);
    mrun = mn;
    u4v p0, p1;
#pragma unroll
    for (int i = 0; i < 16; i++) o[i] *= al;
#pragma unroll
    for (int i = 0; i < 4; i++) {
      p0[i] = pk2(__builtin_amdgcn_exp2f(s[2 * i] - mn), __builtin_amdgcn_exp2f(s[2 * i + 1] - mn));
      p1[i] = pk2(__builtin_amdgcn_exp2f(s[8 + 2 * i] - mn), __builtin_amdgcn_exp2f(s[9 + 2 * i] - mn));
    }
    o = mfma32(v0, p0, o);
    o = mfma32(v1, p1, o);
  };
  for (int c = 0; c < 8; c++) {
    for (int i = 0; i < 8; i++) {
      if (i == 7) lds_bar();
      const char* p = smem + KV_BASE + ((c + 2) % 3) * CHUNK + i * KVT;
      u4v ka = lds128(p + koff), v0 = lds128(p + voff), v1 = lds128(p + voff + 704);
      u2v kb = lds64(p + k1off);
      one(ka, kb, v0, v1, qa0, qb0, m0, o0);
      one(ka, kb, v0, v1, qa1, qb1, m1, o1);
    }
  }
}

DEVI void attn_loop_fast(u32 lds0, int lane, u4v qa0, u2v qb0, u4v qa1, u2v qb1, f16v& o0, f16v& o1) {
  const int r = lane & 31, h = lane >> 5;
  const u32 base = lds0 + KV_BASE;
  const u32 kb = base + lane * 16, k1b = base + 1024 + r * 8, vb = base + 1280 + h * 352 + (r <= 20 ? r : 21) * 16;
  u32 kc, k1c, vc, kn, k1n, vn;
  u32 s_c, s_nx;
  asm volatile(R"ASM(
.macro EXP r
  v_exp_f32 v[\r], v[\r]
.endm
.macro CVT d, a
  v_cvt_pk_bf16_f32 v[72+\d], v[\a], v[\a+1]
.endm
.macro STEPA ka, k1a, koff, va, voff, bar
  .if \bar
  s_barrier
  .endif
  v_mfma_f32_32x32x16_bf16 v[56:71], v[80:83], %[qa1], 0
  ds_read_b128 v[80:83], \ka offset:\koff
  EXP 40
  EXP 41
  EXP 42
  EXP 43
  v_mfma_f32_32x32x16_bf16 %[o1], v[90:93], v[76:79], %[o1]
  ds_read_b128 v[90:93], \va offset:\voff+704
  EXP 44
  CVT 0, 40
  EXP 45
  CVT 1, 42
  EXP 46
  v_mfma_f32_32x32x8_bf16 v[56:71], v[84:85], %[qb1], v[56:71]
  ds_read_b64 v[84:85], \k1a offset:\koff
  EXP 47
  EXP 48
  CVT 2, 44
  EXP 49
  CVT 3, 46
  EXP 50
  EXP 51
  s_waitcnt lgkmcnt(3)
  v_mfma_f32_32x32x16_bf16 %[o0], v[86:89], v[72:75], %[o0]
  EXP 52
  EXP 53
  CVT 4, 48
  EXP 54
  CVT 5, 50
  EXP 55
  CVT 6, 52
  s_waitcnt lgkmcnt(0)
  CVT 7, 54
.endm
.macro STEPB va, voff
  v_mfma_f32_32x32x16_bf16 v[40:55], v[80:83], %[qa0], 0
  EXP 56
  EXP 57
  EXP 58
  EXP 59
  v_mfma_f32_32x32x16_bf16 %[o0], v[90:93], v[76:79], %[o0]
  EXP 60
  CVT 0, 56
  EXP 61
  CVT 1, 58
  EXP 62
  v_mfma_f32_32x32x8_bf16 v[40:55], v[84:85], %[qb0], v[40:55]
  EXP 63
  EXP 64
  CVT 2, 60
  EXP 65
  CVT 3, 62
  EXP 66
  EXP 67
  v_mfma_f32_32x32x16_bf16 %[o1], v[86:89], v[72:75], %[o1]
  ds_read_b128 v[86:89], \va offset:\voff
  EXP 68
  EXP 69
  CVT 4, 64
  EXP 70
  CVT 5, 66
  EXP 71
  CVT 6, 68
  s_nop 0
  CVT 7, 70
.endm
  ; chunk 0 sits in slot 2, chunk 1 goes to slot 0
  v_add_u32 %[kc], 43008, %[kb]
  v_add_u32 %[k1c], 43008, %[k1b]
  v_add_u32 %[vc], 43008, %[vb]
  s_nop 0
  ds_read_b128 v[80:83], %[kc]
  ds_read_b64 v[84:85], %[k1c]
  ds_read_b128 v[86:89], %[vc]
  v_mov_b32 v90, 0
  v_mov_b32 v91, 0
  v_mov_b32 v92, 0
  v_mov_b32 v93, 0
  v_mov_b32 v76, 0
  v_mov_b32 v77, 0
  v_mov_b32 v78, 0
  v_mov_b32 v79, 0
  s_mov_b32 %[snx], 0
  v_mov_b32 %[kn], %[kb]
  v_mov_b32 %[k1n], %[k1b]
  v_mov_b32 %[vn], %[vb]
  s_mov_b32 %[sc_], 0
  s_waitcnt lgkmcnt(0)
  v_mfma_f32_32x32x16_bf16 v[40:55], v[80:83], %[qa0], 0
  v_mfma_f32_32x32x8_bf16 v[40:55], v[84:85], %[qb0], v[40:55]
  s_nop 15
  s_nop 7
3:
  .irp i, 0, 1, 2, 3, 4, 5, 6
  STEPA %[kc], %[k1c], (\i+1)*2688, %[vc], \i*2688, 0
  STEPB %[vc], (\i+1)*2688
  .endr
  STEPA %[kn], %[k1n], 0, %[vc], 7*2688, 1
  STEPB %[vn], 0
  v_mov_b32 %[kc], %[kn]
  v_mov_b32 %[k1c], %[k1n]
  v_mov_b32 %[vc], %[vn]
  s_add_u32 %[snx], %[snx], 21504
  s_cmp_eq_u32 %[snx], 64512
  s_cselect_b32 %[snx], 0, %[snx]
  v_add_u32 %[kn], %[snx], %[kb]
  v_add_u32 %[k1n], %[snx], %[k1b]
  v_add_u32 %[vn], %[snx], %[vb]
  s_add_u32 %[sc_], %[sc_], 1
  s_cmp_lt_u32 %[sc_], 8
  s_cbranch_scc1 3b
  v_mfma_f32_32x32x16_bf16 %[o1], v[90:93], v[76:79], %[o1]
  s_waitcnt lgkmcnt(0)
  s_nop 15
  s_nop 7
.purgem EXP
.purgem CVT
.purgem STEPA
.purgem STEPB
)ASM"
      : [o0] "+v"(o0), [o1] "+v"(o1), [kc] "=&v"(kc), [k1c] "=&v"(k1c), [vc] "=&v"(vc), [kn] "=&v"(kn), [k1n] "=&v"(k1n), [vn] "=&v"(vn),
        [sc_] "=&s"(s_c), [snx] "=&s"(s_nx)
      : [qa0] "v"(qa0), [qb0] "v"(qb0), [qa1] "v"(qa1), [qb1] "v"(qb1), [kb] "v"(kb), [k1b] "v"(k1b), [vb] "v"(vb)
      : "memory", "scc", "v40", "v41", "v42", "v43", "v44", "v45", "v46", "v47", "v48", "v49", "v50", "v51", "v52", "v53", "v54", "v55",
        "v56", "v57", "v58", "v59", "v60", "v61", "v62", "v63", "v64", "v65", "v66", "v67", "v68", "v69", "v70", "v71", "v72", "v73", "v74", "v75",
        "v76", "v77", "v78", "v79", "v80", "v81", "v82", "v83", "v84", "v85", "v86", "v87", "v88", "v89", "v90", "v91", "v92", "v93");
}

template <bool FUSED>
DEVI void a_wg(const Params& P, char* smem, int mt, int nt, int wgi STAMP_ARG) {
  const int tid = fresh_tid(), lane = tid & 63, w = uni(tid >> 6), r = lane & 31, h = lane >> 5;
  const u32 lds0 = (u32)(uintptr_t)(LAS char*)smem;
  const int p0 = mt * 64;
#define APRIO0 0
  __builtin_amdgcn_s_setprio(APRIO0);
  u64 epoch = 0;
  if (FUSED) epoch = read_epoch(P);
  const char* kvg = P.ws + WS_KV;
  u64 tag = 0;
  constexpr int QST = 18432;
  u4v qa[2]; u2v qb[2];
  float qn;
  auto qproj = [&](auto NPc) {
    constexpr int NP = NPc;
    u32 xoff, voff[NP - 1];
    {
      const int row = w * 8 + (lane >> 3), ch = (lane & 7) ^ ((row >> 1) & 7);
      xoff = (s_of_p(p0 + row) * 640 + ch * 8) * 2;
    }
    const int wp0 = NP == 3 ? 2 * w : w + 2;
#pragma unroll
    for (int j = 0; j < NP - 1; j++) {
      const int o = (wp0 + j) * 1024 + lane * 16, row = o / 160, c = (o % 160) >> 4;
      voff[j] = (row * 640 + 80 * nt + 8 * c) * 2;
    }
    auto xpiece = [&](int t) { glds16((const char*)P.x + t * 128, xoff, lds0 + (t & 3) * QST + w * 1024); };
    auto wpiece = [&](int t, int j) { glds16((const char*)P.wq + t * (64 * 1280), voff[j], lds0 + (t & 3) * QST + 8192 + (wp0 + j) * 1024); };
#pragma unroll
    for (int t = 0; t < 3; t++) {
      xpiece(t);
#pragma unroll
      for (int j = 0; j < NP - 1; j++) wpiece(t, j);
    }
    int xo[4];
#pragma unroll
    for (int ks = 0; ks < 4; ks++) xo[ks] = (w >> 2) * 4096 + r * 128 + (((2 * ks + h) ^ ((r >> 1) & 7)) << 4);
    const int woff = 8192 + (8 * h + ((lane & 15) >> 2)) * 160 + 40 * (w & 3) + ((lane >> 4) & 1) * 32 + (lane & 3) * 8;
    struct Fr { u4v x, wf; };
    auto rd = [&](const char* sl, int ks) {
      Fr f;
      f.x = lds128(sl + xo[ks]);
      const char* a = sl + woff + ks * 16 * 160;
      f.wf = cat(ldstr(a), ldstr(a + 4 * 160));
      return f;
    };
    f16v q = {};
    STMP(8);
    vm_bar<2 * NP>();
    STMP(9);
    if (FUSED) tag = tag_of(epoch);
    Fr fa = rd(smem, 0), fb = rd(smem, 1);
    auto stage = [&](int t, auto DMA, auto&& wait) {
      const char* sl = smem + (t & 3) * QST;
      const char* sl1 = smem + ((t + 1) & 3) * QST;
      q = mfma32(fa.wf, fa.x, q); fa = rd(sl, 2);
      if constexpr (DMA) xpiece(t + 3);
      __builtin_amdgcn_sched_barrier(0);
      q = mfma32(fb.wf, fb.x, q); fb = rd(sl, 3);
      if constexpr (DMA) wpiece(t + 3, 0);
      __builtin_amdgcn_sched_barrier(0);
      q = mfma32(fa.wf, fa.x, q);
      if constexpr (DMA && NP == 3) wpiece(t + 3, NP - 2);
      wait();
      fa = rd(sl1, 0);
      __builtin_amdgcn_sched_barrier(0);
      q = mfma32(fb.wf, fb.x, q); fb = rd(sl1, 1);
      __builtin_amdgcn_sched_barrier(0);
    };
#pragma unroll 1
    for (int t = 0; t < 7; t++) {
      stage(t, std::true_type{}, [&] { wait_vm_bar<2 * NP>(); });
      if (FUSED && t == SA_T && w == 4 && lane == 0) put_flag(P, FL_SA + wgi * 8, tag);
    }
    STMP(10);
    u64 fl[4];
    if (FUSED) {
#pragma unroll
      for (int k = 0; k < 4; k++) fl[k] = __hip_atomic_load((const GAS u64*)(P.ws + WS_FL + FL_S1) + lane + 64 * k, __ATOMIC_RELAXED, __HIP_MEMORY_SCOPE_AGENT);
    }
    stage(7, std::false_type{}, [&] { wait_vm_bar<NP + (FUSED ? 4 : 0)>(); });
    const bool got = !FUSED || __all(fl[0] == tag && fl[1] == tag && fl[2] == tag && fl[3] == tag);
    STMP(5);
    auto chunk0 = [&] {
#pragma unroll
      for (int j = 0; j < 2; j++) glds16c(kvg + (w + 8 * j) * 1024, lane * 16, lds0 + KV_BASE + 2 * CHUNK + (w + 8 * j) * 1024);
      if (w < 5) glds16c(kvg + (w + 16) * 1024, lane * 16, lds0 + KV_BASE + 2 * CHUNK + (w + 16) * 1024);
    };
    u32 kmr[4] = {};
    auto km_load = [&] {
      if (w == 5) {
        const rsrc_t r5 = ws_rsrc(P.ws);
#pragma unroll
        for (int k = 0; k < 4; k++) kmr[k] = ld32c(r5, WS_KM + k * 256 + lane * 4);
      }
    };
    if (got) { chunk0(); km_load(); }
    stage(8, std::false_type{}, [&] { if (FUSED) lds_bar(); else wait_vm_bar<2>(); });
    stage(9, std::false_type{}, [&] { lds_bar(); });
    STMP(6);
    if (FUSED && SA_T > 6 && w == 4 && lane == 0) put_flag(P, FL_SA + wgi * 8, tag);
    if (!got) { spin_flags<256, 1>(P, FL_S1, tag, lane); chunk0(); km_load(); }
    const float qs = 0.22360679774997896f * 1.4426950408889634f;
    float n = 0.f;
#pragma unroll
    for (int j = 0; j < 12; j++) { q[j] *= qs; if (j < 8 || h == 0) n += q[j] * q[j]; }
    qa[0] = u4v{pk2(q[0], q[1]), pk2(q[2], q[3]), pk2(q[4], q[5]), pk2(q[6], q[7])};
    qb[0] = h ? u2v{0u, 0u} : u2v{pk2(q[8], q[9]), pk2(q[10], q[11])};
    qn = n + xor32(n);
    if (w >= 4) {
      char* qx = smem + 1024 + (w & 3) * (7 * 256) + lane * 4;
#pragma unroll
      for (int j = 0; j < 4; j++) *(LAS u32*)(LAS char*)(qx + j * 256) = qa[0][j];
#pragma unroll
      for (int j = 0; j < 2; j++) *(LAS u32*)(LAS char*)(qx + (4 + j) * 256) = qb[0][j];
      *(LAS float*)(LAS char*)(qx + 6 * 256) = qn;
      if (w == 5) {
        const float km0 = nmax(__builtin_bit_cast(float, kmr[0]), __builtin_bit_cast(float, kmr[1]));
        const float km1 = nmax(__builtin_bit_cast(float, kmr[2]), __builtin_bit_cast(float, kmr[3]));
        const float kmx = max64(km0) + max64(km1);
        if (lane == 0) *(LAS float*)(LAS char*)smem = kmx;
      }
    }
  };
  if (w < 2) qproj(std::integral_constant<int, 3>{}); else qproj(std::integral_constant<int, 2>{});
  STMP(1);
  lds_bar();
  STMP(11);
  if (w >= 4) { wait_vm_bar<0>(); STMP(2); kv_loader<FUSED>(P, kvg, lds0, w - 4, lane, wgi, tag STAMP_PASS); return; }
  const rsrc_t rs = ws_rsrc(P.ws);
  const float km = *(const LAS float*)(const LAS char*)smem;
  {
    const char* qx = smem + 1024 + w * (7 * 256) + lane * 4;
#pragma unroll
    for (int j = 0; j < 4; j++) qa[1][j] = *(const LAS u32*)(const LAS char*)(qx + j * 256);
#pragma unroll
    for (int j = 0; j < 2; j++) qb[1][j] = *(const LAS u32*)(const LAS char*)(qx + (4 + j) * 256);
    qn = nmax(qn, *(const LAS float*)(const LAS char*)(qx + 6 * 256));
  }
  const bool fast = __all(qn * km * 1.05f <= kBound2);
#define APRIO 0
  __builtin_amdgcn_s_setprio(APRIO);
  f16v o0 = {}, o1 = {};
  wait_vm_bar<0>();
  STMP(2);
  if (fast) attn_loop_fast(lds0, lane, qa[0], qb[0], qa[1], qb[1], o0, o1);
  else attn_loop_safe(smem, lane, qa[0], qb[0], qa[1], qb[1], o0, o1);
  STMP(3);
  __builtin_amdgcn_s_setprio(3);
#pragma unroll
  for (int i = 0; i < 2; i++) {
    const f16v& o = i ? o1 : o0;
    const float up = xor32(o[8]);
    const float sc = __builtin_amdgcn_rcpf(h ? o[8] : up);
    char* dst = smem + (32 * i + r) * 176 + (20 * w + 4 * h) * 2;
    sts64(dst, u2v{pk2(o[0] * sc, o[1] * sc), pk2(o[2] * sc, o[3] * sc)});
    sts64(dst + 16, u2v{pk2(o[4] * sc, o[5] * sc), pk2(o[6] * sc, o[7] * sc)});
    if (h == 0) sts64(dst + 32, u2v{pk2(o[8] * sc, o[9] * sc), pk2(o[10] * sc, o[11] * sc)});
  }
  lds_bar();
  {
    u4v v[3];
    u32 o[3];
#pragma unroll
    for (int k = 0; k < 3; k++) {
      const int ch = tid + 256 * k < 640 ? tid + 256 * k : tid, row = ch / 10, c = ch % 10;
      v[k] = lds128(smem + row * 176 + c * 16);
      o[k] = WS_O + ((p0 + row) * 640 + 80 * nt + 8 * c) * 2;
    }
    st128c(rs, o[0], v[0]); st128c(rs, o[1], v[1]);
    if (w < 2) st128c(rs, o[2], v[2]);
  }
  if (FUSED) publish(P, FL_S4 + (mt * 8 + nt) * 8, tag_of(epoch));
  STMP(4);
}

DEVI void b1_kv(const Params& P, char* smem, int kg, int part STAMP_ARG) {
  const int tid = fresh_tid(), lane = tid & 63, w = uni(tid >> 6), l15 = lane & 15, g = lane >> 4;
  const u32 lds0 = (u32)(uintptr_t)(LAS char*)smem;
  constexpr int RW = 20480, RG = RW + 25600;
  const char* wsrc = (const char*)(part ? P.wv : P.wk);
  {
    const int row = (w & 1) * 8 + (lane >> 3), ch = (lane & 7) ^ ((row >> 1) & 7);
    const u32 xo = (s_of_p(16 * kg + row) * 640 + ch * 8) * 2;
    glds16((const char*)P.x + (w >> 1) * 128, xo, lds0 + w * 1024);
    glds16((const char*)P.x + ((w + 8) >> 1) * 128, xo, lds0 + (w + 8) * 1024);
    if (w < 4) glds16((const char*)P.x + ((w + 16) >> 1) * 128, xo, lds0 + (w + 16) * 1024);
    else glds16(wsrc + (w - 4) * 1024, lane * 16, lds0 + (w + 16) * 1024);
    glds16(wsrc + (w + 4) * 1024, lane * 16, lds0 + (w + 24) * 1024);
    glds16(wsrc + (w + 12) * 1024, lane * 16, lds0 + (w + 32) * 1024);
    if (w < 5) glds16(wsrc + (w + 20) * 1024, lane * 16, lds0 + (w + 40) * 1024);
    else if (part && w < 7) { const int e = (w - 5) * 512 + lane * 8; glds16(P.gw, (e > 632 ? 632 : e) * 2, lds0 + (w + 40) * 1024); }
  }
  STMP(13);
  wait_vm_bar<0>();
  STMP(14);
  const rsrc_t rs = ws_rsrc(P.ws);
  const int s = kg & 1;
  const u32 tile = WS_KV + (kg >> 1) * KVT;
  if (w < 2 + part) {
    const char* xa = smem + l15 * 128;
    const int sw = (l15 >> 1) & 7;
    const char* aw = smem + RW + (8 * g + (l15 >> 2)) * 40 + (lane & 3) * 8 + w * 32;
    f4v a0 = {}, a1 = {};
    auto run = [&](auto GATE, auto KPART) {
      u4v xf[2][4], wf[2][4];
      auto rd = [&](int b) {
#pragma unroll
        for (int e = 0; e < 4; e++) {
          const int ks = 4 * b + e;
          xf[b & 1][e] = lds128(xa + (ks >> 1) * 2048 + (((4 * (ks & 1) + g) ^ sw) << 4));
          if constexpr (GATE) wf[b & 1][e] = lds128(smem + RG + (32 * ks + 8 * g) * 2);
          else wf[b & 1][e] = cat(ldstr(aw + ks * 32 * 40), ldstr(aw + ks * 32 * 40 + 160));
        }
      };
      rd(0);
#pragma unroll
      for (int b = 0; b < 5; b++) {
        if (b < 4) rd(b + 1);
        __builtin_amdgcn_sched_barrier(0);
#pragma unroll
        for (int e = 0; e < 4; e++) {
          f4v& a = (e & 1) ? a1 : a0;
          a = KPART ? mfma16(wf[b & 1][e], xf[b & 1][e], a) : mfma16(xf[b & 1][e], wf[b & 1][e], a);
        }
        __builtin_amdgcn_sched_barrier(0);
      }
    };
    if (part == 0) run(std::false_type{}, std::true_type{});
    else if (w < 2) run(std::false_type{}, std::false_type{});
    else run(std::true_type{}, std::false_type{});
    const f4v a = a0 + a1;
    STMP(15);
    if (part == 0) {
      const u2v v = {pk2(a[0], a[1]), pk2(a[2], a[3])};
      float n2 = a[0] * a[0] + a[1] * a[1] + a[2] * a[2] + a[3] * a[3];
      if (w == 0) {
        st64c(rs, tile + ((g & 1) * 32 + 16 * s + l15) * 16 + (g >> 1) * 8, v);
        n2 += xor16(n2); n2 += xor32(n2);
      } else if (g == 0) st64c(rs, tile + 1024 + (16 * s + l15) * 8, v);
      n2 = max16(n2);
      if (lane == 0) st32c(rs, WS_KM + w * 512 + kg * 4, __builtin_bit_cast(u32, n2));
    } else if (w < 2) {
      u2v v = {pk2(a[0], a[1]), pk2(a[2], a[3])};
      if (w == 1 && l15 == 4) v = u2v{0x3f803f80u, 0x3f803f80u};
      if (w == 1 && l15 == 5) v = u2v{0u, 0u};
      if (w == 0 || l15 <= 5) st64c(rs, tile + 1280 + (2 * s + (g & 1)) * 352 + (g >> 1) * 8 + (16 * w + l15) * 16, v);
    } else if (l15 == 0) {
      u4v o;
#pragma unroll
      for (int i = 0; i < 4; i++) o[i] = __builtin_bit_cast(u32, 1.f / (1.f + __expf(-a[i])));
      st128c(rs, WS_G + (16 * kg + 4 * g) * 4, o);
    }
  }
}

struct WtUnit { u4v re, im; };
DEVI WtUnit wt_load(const Params& P, int u, int lane) {
  const int k2 = u / 20, f0 = 16 * (u % 20) + 8 * (lane >> 5), row = 32 * k2 + (lane & 31);
  WtUnit t;
  __builtin_memcpy(&t.re, P.wr + row * 321 + f0, 16);
  __builtin_memcpy(&t.im, P.wi + row * 321 + f0, 16);
  return t;
}
DEVI void wt_store(const Params& P, int u, int lane, const WtUnit& t) {
  const int k2 = u / 20, f0 = 16 * (u % 20) + 8 * (lane >> 5);
  const rsrc_t rs = ws_rsrc(P.ws);
  const u32 o = WS_WT + f0 * 8192 + k2 * 128 + (lane & 31) * 4;
#pragma unroll
  for (int j = 0; j < 4; j++) {
    st32c(rs, o + (2 * j) * 8192, (t.re[j] & 0xffffu) | (t.im[j] << 16));
    st32c(rs, o + (2 * j + 1) * 8192, (t.re[j] >> 16) | (t.im[j] & 0xffff0000u));
  }
}

template <bool COH> DEVI void b_piece(const char* base, u32 vo, int adv, u32 lds0, int w, int t) {
  if (COH) glds16c(base + t * adv, vo, lds0 + (t & 7) * 8192 + w * 1024);
  else glds16(base + t * adv, vo, lds0 + (t & 7) * 8192 + w * 1024);
}
template <int KIND> DEVI u4v b_frag(const char* smem, const int (&ao)[4], int n, int mb) {
  if (KIND == 2) { const char* p = smem + ao[mb] + (n & 15) * 4096; return cat(ldstr(p), ldstr(p + 1024)); }
  return lds128(smem + ao[n & 1] + ((n >> 1) & 7) * 8192 + mb * 2048);
}
template <int N, class F> DEVI void forN(F&& f) { if constexpr (N > 0) { forN<N - 1>(f); f(std::integral_constant<int, N - 1>{}); } }
template <class F> DEVI void call0(F& f) { if constexpr (std::is_invocable_v<F&>) f(); }
template <int J, class F> DEVI void call1(F& f) { if constexpr (std::is_invocable_v<F&, std::integral_constant<int, J>>) f(std::integral_constant<int, J>{}); }
constexpr int b_landed(int n, int npre) {
  const int m1 = npre == 20 ? 3 : -1;
  const int la = n < m1 ? 1 : 2, lap = (n - 1) < m1 ? 1 : 2;
  const int need = (n + la) >> 1, had = n == 0 ? 0 : (n - 1 + lap) >> 1;
  return need > had ? need : had;
}
template <int KIND, bool COH, bool MAIN, int NPRE, int NSIDE, class SIDE>
DEVI void b_gemm_w(const char* smem, u4v (&W)[20], const u4v* tab, f4v (&acc)[4], const int (&ao)[4],
                   const char* base, u32 vo, int adv, u32 lds0, int w, SIDE&& side STAMP_ARG) {
  STMP(16 + 4 * KIND);
#pragma unroll
  for (int t = 0; t < 5; t++) b_piece<COH>(base, vo, adv, lds0, w, t);
  STMP(17 + 4 * KIND);
  vm_bar<4>();
  STMP(18 + 4 * KIND);
  constexpr int N1 = (MAIN && NPRE == 20) ? 3 : -1;
  const int kq = w & 3;
  u4v a[2][4];
  if constexpr (MAIN) {
#pragma unroll
    for (int i = 0; i < (NPRE == 20 ? 1 : 2); i++)
#pragma unroll
      for (int mb = 0; mb < 4; mb++) a[i][mb] = b_frag<KIND>(smem, ao, i, mb);
  } else if (kq < 2) {
#pragma unroll
    for (int mb = 0; mb < 4; mb++) a[0][mb] = kq ? b_frag<KIND>(smem, ao, 1, mb) : b_frag<KIND>(smem, ao, 0, mb);
  }
  forN<20>([&](auto NN) {
    constexpr int n = NN, T = n >> 1;
    constexpr int M1 = NPRE == 20 ? 3 : -1;
    constexpr int LA = n < M1 ? 1 : 2, LAP = (n - 1) < M1 ? 1 : 2;
    constexpr int need = (n + LA) >> 1, had = n == 0 ? 0 : (n - 1 + LAP) >> 1;
    if constexpr (n == 10) { STMP(19 + 4 * KIND); }
    if constexpr (MAIN && NPRE == 12 && (n == 2 || n == 6)) {
#pragma unroll
      for (int i = 0; i < 4; i++) W[(n == 2 ? 12 : 16) + i] = tab[((n == 2 ? 12 : 16) + i) * 64];
    }
    if constexpr (!(n & 1) && n < 10) b_piece<COH>(base, vo, adv, lds0, w, 5 + T);
    if constexpr (!MAIN && n == 8) call0(side);
    if constexpr (MAIN && NSIDE >= 0 && n == 12) call0(side);
    if constexpr (MAIN && NSIDE < 0 && n >= 10 && n <= 15) call1<n - 10>(side);
    if constexpr (need > had && need <= 9) {
      constexpr int issued = 5 + (T + 1 < 5 ? T + 1 : 5);
      constexpr int wy = MAIN ? (NPRE == 12 ? ((n >= 2 && need <= 5) ? 4 : 0) + ((n >= 6 && need <= 7) ? 4 : 0) : 0) + (NSIDE >= 0 ? (n >= 12 ? NSIDE : 0) : (n >= 10 ? 2 * ((n < 15 ? n : 15) - 9) : 0)) : (n >= 8 ? NSIDE : 0);
      vm_bar<(issued - need - 1 + wy)>();
    }
    if constexpr (MAIN) {
      constexpr int cur = n <= N1 ? 0 : (n & 1);
#pragma unroll
      for (int mb = 0; mb < 4; mb++) {
        const u4v x = a[cur][mb];
        acc[mb] = KIND == 0 ? mfma16(x, W[n], acc[mb]) : mfma16(W[n], x, acc[mb]);
        if constexpr (n == N1) { a[(n + 1) & 1][mb] = b_frag<KIND>(smem, ao, n + 1, mb); a[n & 1][mb] = b_frag<KIND>(smem, ao, n + 2, mb); }
        else if constexpr (n + LA < 20) a[LA == 1 ? 0 : (n & 1)][mb] = b_frag<KIND>(smem, ao, n + LA, mb);
      }
    } else {
      if ((n & 3) == kq) {
#pragma unroll
        for (int mb = 0; mb < 4; mb++) acc[mb] = KIND == 0 ? mfma16(a[0][mb], W[n >> 2], acc[mb]) : mfma16(W[n >> 2], a[0][mb], acc[mb]);
      }
      constexpr bool now2 = n + 2 < 20 && ((n + 2) >> 1) <= b_landed(n, NPRE);
      constexpr bool late1 = n >= 1 && ((n + 1) >> 1) > b_landed(n - 1, NPRE);
      if constexpr (late1) {
        if (((n + 1) & 3) == kq) {
#pragma unroll
          for (int mb = 0; mb < 4; mb++) a[0][mb] = b_frag<KIND>(smem, ao, n + 1, mb);
        }
      }
      if constexpr (now2) {
        if (((n + 2) & 3) == kq) {
#pragma unroll
          for (int mb = 0; mb < 4; mb++) a[0][mb] = b_frag<KIND>(smem, ao, n + 2, mb);
        }
      }
    }
    __builtin_amdgcn_sched_barrier(0);
  });
}
template <bool MAIN>
DEVI void b_gemm_res(const char* smem, const u4v (&W)[20], f4v (&acc)[4], const int (&ao)[4], int w) {
  auto frag = [&](int n, int mb) { const char* p = smem + ao[mb] + n * 4096; return cat(ldstr(p), ldstr(p + 1024)); };
  if constexpr (MAIN) {
    u4v a[2][4];
#pragma unroll
    for (int mb = 0; mb < 4; mb++) a[0][mb] = frag(0, mb);
    forN<20>([&](auto NN) {
      constexpr int n = NN, cur = n <= 3 ? 0 : (n & 1);
#pragma unroll
      for (int mb = 0; mb < 4; mb++) {
        acc[mb] = mfma16(W[n], a[cur][mb], acc[mb]);
        if constexpr (n < 3) a[0][mb] = frag(n + 1, mb);
        else if constexpr (n == 3) { a[0][mb] = frag(4, mb); a[1][mb] = frag(5, mb); }
        else if constexpr (n + 2 < 20) a[n & 1][mb] = frag(n + 2, mb);
      }
      __builtin_amdgcn_sched_barrier(0);
    });
  } else {
    const int kq = w & 3;
    const char* sm = smem + kq * 4096;
    auto fr = [&](int j, int mb) { const char* p = sm + ao[mb] + j * 16384; return cat(ldstr(p), ldstr(p + 1024)); };
    u4v a[2][4];
#pragma unroll
    for (int mb = 0; mb < 4; mb++) a[0][mb] = fr(0, mb);
#pragma unroll
    for (int j = 0; j < 5; j++) {
      if (j < 4) {
#pragma unroll
        for (int mb = 0; mb < 4; mb++) a[(j + 1) & 1][mb] = fr(j + 1, mb);
      }
#pragma unroll
      for (int mb = 0; mb < 4; mb++) acc[mb] = mfma16(W[j], a[j & 1][mb], acc[mb]);
      __builtin_amdgcn_sched_barrier(0);
    }
  }
}
template <bool MAIN> DEVI f4v b_reduce(char* xa, const f4v (&acc)[4], int w, int lane) {
  lds_bar();
  const int kq = w & 3;
  if constexpr (!MAIN) {
#pragma unroll
    for (int mb = 0; mb < 4; mb++) *(LAS f4v*)(LAS char*)(xa + (kq * 4 + mb) * 1024 + lane * 16) = acc[mb];
  }
  lds_bar();
  f4v r = {};
  if constexpr (!MAIN) {
#pragma unroll
    for (int q = 0; q < 4; q++) r += *(const LAS f4v*)(const LAS char*)(xa + (q * 4 + kq) * 1024 + lane * 16);
  }
  return r;
}
DEVI u32 rows_off(int p0, bool perm, int w, int lane) {
  const int row = w * 8 + (lane >> 3), ch = (lane & 7) ^ ((row >> 1) & 7);
  return ((perm ? s_of_p(p0 + row) : p0 + row) * 640 + ch * 8) * 2;
}
DEVI void mk_ro(int (&ao)[4], int l15, int g) {
  ao[0] = l15 * 128 + ((g ^ ((l15 >> 1) & 7)) << 4);
  ao[1] = l15 * 128 + (((4 + g) ^ ((l15 >> 1) & 7)) << 4);
  ao[2] = ao[3] = 0;
}

template <bool FUSED>
DEVI void b2_fwd(const Params& P, char* smem, int mt, int nt, int i, u64 tag STAMP_ARG) {
  const int tid = fresh_tid(), lane = tid & 63, w = uni(tid >> 6), l15 = lane & 15, g = lane >> 4;
  const u32 lds0 = (u32)(uintptr_t)(LAS char*)smem;
  const u32 vo = rows_off(mt * 64, true, w, lane);
  f4v acc[4] = {};
  u4v W[20];
  int ao[4];
  mk_ro(ao, l15, g);
  char* stg = smem + 16384;
  auto put = [&](int blk, int mb, const f4v& v) {
    sts64(stg + blk * 2304 + l15 * 144 + (16 * mb + 4 * g) * 2, u2v{pk2(v[0], v[1]), pk2(v[2], v[3])});
  };
  auto go = [&] {
    if (FUSED) wait_flags<1>(P, FL_SA + i * 8, tag);
    STMP(9);
  };
  go();
  if (w < 4) {
    const u4v* tf = (const u4v*)&gTF + (nt * 5 + w) * 1280 + lane;
#pragma unroll
    for (int k = 0; k < 12; k++) W[k] = tf[k * 64];
    b_gemm_w<0, false, true, 12, 0>(smem, W, tf, acc, ao, (const char*)P.x, vo, 128, lds0, w, [] {} STAMP_PASS);
    STMP(11);
    b_reduce<true>(smem, acc, w, lane);
#pragma unroll
    for (int mb = 0; mb < 4; mb++) put(w, mb, acc[mb]);
    lds_bar();
  } else {
    const u4v* tf = (const u4v*)&gTF + (nt * 5 + 4) * 1280 + (w & 3) * 64 + lane;
#pragma unroll
    for (int k = 0; k < 5; k++) W[k] = tf[k * 256];
    const int u0 = 5 * i + 2 * (w > 4 ? w - 5 : 0), u1 = w < 7 ? u0 + 1 : u0;
    WtUnit t0, t1;
    u32 ny = 0, tv = 0;
    auto side = [&] {
      t0 = wt_load(P, u0, lane);
      t1 = wt_load(P, u1, lane);
      const int row = 32 * (i & 63) + (lane & 31);
      ny = (lane >> 5) ? P.wi[row * 321 + 320] : P.wr[row * 321 + 320];
    };
    b_gemm_w<0, false, false, 12, 5>(smem, W, nullptr, acc, ao, (const char*)P.x, vo, 128, lds0, w, side STAMP_PASS);
    asm volatile("" ::"v"(tv));
    const f4v r = b_reduce<false>(smem, acc, w, lane);
    put(4, w & 3, r);
    if (w > 4) {
      wt_store(P, u0, lane, t0);
      if (w < 7) wt_store(P, u1, lane, t1);
      else if (i < 64) {
        const u32 o = __shfl_xor(ny, 32);
        if (lane < 32) st32c(ws_rsrc(P.ws), WS_WT + 320 * 8192 + i * 128 + lane * 4, ny | (o << 16));
      }
    }
    lds_bar();
  }
  if (w < 5) {
    const rsrc_t rs = ws_rsrc(P.ws);
#pragma unroll
    for (int k = 0; k < 2; k++) {
      const int row = 8 * k + (lane >> 3), c = lane & 7;
      st128c(rs, WS_X1 + ((80 * nt + 16 * w + row) * 2048 + mt * 64 + 8 * c) * 2, lds128(stg + w * 2304 + row * 144 + c * 16));
    }
  }
}

constexpr int CT_A1 = 0, CT_A1C = 4096, CT_A2 = 8192, CT_TW1 = 24576, CT_TW2 = 40960, CT_XCH = 57344;
static_assert(sizeof(ColTabD) == CT_XCH, "table image");
DEVI u4v pk8(const f16v& a, int s) {
  u4v v = {pk2(a[8 * s], a[8 * s + 1]), pk2(a[8 * s + 2], a[8 * s + 3]), pk2(a[8 * s + 4], a[8 * s + 5]), pk2(a[8 * s + 6], a[8 * s + 7])};
  return v;
}
DEVI u4v neg8(u4v v) { return v ^ u4v{0x80008000u, 0x80008000u, 0x80008000u, 0x80008000u}; }
DEVI void column_tables(char* smem) {
  const int tid = fresh_tid(), lane = tid & 63, w = uni(tid >> 6);
  const u32 lds0 = (u32)(uintptr_t)(LAS char*)smem;
#pragma unroll
  for (int j = 0; j < 7; j++) glds16((const char*)&gCT + (w + 8 * j) * 1024, lane * 16, lds0 + (w + 8 * j) * 1024);
}
DEVI f2v lo2(f4v c) { return f2v{c[0], c[1]}; }
DEVI f2v hi2(f4v c) { return f2v{c[2], c[3]}; }
template <int SG> DEVI void twiddle(f16v& re, f16v& im, const char* tab) {
#pragma unroll
  for (int hq = 0; hq < 2; hq++) {
    f4v c[4];
#pragma unroll
    for (int q = 0; q < 4; q++) c[q] = __builtin_bit_cast(f4v, lds128(tab + (4 * hq + q) * 1024));
#pragma unroll
    for (int q = 0; q < 4; q++) {
      const int k = 2 * (4 * hq + q);
      const f2v a = {re[k], re[k + 1]}, b = {im[k], im[k + 1]}, cc = lo2(c[q]), ss = hi2(c[q]);
      const f2v nr = SG > 0 ? a * cc - b * ss : a * cc + b * ss;
      const f2v ni = SG > 0 ? b * cc + a * ss : b * cc - a * ss;
      re[k] = nr[0]; re[k + 1] = nr[1]; im[k] = ni[0]; im[k + 1] = ni[1];
    }
  }
}
DEVI void column_job(const Params& P, char* smem, int col, int f, int t, int lane) {
  const int r = lane & 31, h = lane >> 5;
  const rsrc_t rs = ws_rsrc(P.ws);
  const int cr = f == 0 ? 0 : f == 320 ? 1 : 2 * f;
  const bool real_in = (f == 0 || f == 320);
  const u32 zo = WS_X1 + (cr * 2048 + (32 * t + r) * 32 + 8 * h) * 2;
  u4v Zr[2], Zi[2];
#pragma unroll
  for (int ks = 0; ks < 2; ks++) {
    Zr[ks] = ld128c(rs, zo + 32 * ks);
    Zi[ks] = ld128c(rs, zo + 4096 + 32 * ks);
  }
  u32 wv[16];
#pragma unroll
  for (int i = 0; i < 16; i++) wv[i] = ld32c(rs, WS_WT + f * 8192 + (32 * t + (i & 3) + 8 * (i >> 2) + 4 * h) * 128 + r * 4);
  if (real_in) { Zi[0] = u4v{0u, 0u, 0u, 0u}; Zi[1] = Zi[0]; }
  const char* tb = smem + lane * 16;
  char* xch = smem + CT_XCH + col * 8192 + lane * 16;
  f16v re = {}, im = {};
#pragma unroll
  for (int ks = 0; ks < 2; ks++) {
    u4v c = lds128(tb + CT_A1 + ks * 1024), ns = lds128(tb + CT_A1 + 2048 + ks * 1024);
    re = mfma32(Zr[ks], c, re); re = mfma32(neg8(Zi[ks]), ns, re);
    im = mfma32(Zr[ks], ns, im); im = mfma32(Zi[ks], c, im);
  }
  twiddle<-1>(re, im, tb + CT_TW1 + t * 8192);
  u4v yr[4], yi[4];
  auto exchange = [&]() {
    yr[0] = pk8(re, 0); yr[1] = pk8(re, 1); yi[0] = pk8(im, 0); yi[1] = pk8(im, 1);
    char* mine = xch + t * 4096;
    sts128(mine, yr[0]); sts128(mine + 1024, yr[1]); sts128(mine + 2048, yi[0]); sts128(mine + 3072, yi[1]);
    lds_bar();
    const char* oth = xch + (t ^ 1) * 4096;
    yr[2] = lds128(oth); yr[3] = lds128(oth + 1024); yi[2] = lds128(oth + 2048); yi[3] = lds128(oth + 3072);
  };
  const char* a2 = tb + CT_A2 + t * 8192;
  const int ko = t * 4096;
  exchange();
  re = f16v{}; im = f16v{};
#pragma unroll
  for (int kk = 0; kk < 4; kk++) {
    u4v c = lds128(a2 + ((kk * 2048) ^ ko)), ns = lds128(a2 + ((kk * 2048) ^ ko) + 1024);
    re = mfma32(c, yr[kk], re); re = mfma32(ns, neg8(yi[kk]), re);
    im = mfma32(ns, yr[kk], im); im = mfma32(c, yi[kk], im);
  }
  const float wsc = real_in ? 0.5f : 1.f;
#pragma unroll
  for (int i = 0; i < 16; i += 2) {
    const f2v wr_ = f2v{bflo(wv[i]), bflo(wv[i + 1])} * wsc, wi_ = f2v{bfhi(wv[i]), bfhi(wv[i + 1])} * wsc;
    const f2v a = {re[i], re[i + 1]}, b = {im[i], im[i + 1]};
    const f2v nr = a * wr_ - b * wi_, ni = a * wi_ + b * wr_;
    re[i] = nr[0]; re[i + 1] = nr[1]; im[i] = ni[0]; im[i + 1] = ni[1];
  }
  lds_bar();
  exchange();
  re = f16v{}; im = f16v{};
#pragma unroll
  for (int kk = 0; kk < 4; kk++) {
    u4v c = lds128(a2 + ((kk * 2048) ^ ko)), ns = lds128(a2 + ((kk * 2048) ^ ko) + 1024);
    re = mfma32(yr[kk], c, re); re = mfma32(yi[kk], ns, re);
    im = mfma32(neg8(yr[kk]), ns, im); im = mfma32(yi[kk], c, im);
  }
  twiddle<1>(re, im, tb + CT_TW2 + t * 8192);
  u4v gr0 = pk8(re, 0), gr1 = pk8(re, 1), gi0 = pk8(im, 0), gi1 = pk8(im, 1);
  re = f16v{}; im = f16v{};
#pragma unroll
  for (int ks = 0; ks < 2; ks++) {
    u4v c = lds128(tb + CT_A1C + ks * 1024), ns = lds128(tb + CT_A1C + 2048 + ks * 1024);
    u4v gr = ks ? gr1 : gr0, gi = ks ? gi1 : gi0;
    re = mfma32(c, gr, re); re = mfma32(ns, gi, re);
    im = mfma32(ns, neg8(gr), im); im = mfma32(c, gi, im);
  }
  const u32 dst = WS_X2 + (cr * 2048 + (32 * t + r) * 32 + 8 * h) * 2;
#pragma unroll
  for (int g = 0; g < 4; g += 2) {
    st128c(rs, dst + 16 * g, swap_pack(pk2(re[4 * g], re[4 * g + 1]), pk2(re[4 * g + 2], re[4 * g + 3]),
                                       pk2(re[4 * g + 4], re[4 * g + 5]), pk2(re[4 * g + 6], re[4 * g + 7])));
    u4v vi = swap_pack(pk2(im[4 * g], im[4 * g + 1]), pk2(im[4 * g + 2], im[4 * g + 3]),
                       pk2(im[4 * g + 4], im[4 * g + 5]), pk2(im[4 * g + 6], im[4 * g + 7]));
    if (!real_in) st128c(rs, dst + 4096 + 16 * g, vi);
  }
}
DEVI void wot_job(const Params& P, char* img, int fr, int lane) {
  const int cb = fr / 20, ks = fr % 20;
  glds16((const char*)P.wo + (32 * ks * 640 + 16 * cb) * 2, (lane >> 1) * 1280 + (lane & 1) * 16, (u32)(uintptr_t)(LAS char*)img);
  wait_vm<0>();
  const char* a = img + (8 * (lane >> 4) + ((lane & 15) >> 2)) * 32 + (lane & 3) * 8;
  st128c(ws_rsrc(P.ws), WS_WOT + fr * 1024 + lane * 16, cat(ldstr(a), ldstr(a + 128)));
}
template <bool FUSED>
DEVI void b3_columns(const Params& P, char* smem, int i, u64 tag STAMP_ARG) {
  lds_bar();
  column_tables(smem);
  if (FUSED) wait_flags<256>(P, FL_S2, tag); else wait_vm_bar<0>();
  STMP(12);
  const int tid = fresh_tid(), w = uni(tid >> 6), lane = tid & 63;
  const int f = (w >> 1) == 0 ? i : 256 + i;
  if (w < 4 && f <= 320) column_job(P, smem, w >> 1, f, w & 1, lane);
  else {
    if (w >= 4 && i + 256 * (w - 4) < 800) wot_job(P, smem + CT_XCH + 16384 + (w - 4) * 1024, i + 256 * (w - 4), lane);
    lds_bar(); lds_bar(); lds_bar();
  }
}

template <bool FUSED>
DEVI void b45_out(const Params& P, char* smem, int mt, int nt, int wgi, u64 tag STAMP_ARG) {
  const u32 lds0 = (u32)(uintptr_t)(LAS char*)smem;
  const int p0 = mt * 64, n0 = nt * 80;
  constexpr int STG = 65536;
  const int tid = fresh_tid(), lane = tid & 63, w = uni(tid >> 6), l15 = lane & 15, g = lane >> 4;
  auto synth_off = [&] { const int o = w * 1024 + lane * 16, rho = o >> 7, s16 = (o >> 4) & 7, mbs = (s16 >> 1) ^ ((rho >> 1) & 3);
                         return (u32)((swap23(rho) * 2048 + p0 + mbs * 16 + (s16 & 1) * 8) * 2); };
  auto fetch_b = [&] {
    if (FUSED) {
      wait_flags<256>(P, FL_S3, tag);
    } else lds_bar();
    const u32 vo = synth_off();
#pragma unroll
    for (int t = 0; t < 10; t++) glds16c(P.ws + WS_X2 + t * (64 * 4096), vo, lds0 + t * 8192 + w * 1024);
    if (FUSED) {
      wait_flags<256>(P, FL_S1, tag);
      wait_flags<1>(P, FL_SB + wgi * 8, tag);
    }
    wait_vm_bar<0>();
  };
  auto mk_co = [&](int (&ao)[4]) {
    const int rho0 = 16 * (g >> 1) + 4 * (g & 1) + (l15 >> 2);
#pragma unroll
    for (int mb = 0; mb < 4; mb++) ao[mb] = rho0 * 128 + ((mb ^ ((rho0 >> 1) & 3)) << 5) + (lane & 3) * 8;
  };
  const float sb = 2.f / (float)(S_ * D_);
  const int blk = w < 4 ? w : 4;
  auto stg = [&](int mb) { return smem + STG + (16 * mb + l15) * 176 + (16 * blk + 4 * g) * 2; };
  auto park = [&](int mb, const f4v& v) { sts64(stg(mb), u2v{pk2(v[0] * sb, v[1] * sb), pk2(v[2] * sb, v[3] * sb)}); };
  auto mix = [&](int mb, const f4v& v, float gg) {
    const float og = 1.f - gg;
    const u2v b = lds64(stg(mb));
    sts64(stg(mb), u2v{pk2(gg * v[0] + og * bflo(b[0]), gg * v[1] + og * bfhi(b[0])), pk2(gg * v[2] + og * bflo(b[1]), gg * v[3] + og * bfhi(b[1]))});
  };
  const rsrc_t rs = ws_rsrc(P.ws);
  if (w < 4) {
    u4v W[20], WO[20];
    f4v accb[4] = {}, acca[4] = {};
    int ao[4];
    mk_co(ao);
    const u4v* ti = (const u4v*)&gTI + (nt * 5 + w) * 1280 + lane;
#pragma unroll
    for (int i = 0; i < 20; i++) W[i] = ti[i * 64];
    fetch_b();
    STMP(4);
    __builtin_amdgcn_s_waitcnt(0x0F70);
    b_gemm_res<true>(smem, W, accb, ao, w);
    STMP(27);
    lds_bar();
#pragma unroll
    for (int mb = 0; mb < 4; mb++) park(mb, accb[mb]);
    const u32 woff = WS_WOT + (nt * 5 + w) * 20480 + lane * 16;
#pragma unroll
    for (int i = 0; i < 20; i++) WO[i] = ld128c(rs, woff + i * 1024);
    lds_bar();
    STMP(5);
    mk_ro(ao, l15, g);
    const u32 vo = rows_off(p0, false, w, lane);
    if (FUSED) wait_flags<8, 1>(P, FL_S4 + mt * 64, tag); else lds_bar();
    STMP(7);
    __builtin_amdgcn_s_waitcnt(0x0F70);
    float gate[4];
    b_gemm_w<1, true, true, 20, 4>(smem, WO, nullptr, acca, ao, P.ws + WS_O, vo, 128, lds0, w, [&] {
#pragma unroll
      for (int mb = 0; mb < 4; mb++) gate[mb] = __builtin_bit_cast(float, ld32c(rs, WS_G + (p0 + 16 * mb + l15) * 4));
    } STAMP_PASS);
#pragma unroll
    for (int mb = 0; mb < 4; mb++) mix(mb, acca[mb], gate[mb]);
    b_reduce<true>(smem, acca, w, lane);
  } else {
    const int kq = w & 3;
    u4v W[20];
    f4v acc[4] = {};
    int ao[4];
    mk_co(ao);
    const u4v* ti = (const u4v*)&gTI + (nt * 5 + 4) * 1280 + kq * 64 + lane;
#pragma unroll
    for (int i = 0; i < 5; i++) W[i] = ti[i * 256];
    fetch_b();
    __builtin_amdgcn_s_waitcnt(0x0F70);
    b_gemm_res<false>(smem, W, acc, ao, w);
    const u32 woff = WS_WOT + (nt * 5 + 4) * 20480 + kq * 1024 + lane * 16;
#pragma unroll
    for (int i = 0; i < 5; i++) W[i] = ld128c(rs, woff + i * 4096);
    const float gate = __builtin_bit_cast(float, ld32c(rs, WS_G + (p0 + 16 * kq + l15) * 4));
    park(kq, b_reduce<false>(smem, acc, w, lane));
#pragma unroll
    for (int mb = 0; mb < 4; mb++) acc[mb] = f4v{};
    mk_ro(ao, l15, g);
    const u32 vo = rows_off(p0, false, w, lane);
    if (FUSED) wait_flags<8, 1>(P, FL_S4 + mt * 64, tag); else lds_bar();
    __builtin_amdgcn_s_waitcnt(0x0F70);
    b_gemm_w<1, true, false, 20, 0>(smem, W, nullptr, acc, ao, P.ws + WS_O, vo, 128, lds0, w, [] {} STAMP_PASS);
    mix(kq, b_reduce<false>(smem, acc, w, lane), gate);
  }
  lds_bar();
  {
    const int t2 = fresh_tid();
    u4v v[2];
    u16* d[2];
#pragma unroll
    for (int k = 0; k < 2; k++) {
      const int ch = t2 + NTHR * k < 640 ? t2 + NTHR * k : t2, row = ch / 10, c = ch % 10;
      v[k] = lds128(smem + STG + row * 176 + c * 16);
      d[k] = P.out + s_of_p(p0 + row) * 640 + n0 + 8 * c;
    }
    *(u4v*)d[0] = v[0];
    if (t2 < 128) *(u4v*)d[1] = v[1];
  }
}

template <int PH>
__global__ void __launch_bounds__(NTHR, 4) mega(Params P) {
  extern __shared__ __attribute__((aligned(16))) char smem[];
  STAMP_DECL()
  asm volatile("" ::"s"(P.x), "s"(P.wq), "s"(P.wk), "s"(P.wv), "s"(P.wo), "s"(P.wr), "s"(P.wi), "s"(P.gw), "s"(P.out), "s"(P.ws));
  constexpr bool FUSED = PH == 0;
  const int i = blockIdx.x & 255, xcd = i & 7, idx = i >> 3;
  const int mt = (xcd >> 1) * 8 + (idx >> 2), nt = (xcd & 1) * 4 + (idx & 3);
  if (blockIdx.x >= 256) {
    if (PH == 0 || PH == 2) a_wg<FUSED>(P, smem, mt, nt, i STAMP_PASS);
    STAMP_END(P, PH);
    return;
  }
#define BPRIO1 3
  __builtin_amdgcn_s_setprio(BPRIO1);
  u64 tag = 0, epoch = 0;
  if (FUSED) epoch = read_epoch(P);
  if (PH == 0 || PH == 1) {
    b1_kv(P, smem, i >> 1, i & 1 STAMP_PASS);
    STMP(8);
    if (FUSED) tag = tag_of(epoch);
    if (FUSED) publish(P, FL_S1 + i * 8, tag); else lds_bar();
    STMP(1);
#define BPRIO 1
#define BPRIO2 3
#define BPRIO4 2
    __builtin_amdgcn_s_setprio(BPRIO2);
    b2_fwd<FUSED>(P, smem, mt, nt, i, tag STAMP_PASS);
    if (FUSED) publish(P, FL_S2 + i * 8, tag);
    STMP(2);
  }
  __builtin_amdgcn_s_setprio(BPRIO);
  if (PH == 0 || PH == 2) {
    b3_columns<FUSED>(P, smem, i, tag STAMP_PASS);
    if (FUSED) publish(P, FL_S3 + i * 8, tag);
    STMP(3);
  }
  __builtin_amdgcn_s_setprio(BPRIO4);
  if (FUSED && i == 0 && uni(threadIdx.x >> 6) == 7) {
    spin_flags<256>(P, FL_SA, tag, threadIdx.x & 63);
    if ((threadIdx.x & 63) == 0) put_flag(P, FL_E, tag);
  }
  if (PH == 0 || PH == 3) b45_out<FUSED>(P, smem, mt, nt, i, tag STAMP_PASS);
  STAMP_END(P, PH);
}

extern "C" void kernel_launch(void* const* d_in, const int* in_sizes, int n_in, void* d_out, int out_size, void* d_ws,
                              size_t ws_size, hipStream_t stream) {
  Params P;
  P.x = (const u16*)d_in[0];
  P.wq = (const u16*)d_in[1];
  P.wk = (const u16*)d_in[2];
  P.wv = (const u16*)d_in[3];
  P.wo = (const u16*)d_in[4];
  P.wr = (const u16*)d_in[6];
  P.wi = (const u16*)d_in[7];
  P.gw = (const u16*)d_in[8];
  P.out = (u16*)d_out;
  P.ws = (char*)d_ws;
  (void)hipFuncSetAttribute((const void*)mega<0>, hipFuncAttributeMaxDynamicSharedMemorySize, LDS_BYTES);
  mega<0><<<512, NTHR, LDS_BYTES, stream>>>(P);
}
```

```cpp
#include <hip/hip_runtime.h>
#include <stdint.h>
#include <type_traits>
#include <utility>

#define WQTOUCH
#define MODE_SPLIT 0

typedef unsigned short u16;
typedef unsigned int u32;
typedef unsigned long long u64;
typedef __attribute__((ext_vector_type(8))) __bf16 bf8;
typedef __attribute__((ext_vector_type(4))) short s4;
typedef __attribute__((ext_vector_type(16))) float f16v;
typedef __attribute__((ext_vector_type(4))) float f4v;
typedef __attribute__((ext_vector_type(2))) float f2v;
typedef __attribute__((ext_vector_type(2))) __bf16 bf2;
typedef __attribute__((ext_vector_type(4))) u32 u4v;
typedef __attribute__((ext_vector_type(2))) u32 u2v;
#define LAS __attribute__((address_space(3)))
#define GAS __attribute__((address_space(1)))
#define DEVI __device__ __forceinline__

constexpr int S_ = 2048, D_ = 640;
constexpr int NTHR = 512, LDS_BYTES = 81920;

constexpr u32 WS_X1 = 0x000000;
constexpr u32 WS_X2 = 0x280000;
constexpr u32 WS_O = 0x500000;
constexpr u32 WS_WT = 0x780000;
constexpr u32 WS_KV = 0xA10000;
constexpr u32 WS_KM = 0xA3A000;
constexpr u32 WS_G = 0xA3B000;
constexpr u32 WS_FL = 0xA40000;
constexpr u32 WS_WOT = 0xA50000;
constexpr u32 FL_E = 0, FL_S1 = 0x800, FL_S2 = 0x1000, FL_S3 = 0x1800, FL_S4 = 0x2000, FL_SA = 0x2800, FL_SB = 0x3000;
constexpr int KVT = 2688;

constexpr double kPi = 3.14159265358979323846;
constexpr double cx_sin(double x) { double t = x, s = x; for (int i = 1; i < 10; i++) { t *= -x * x / ((2 * i) * (2 * i + 1)); s += t; } return s; }
constexpr double cx_cos(double x) { double t = 1, s = 1; for (int i = 1; i < 10; i++) { t *= -x * x / ((2 * i - 1) * (2 * i)); s += t; } return s; }
constexpr double cos2pi(int m, int N) {
  m %= N; if (m < 0) m += N;
  if (2 * m > N) m = N - m;
  if (4 * m > N) return -cos2pi(N / 2 - m, N);
  if (8 * m > N) return cx_sin(2 * kPi * (N / 4 - m) / N);
  return cx_cos(2 * kPi * m / N);
}
constexpr double sin2pi(int m, int N) { return cos2pi(m + 3 * (N / 4), N); }
constexpr u16 to_bf16(double d) { float f = (float)d; u32 u = __builtin_bit_cast(u32, f); u += 0x7fffu + ((u >> 16) & 1); return (u16)(u >> 16); }
constexpr int crow_c(int r, int h) { return (r & 3) + 8 * (r >> 2) + 4 * h; }
constexpr int kperm_c(int ks, int h, int j) { return 16 * ks + 8 * (j >> 2) + 4 * h + (j & 3); }

struct FragB { u16 v[20][64][8]; };
struct FragAll { FragB c[40]; };
struct A1T { u16 a1[2][2][64][8]; u16 a1c[2][2][64][8]; };
struct A2T { u16 v[4][2][64][8]; };
struct TWT { float v[8][64][4]; };
struct ColTabD { A1T a1; A2T a2[2]; TWT tw1[2]; TWT tw2[2]; };

#if defined(__HIP_DEVICE_COMPILE__) && __HIP_DEVICE_COMPILE__
struct Base640 { u16 c[640]; u16 ns[640]; };
constexpr Base640 mk_base() { Base640 b{}; for (int i = 0; i < 640; i++) { b.c[i] = to_bf16(cos2pi(i, 640)); b.ns[i] = to_bf16(-sin2pi(i, 640)); } return b; }
constexpr Base640 kBase = mk_base();
constexpr u16 Fval(int d, int comp) {
  return comp == 0 ? kBase.c[0] : comp == 1 ? kBase.c[(320 * d) % 640] : (comp & 1) ? kBase.ns[((comp >> 1) * d) % 640] : kBase.c[((comp >> 1) * d) % 640];
}
constexpr FragB mk_frag(int cb, int inv) {
  FragB r{};
  for (int ks = 0; ks < 20; ks++) for (int l = 0; l < 64; l++) for (int j = 0; j < 8; j++) {
    int n = 16 * cb + (l & 15), k = 32 * ks + 8 * (l >> 4) + j;
    r.v[ks][l][j] = inv ? Fval(n, k) : Fval(k, n);
  }
  return r;
}
template <int C, int I> constexpr FragB cFrag = mk_frag(C, I);
template <int INV, int... I> constexpr FragAll mk_fragall(std::integer_sequence<int, I...>) { return FragAll{{cFrag<I, INV>...}}; }
__device__ const FragAll gTF = mk_fragall<0>(std::make_integer_sequence<int, 40>{});
__device__ const FragAll gTI = mk_fragall<1>(std::make_integer_sequence<int, 40>{});

struct Trig2048 { double c[2048]; };
constexpr Trig2048 mk_trig() { Trig2048 t{}; for (int i = 0; i < 2048; i++) t.c[i] = cos2pi(i, 2048); return t; }
constexpr Trig2048 kTrig = mk_trig();
constexpr double C2K(int m) { return kTrig.c[m & 2047]; }
constexpr double S2K(int m) { return kTrig.c[(m + 1536) & 2047]; }
constexpr A1T mk_a1() {
  A1T t{};
  for (int ks = 0; ks < 2; ks++) for (int l = 0; l < 64; l++) for (int j = 0; j < 8; j++) {
    int h = l >> 5, r = l & 31;
    int n1 = 16 * ks + 8 * h + j;
    double c = C2K(64 * n1 * r), s = S2K(64 * n1 * r);
    t.a1[0][ks][l][j] = to_bf16(c); t.a1[1][ks][l][j] = to_bf16(-s);
    int k1 = kperm_c(ks, h, j);
    c = C2K(64 * k1 * r); s = S2K(64 * k1 * r);
    t.a1c[0][ks][l][j] = to_bf16(c); t.a1c[1][ks][l][j] = to_bf16(-s);
  }
  return t;
}
constexpr A2T mk_a2(int u) {
  A2T t{};
  for (int kk = 0; kk < 4; kk++) for (int l = 0; l < 64; l++) for (int j = 0; j < 8; j++) {
    int h = l >> 5, row = 32 * u + (l & 31), col = 32 * (kk >> 1) + kperm_c(kk & 1, h, j);
    double c = C2K(32 * row * col), s = S2K(32 * row * col);
    t.v[kk][0][l][j] = to_bf16(c); t.v[kk][1][l][j] = to_bf16(-s);
  }
  return t;
}
constexpr TWT mk_tw(int t, int inv) {
  TWT o{};
  for (int q = 0; q < 8; q++) for (int l = 0; l < 64; l++) for (int e = 0; e < 2; e++) {
    int h = l >> 5, r = 2 * q + e;
    int n2 = inv ? 32 * t + (l & 31) : 32 * t + crow_c(r, h);
    int k1 = inv ? crow_c(r, h) : (l & 31);
    o.v[q][l][e] = (float)C2K(n2 * k1); o.v[q][l][2 + e] = (float)S2K(n2 * k1);
  }
  return o;
}
constexpr A1T cA1 = mk_a1();
template <int U> constexpr A2T cA2 = mk_a2(U);
template <int T, int I> constexpr TWT cTW = mk_tw(T, I);
__device__ const ColTabD gCT = {cA1, {cA2<0>, cA2<1>}, {cTW<0, 0>, cTW<1, 0>}, {cTW<0, 1>, cTW<1, 1>}};
#else
extern __device__ const FragAll gTF;
extern __device__ const FragAll gTI;
extern __device__ const ColTabD gCT;
#endif

DEVI f16v mfma32(u4v a, u4v b, f16v c) { return __builtin_amdgcn_mfma_f32_32x32x16_bf16(__builtin_bit_cast(bf8, a), __builtin_bit_cast(bf8, b), c, 0, 0, 0); }
DEVI f16v mfma32k8(u2v a, u2v b, f16v c) { return __builtin_amdgcn_mfma_f32_32x32x8bf16_1k(__builtin_bit_cast(s4, a), __builtin_bit_cast(s4, b), c, 0, 0, 0); }
DEVI f4v mfma16(u4v a, u4v b, f4v c) { return __builtin_amdgcn_mfma_f32_16x16x32_bf16(__builtin_bit_cast(bf8, a), __builtin_bit_cast(bf8, b), c, 0, 0, 0); }
DEVI u32 pk2(float a, float b) { f2v v = {a, b}; return __builtin_bit_cast(u32, __builtin_convertvector(v, bf2)); }
DEVI float bflo(u32 w) { return __builtin_bit_cast(float, w << 16); }
DEVI float bfhi(u32 w) { return __builtin_bit_cast(float, w & 0xffff0000u); }
DEVI u4v lds128(const char* p) { return *(const LAS u4v*)(const LAS char*)p; }
DEVI u2v lds64(const char* p) { return *(const LAS u2v*)(const LAS char*)p; }
DEVI void sts128(char* p, u4v v) { *(LAS u4v*)(LAS char*)p = v; }
DEVI void sts64(char* p, u2v v) { *(LAS u2v*)(LAS char*)p = v; }
DEVI u2v ldstr(const char* p) { return __builtin_bit_cast(u2v, __builtin_amdgcn_ds_read_tr16_b64_v4i16((LAS s4*)(LAS char*)p)); }
DEVI u4v cat(u2v a, u2v b) { u4v r = {a.x, a.y, b.x, b.y}; return r; }
DEVI void glds16(const void* base, u32 voff, u32 lds) {
  u32 sv;
  asm volatile("s_mov_b32 %0, m0\n\ts_mov_b32 m0, %3\n\ts_nop 0\n\tglobal_load_lds_dwordx4 %1, %2\n\ts_mov_b32 m0, %0" : "=&s"(sv) : "v"(voff), "s"(base), "s"(lds) : "memory");
}
DEVI void glds16c(const void* base, u32 voff, u32 lds) {
  u32 sv;
  asm volatile("s_mov_b32 %0, m0\n\ts_mov_b32 m0, %3\n\ts_nop 0\n\tglobal_load_lds_dwordx4 %1, %2 sc1\n\ts_mov_b32 m0, %0" : "=&s"(sv) : "v"(voff), "s"(base), "s"(lds) : "memory");
}
template <int N> DEVI void wait_vm() { asm volatile("s_waitcnt vmcnt(%0)" ::"i"(N) : "memory"); }
template <int N> DEVI void wait_vm_bar() { asm volatile("s_waitcnt vmcnt(%0) lgkmcnt(0)\n\ts_barrier" ::"i"(N) : "memory"); }
template <int N> DEVI void vm_bar() { asm volatile("s_waitcnt vmcnt(%0)\n\ts_barrier" ::"i"(N) : "memory"); }
DEVI void raw_bar() { asm volatile("s_barrier" ::: "memory"); }
DEVI void lds_bar() { asm volatile("s_waitcnt lgkmcnt(0)\n\ts_barrier" ::: "memory"); }
DEVI u32 uni(u32 v) { return __builtin_amdgcn_readfirstlane(v); }
DEVI int fresh_tid() { int t = threadIdx.x; asm volatile("" : "+v"(t)); return t; }
DEVI u4v swap_pack(u32 a0, u32 a1, u32 b0, u32 b1) {
  auto r0 = __builtin_amdgcn_permlane32_swap(a0, b0, false, false);
  auto r1 = __builtin_amdgcn_permlane32_swap(a1, b1, false, false);
  u4v o = {r0[0], r1[0], r0[1], r1[1]};
  return o;
}
template <int CTRL> DEVI float dpp(float v) { return __builtin_bit_cast(float, __builtin_amdgcn_update_dpp(0, __builtin_bit_cast(int, v), CTRL, 0xf, 0xf, true)); }
DEVI float xor16(float v) { auto r = __builtin_amdgcn_permlane16_swap(__builtin_bit_cast(u32, v), __builtin_bit_cast(u32, v), false, false); return __builtin_bit_cast(float, (threadIdx.x & 16) ? r[0] : r[1]); }
DEVI float xor32(float v) { auto r = __builtin_amdgcn_permlane32_swap(__builtin_bit_cast(u32, v), __builtin_bit_cast(u32, v), false, false); return __builtin_bit_cast(float, (threadIdx.x & 32) ? r[0] : r[1]); }
DEVI float nmax(float a, float b) { return (a > b || a != a) ? a : b; }
DEVI float max16(float v) {
  v = nmax(v, dpp<0xB1>(v)); v = nmax(v, dpp<0x4E>(v)); v = nmax(v, dpp<0x141>(v)); return nmax(v, dpp<0x140>(v));
}
DEVI float max64(float v) { v = max16(v); v = nmax(v, xor16(v)); return nmax(v, xor32(v)); }
DEVI int swap23(int k) { return (k & ~12) | ((k & 4) << 1) | ((k & 8) >> 1); }
DEVI int s_of_p(int p) { return 64 * (p & 31) + (p >> 5); }

typedef __amdgpu_buffer_rsrc_t rsrc_t;
constexpr int SC1 = 16;
DEVI rsrc_t ws_rsrc(const char* ws) { return __builtin_amdgcn_make_buffer_rsrc((void*)ws, 0, 0x1000000, 0x00020000); }
DEVI void st128c(rsrc_t r, u32 off, u4v v) { __builtin_amdgcn_raw_buffer_store_b128(v, r, off, 0, SC1); }
DEVI void st64c(rsrc_t r, u32 off, u2v v) { __builtin_amdgcn_raw_buffer_store_b64(v, r, off, 0, SC1); }
DEVI void st32c(rsrc_t r, u32 off, u32 v) { __builtin_amdgcn_raw_buffer_store_b32(v, r, off, 0, SC1); }
DEVI u4v ld128c(rsrc_t r, u32 off) { return __builtin_amdgcn_raw_buffer_load_b128(r, off, 0, SC1); }
DEVI u32 ld32c(rsrc_t r, u32 off) { return __builtin_amdgcn_raw_buffer_load_b32(r, off, 0, SC1); }

struct Params {
  const u16 *x, *wq, *wk, *wv, *wo, *wr, *wi, *gw;
  u16* out;
  char* ws;
};

#define STAMP_DECL()
#define STMP(n)
#define STAMP_ARG
#define STAMP_PASS
#define STAMP_END(P, K)

DEVI u64 read_epoch(const Params& P) { return __hip_atomic_load((GAS u64*)(P.ws + WS_FL + FL_E), __ATOMIC_RELAXED, __HIP_MEMORY_SCOPE_AGENT); }
DEVI u64 tag_of(u64 e) { return (((u64)uni((u32)(e >> 32)) << 32) | uni((u32)e)) + 1; }
DEVI void put_flag(const Params& P, u32 off, u64 tag) {
  __hip_atomic_store((GAS u64*)(P.ws + WS_FL + off), tag, __ATOMIC_RELAXED, __HIP_MEMORY_SCOPE_AGENT);
}
DEVI void publish(const Params& P, u32 off, u64 tag) {
  asm volatile("s_waitcnt vmcnt(0) lgkmcnt(0)\n\ts_barrier" ::: "memory");
  if (threadIdx.x == 0) put_flag(P, off, tag);
}
template <int N> DEVI bool test_flags(const Params& P, u32 off, u64 tag, int lane) {
  const GAS u64* f = (const GAS u64*)(P.ws + WS_FL + off);
  bool ok = true;
#pragma unroll
  for (int i = 0; i < (N + 63) / 64; i++) {
    const int idx = N >= 64 ? lane + 64 * i : (lane & (N - 1));
    ok &= __hip_atomic_load(f + idx, __ATOMIC_RELAXED, __HIP_MEMORY_SCOPE_AGENT) == tag;
  }
  return __all(ok);
}
#define PSLEEP 4
template <int N, int SLEEP = PSLEEP> DEVI void spin_flags(const Params& P, u32 off, u64 tag, int lane) {
  for (int it = 0; it < (1 << 20); it++) {
    if (test_flags<N>(P, off, tag, lane)) break;
    __builtin_amdgcn_s_sleep(SLEEP);
  }
}
template <int N, int SLEEP = PSLEEP> DEVI void wait_flags(const Params& P, u32 off, u64 tag) {
  if (uni(threadIdx.x >> 6) == 0) spin_flags<N, SLEEP>(P, off, tag, threadIdx.x & 63);
  asm volatile("s_waitcnt vmcnt(0) lgkmcnt(0)\n\ts_barrier" ::: "memory");
}

constexpr int CHUNK = 8 * KVT;
constexpr int KV_BASE = LDS_BYTES - 3 * CHUNK;
#define SA_T 7
#define HINT_CHUNK 7
#define HINT_SLEEP 40
constexpr float kBound2 = 48.f * 48.f;

DEVI void kv_fetch(const char* src, u32 dst, int lw, int lane) {
#pragma unroll
  for (int j = 0; j < 5; j++) glds16c(src + (lw + 4 * j) * 1024, lane * 16, dst + (lw + 4 * j) * 1024);
  if (lw == 0) glds16c(src + 20 * 1024, lane * 16, dst + 20 * 1024);
}
DEVI void kv_chunk0_4(const char* kvg, u32 lds0, int lw, int lane) { kv_fetch(kvg, lds0 + KV_BASE + 2 * CHUNK, lw, lane); }
template <bool FUSED>
DEVI void kv_loader(const Params& P, const char* kvg, u32 lds0, int lw, int lane, int wgi, u64 tag STAMP_ARG) {
  for (int c = 0; c < 8; c++) {
    if (c < 7) kv_fetch(kvg + (c + 1) * CHUNK, lds0 + KV_BASE + (c % 3) * CHUNK, lw, lane);
    if (FUSED && c == HINT_CHUNK && lw == 0) {
      __builtin_amdgcn_s_sleep(HINT_SLEEP);
      if (lane == 0) put_flag(P, FL_SB + wgi * 8, tag);
    }
    wait_vm_bar<0>();
  }
  if (FUSED && HINT_CHUNK == 8 && lw == 0 && lane == 0) put_flag(P, FL_SB + wgi * 8, tag);
}

DEVI void attn_loop_safe(char* smem, int lane, u4v qa0, u2v qb0, u4v qa1, u2v qb1, f16v& o0, f16v& o1) {
  const int r = lane & 31, h = lane >> 5;
  const int koff = lane * 16, k1off = 1024 + r * 8;
  const int voff = 1280 + h * 352 + (r <= 20 ? r : 21) * 16;
  float m0 = -__builtin_inff(), m1 = -__builtin_inff();
  auto one = [&](u4v ka, u2v kb, u4v v0, u4v v1, u4v qa, u2v qb, float& mrun, f16v& o) {
    f16v s = {};
    s = mfma32(ka, qa, s);
    s = mfma32k8(kb, qb, s);
    float mx = s[0];
#pragma unroll
    for (int i = 1; i < 16; i++) mx = fmaxf(mx, s[i]);
    mx = fmaxf(mx, __shfl_xor(mx, 32));
    const float mn = fmaxf(mrun, mx), al = __builtin_amdgcn_exp2f(mrun - mn);
    mrun = mn;
    u4v p0, p1;
#pragma unroll
    for (int i = 0; i < 16; i++) o[i] *= al;
#pragma unroll
    for (int i = 0; i < 4; i++) {
      p0[i] = pk2(__builtin_amdgcn_exp2f(s[2 * i] - mn), __builtin_amdgcn_exp2f(s[2 * i + 1] - mn));
      p1[i] = pk2(__builtin_amdgcn_exp2f(s[8 + 2 * i] - mn), __builtin_amdgcn_exp2f(s[9 + 2 * i] - mn));
    }
    o = mfma32(v0, p0, o);
    o = mfma32(v1, p1, o);
  };
  for (int c = 0; c < 8; c++) {
    for (int i = 0; i < 8; i++) {
      if (i == 7) lds_bar();
      const char* p = smem + KV_BASE + ((c + 2) % 3) * CHUNK + i * KVT;
      u4v ka = lds128(p + koff), v0 = lds128(p + voff), v1 = lds128(p + voff + 704);
      u2v kb = lds64(p + k1off);
      one(ka, kb, v0, v1, qa0, qb0, m0, o0);
      one(ka, kb, v0, v1, qa1, qb1, m1, o1);
    }
  }
}

DEVI void attn_loop_fast(u32 lds0, int lane, u4v qa0, u2v qb0, u4v qa1, u2v qb1, f16v& o0, f16v& o1) {
  const int r = lane & 31, h = lane >> 5;
  const u32 base = lds0 + KV_BASE;
  const u32 kb = base + lane * 16, k1b = base + 1024 + r * 8, vb = base + 1280 + h * 352 + (r <= 20 ? r : 21) * 16;
  u32 s_c;
  asm volatile(R"ASM(
.macro EXP r
  v_exp_f32 v[\r], v[\r]
.endm
.macro CVT d, a
  v_cvt_pk_bf16_f32 v[72+\d], v[\a], v[\a+1]
.endm
; tile t, query tile A: scores B of tile t are produced; K(t+1), V(t) keys 16..31 and V(t+1) keys 0..15 are fetched
.macro STEPA cur, nxt, v0c, v0n, bar
  .if \bar
  s_barrier
  .endif
  v_mfma_f32_32x32x16_bf16 v[56:71], v[80:83], %[qa1], 0
  ds_read_b128 v[80:83], %[kb] offset:\nxt
  EXP 40
  EXP 41
  EXP 42
  EXP 43
  v_mfma_f32_32x32x16_bf16 %[o1], v[90:93], v[76:79], %[o1]
  ds_read_b128 v[90:93], %[vb] offset:\cur+704
  EXP 44
  CVT 0, 40
  EXP 45
  CVT 1, 42
  EXP 46
  v_mfma_f32_32x32x8_bf16 v[56:71], v[84:85], %[qb1], v[56:71]
  ds_read_b64 v[84:85], %[k1b] offset:\nxt
  EXP 47
  EXP 48
  CVT 2, 44
  EXP 49
  CVT 3, 46
  EXP 50
  EXP 51
  v_mfma_f32_32x32x16_bf16 %[o0], v[\v0c:\v0c+3], v[72:75], %[o0]
  ds_read_b128 v[\v0n:\v0n+3], %[vb] offset:\nxt
  EXP 52
  EXP 53
  CVT 4, 48
  EXP 54
  CVT 5, 50
  EXP 55
  CVT 6, 52
  s_waitcnt lgkmcnt(1)
  CVT 7, 54
.endm
; tile t, query tile B: scores A of tile t+1 are produced
.macro STEPB v0c
  v_mfma_f32_32x32x16_bf16 v[40:55], v[80:83], %[qa0], 0
  EXP 56
  EXP 57
  EXP 58
  EXP 59
  v_mfma_f32_32x32x16_bf16 %[o0], v[90:93], v[76:79], %[o0]
  EXP 60
  CVT 0, 56
  EXP 61
  CVT 1, 58
  EXP 62
  v_mfma_f32_32x32x8_bf16 v[40:55], v[84:85], %[qb0], v[40:55]
  EXP 63
  EXP 64
  CVT 2, 60
  EXP 65
  CVT 3, 62
  EXP 66
  EXP 67
  v_mfma_f32_32x32x16_bf16 %[o1], v[\v0c:\v0c+3], v[72:75], %[o1]
  EXP 68
  EXP 69
  CVT 4, 64
  EXP 70
  CVT 5, 66
  EXP 71
  CVT 6, 68
  s_waitcnt lgkmcnt(0)
  CVT 7, 70
.endm
.macro PAIR cur, nxt, v0c, v0n, bar
  STEPA \cur, \nxt, \v0c, \v0n, \bar
  STEPB \v0c
.endm
; one chunk of 8 key tiles in the ring slot at \s; the next chunk sits at \n
.macro CHUNK s, n
  PAIR \s+0*2688, \s+1*2688, 86, 94, 0
  PAIR \s+1*2688, \s+2*2688, 94, 86, 0
  PAIR \s+2*2688, \s+3*2688, 86, 94, 0
  PAIR \s+3*2688, \s+4*2688, 94, 86, 0
  PAIR \s+4*2688, \s+5*2688, 86, 94, 0
  PAIR \s+5*2688, \s+6*2688, 94, 86, 0
  PAIR \s+6*2688, \s+7*2688, 86, 94, 0
  PAIR \s+7*2688, \n, 94, 86, 1
.endm
  ; chunk c sits in slot (c+2)%3
  ds_read_b128 v[80:83], %[kb] offset:43008
  ds_read_b64 v[84:85], %[k1b] offset:43008
  ds_read_b128 v[86:89], %[vb] offset:43008
  v_mov_b32 v90, 0
  v_mov_b32 v91, 0
  v_mov_b32 v92, 0
  v_mov_b32 v93, 0
  v_mov_b32 v76, 0
  v_mov_b32 v77, 0
  v_mov_b32 v78, 0
  v_mov_b32 v79, 0
  s_mov_b32 %[sc_], 0
  s_waitcnt lgkmcnt(0)
  v_mfma_f32_32x32x16_bf16 v[40:55], v[80:83], %[qa0], 0
  v_mfma_f32_32x32x8_bf16 v[40:55], v[84:85], %[qb0], v[40:55]
  s_nop 15
  s_nop 7
3:
  CHUNK 43008, 0
  CHUNK 0, 21504
  s_cmp_eq_u32 %[sc_], 2
  s_cbranch_scc1 4f
  CHUNK 21504, 43008
  s_add_u32 %[sc_], %[sc_], 1
  s_branch 3b
4:
  v_mfma_f32_32x32x16_bf16 %[o1], v[90:93], v[76:79], %[o1]
  s_waitcnt lgkmcnt(0)
  s_nop 15
  s_nop 7
.purgem EXP
.purgem CVT
.purgem STEPA
.purgem STEPB
.purgem PAIR
.purgem CHUNK
)ASM"
      : [o0] "+v"(o0), [o1] "+v"(o1), [sc_] "=&s"(s_c)
      : [qa0] "v"(qa0), [qb0] "v"(qb0), [qa1] "v"(qa1), [qb1] "v"(qb1), [kb] "v"(kb), [k1b] "v"(k1b), [vb] "v"(vb)
      : "memory", "scc", "v40", "v41", "v42", "v43", "v44", "v45", "v46", "v47", "v48", "v49", "v50", "v51", "v52", "v53", "v54", "v55",
        "v56", "v57", "v58", "v59", "v60", "v61", "v62", "v63", "v64", "v65", "v66", "v67", "v68", "v69", "v70", "v71", "v72", "v73", "v74", "v75",
        "v76", "v77", "v78", "v79", "v80", "v81", "v82", "v83", "v84", "v85", "v86", "v87", "v88", "v89", "v90", "v91", "v92", "v93",
        "v94", "v95", "v96", "v97");
}

template <bool FUSED>
DEVI void a_wg(const Params& P, char* smem, int mt, int nt, int wgi STAMP_ARG) {
  const int tid = fresh_tid(), lane = tid & 63, w = uni(tid >> 6), r = lane & 31, h = lane >> 5;
  const u32 lds0 = (u32)(uintptr_t)(LAS char*)smem;
  const int p0 = mt * 64;
#define APRIO0 0
  __builtin_amdgcn_s_setprio(APRIO0);
  u64 epoch = 0;
  if (FUSED) epoch = read_epoch(P);
  const char* kvg = P.ws + WS_KV;
  u64 tag = 0;
  constexpr int QST = 18432;
  u4v qa[2]; u2v qb[2];
  float qn;
  auto qproj = [&](auto NPc) {
    constexpr int NP = NPc;
    u32 xoff, voff[NP - 1];
    {
      const int row = w * 8 + (lane >> 3), ch = (lane & 7) ^ ((row >> 1) & 7);
      xoff = (s_of_p(p0 + row) * 640 + ch * 8) * 2;
    }
    const int wp0 = NP == 3 ? 2 * w : w + 2;
#pragma unroll
    for (int j = 0; j < NP - 1; j++) {
      const int o = (wp0 + j) * 1024 + lane * 16, row = o / 160, c = (o % 160) >> 4;
      voff[j] = (row * 640 + 80 * nt + 8 * c) * 2;
    }
    auto xpiece = [&](int t) { glds16((const char*)P.x + t * 128, xoff, lds0 + (t & 3) * QST + w * 1024); };
    auto wpiece = [&](int t, int j) { glds16((const char*)P.wq + t * (64 * 1280), voff[j], lds0 + (t & 3) * QST + 8192 + (wp0 + j) * 1024); };
#pragma unroll
    for (int t = 0; t < 3; t++) {
      xpiece(t);
#pragma unroll
      for (int j = 0; j < NP - 1; j++) wpiece(t, j);
    }
    int xo[4];
#pragma unroll
    for (int ks = 0; ks < 4; ks++) xo[ks] = (w >> 2) * 4096 + r * 128 + (((2 * ks + h) ^ ((r >> 1) & 7)) << 4);
    const int woff = 8192 + (8 * h + ((lane & 15) >> 2)) * 160 + 40 * (w & 3) + ((lane >> 4) & 1) * 32 + (lane & 3) * 8;
    struct Fr { u4v x, wf; };
    auto rd = [&](const char* sl, int ks) {
      Fr f;
      f.x = lds128(sl + xo[ks]);
      const char* a = sl + woff + ks * 16 * 160;
      f.wf = cat(ldstr(a), ldstr(a + 4 * 160));
      return f;
    };
    f16v q = {};
    STMP(8);
    vm_bar<2 * NP>();
    STMP(9);
    if (FUSED) tag = tag_of(epoch);
    Fr fa = rd(smem, 0), fb = rd(smem, 1);
    auto stage = [&](int t, auto DMA, auto&& wait) {
      const char* sl = smem + (t & 3) * QST;
      const char* sl1 = smem + ((t + 1) & 3) * QST;
      q = mfma32(fa.wf, fa.x, q); fa = rd(sl, 2);
      if constexpr (DMA) xpiece(t + 3);
      __builtin_amdgcn_sched_barrier(0);
      q = mfma32(fb.wf, fb.x, q); fb = rd(sl, 3);
      if constexpr (DMA) wpiece(t + 3, 0);
      __builtin_amdgcn_sched_barrier(0);
      q = mfma32(fa.wf, fa.x, q);
      if constexpr (DMA && NP == 3) wpiece(t + 3, NP - 2);
      wait();
      fa = rd(sl1, 0);
      __builtin_amdgcn_sched_barrier(0);
      q = mfma32(fb.wf, fb.x, q); fb = rd(sl1, 1);
      __builtin_amdgcn_sched_barrier(0);
    };
#pragma unroll 1
    for (int t = 0; t < 7; t++) {
      stage(t, std::true_type{}, [&] { wait_vm_bar<2 * NP>(); });
      if (FUSED && t == SA_T && w == 4 && lane == 0) put_flag(P, FL_SA + wgi * 8, tag);
    }
    STMP(10);
    u64 fl[4];
    if (FUSED) {
#pragma unroll
      for (int k = 0; k < 4; k++) fl[k] = __hip_atomic_load((const GAS u64*)(P.ws + WS_FL + FL_S1) + lane + 64 * k, __ATOMIC_RELAXED, __HIP_MEMORY_SCOPE_AGENT);
    }
    stage(7, std::false_type{}, [&] { wait_vm_bar<NP + (FUSED ? 4 : 0)>(); });
    const bool got = !FUSED || __all(fl[0] == tag && fl[1] == tag && fl[2] == tag && fl[3] == tag);
    STMP(5);
    auto chunk0 = [&] {
#pragma unroll
      for (int j = 0; j < 2; j++) glds16c(kvg + (w + 8 * j) * 1024, lane * 16, lds0 + KV_BASE + 2 * CHUNK + (w + 8 * j) * 1024);
      if (w < 5) glds16c(kvg + (w + 16) * 1024, lane * 16, lds0 + KV_BASE + 2 * CHUNK + (w + 16) * 1024);
    };
    u32 kmr[4] = {};
    auto km_load = [&] {
      if (w == 5) {
        const rsrc_t r5 = ws_rsrc(P.ws);
#pragma unroll
        for (int k = 0; k < 4; k++) kmr[k] = ld32c(r5, WS_KM + k * 256 + lane * 4);
      }
    };
    if (got) { chunk0(); km_load(); }
    stage(8, std::false_type{}, [&] { if (FUSED) lds_bar(); else wait_vm_bar<2>(); });
    stage(9, std::false_type{}, [&] { lds_bar(); });
    STMP(6);
    if (FUSED && SA_T > 6 && w == 4 && lane == 0) put_flag(P, FL_SA + wgi * 8, tag);
    if (!got) { spin_flags<256, 1>(P, FL_S1, tag, lane); chunk0(); km_load(); }
    const float qs = 0.22360679774997896f * 1.4426950408889634f;
    float n = 0.f;
#pragma unroll
    for (int j = 0; j < 12; j++) { q[j] *= qs; if (j < 8 || h == 0) n += q[j] * q[j]; }
    qa[0] = u4v{pk2(q[0], q[1]), pk2(q[2], q[3]), pk2(q[4], q[5]), pk2(q[6], q[7])};
    qb[0] = h ? u2v{0u, 0u} : u2v{pk2(q[8], q[9]), pk2(q[10], q[11])};
    qn = n + xor32(n);
    if (w >= 4) {
      char* qx = smem + 1024 + (w & 3) * (7 * 256) + lane * 4;
#pragma unroll
      for (int j = 0; j < 4; j++) *(LAS u32*)(LAS char*)(qx + j * 256) = qa[0][j];
#pragma unroll
      for (int j = 0; j < 2; j++) *(LAS u32*)(LAS char*)(qx + (4 + j) * 256) = qb[0][j];
      *(LAS float*)(LAS char*)(qx + 6 * 256) = qn;
      if (w == 5) {
        const float km0 = nmax(__builtin_bit_cast(float, kmr[0]), __builtin_bit_cast(float, kmr[1]));
        const float km1 = nmax(__builtin_bit_cast(float, kmr[2]), __builtin_bit_cast(float, kmr[3]));
        const float kmx = max64(km0) + max64(km1);
        if (lane == 0) *(LAS float*)(LAS char*)smem = kmx;
      }
    }
  };
  if (w < 2) qproj(std::integral_constant<int, 3>{}); else qproj(std::integral_constant<int, 2>{});
  STMP(1);
  lds_bar();
  STMP(11);
  if (w >= 4) { wait_vm_bar<0>(); STMP(2); kv_loader<FUSED>(P, kvg, lds0, w - 4, lane, wgi, tag STAMP_PASS); return; }
  const rsrc_t rs = ws_rsrc(P.ws);
  const float km = *(const LAS float*)(const LAS char*)smem;
  {
    const char* qx = smem + 1024 + w * (7 * 256) + lane * 4;
#pragma unroll
    for (int j = 0; j < 4; j++) qa[1][j] = *(const LAS u32*)(const LAS char*)(qx + j * 256);
#pragma unroll
    for (int j = 0; j < 2; j++) qb[1][j] = *(const LAS u32*)(const LAS char*)(qx + (4 + j) * 256);
    qn = nmax(qn, *(const LAS float*)(const LAS char*)(qx + 6 * 256));
  }
  const bool fast = __all(qn * km * 1.05f <= kBound2);
#define APRIO 0
  __builtin_amdgcn_s_setprio(APRIO);
  f16v o0 = {}, o1 = {};
  wait_vm_bar<0>();
  STMP(2);
  if (fast) attn_loop_fast(lds0, lane, qa[0], qb[0], qa[1], qb[1], o0, o1);
  else attn_loop_safe(smem, lane, qa[0], qb[0], qa[1], qb[1], o0, o1);
  STMP(3);
  __builtin_amdgcn_s_setprio(3);
#pragma unroll
  for (int i = 0; i < 2; i++) {
    const f16v& o = i ? o1 : o0;
    const float up = xor32(o[8]);
    const float sc = __builtin_amdgcn_rcpf(h ? o[8] : up);
    char* dst = smem + (32 * i + r) * 176 + (20 * w + 4 * h) * 2;
    sts64(dst, u2v{pk2(o[0] * sc, o[1] * sc), pk2(o[2] * sc, o[3] * sc)});
    sts64(dst + 16, u2v{pk2(o[4] * sc, o[5] * sc), pk2(o[6] * sc, o[7] * sc)});
    if (h == 0) sts64(dst + 32, u2v{pk2(o[8] * sc, o[9] * sc), pk2(o[10] * sc, o[11] * sc)});
  }
  STMP(12);
  lds_bar();
  STMP(13);
  {
    u4v v[3];
    u32 o[3];
#pragma unroll
    for (int k = 0; k < 3; k++) {
      const int ch = tid + 256 * k < 640 ? tid + 256 * k : tid, row = ch / 10, c = ch % 10;
      v[k] = lds128(smem + row * 176 + c * 16);
      o[k] = WS_O + ((p0 + row) * 640 + 80 * nt + 8 * c) * 2;
    }
    st128c(rs, o[0], v[0]); st128c(rs, o[1], v[1]);
    if (w < 2) st128c(rs, o[2], v[2]);
  }
  STMP(14);
  if (FUSED) publish(P, FL_S4 + (mt * 8 + nt) * 8, tag_of(epoch));
  STMP(4);
}

DEVI void b1_kv(const Params& P, char* smem, int kg, int part, int idx STAMP_ARG) {
  const int tid = fresh_tid(), lane = tid & 63, w = uni(tid >> 6), l15 = lane & 15, g = lane >> 4;
  const u32 lds0 = (u32)(uintptr_t)(LAS char*)smem;
  constexpr int RW = 20480, RG = RW + 25600;
  const char* wsrc = (const char*)(part ? P.wv : P.wk);
  {
    const int row = (w & 1) * 8 + (lane >> 3), ch = (lane & 7) ^ ((row >> 1) & 7);
    const u32 xo = (s_of_p(16 * kg + row) * 640 + ch * 8) * 2;
    glds16((const char*)P.x + (w >> 1) * 128, xo, lds0 + w * 1024);
    glds16((const char*)P.x + ((w + 8) >> 1) * 128, xo, lds0 + (w + 8) * 1024);
    if (w < 4) glds16((const char*)P.x + ((w + 16) >> 1) * 128, xo, lds0 + (w + 16) * 1024);
    else glds16(wsrc + (w - 4) * 1024, lane * 16, lds0 + (w + 16) * 1024);
    glds16(wsrc + (w + 4) * 1024, lane * 16, lds0 + (w + 24) * 1024);
    glds16(wsrc + (w + 12) * 1024, lane * 16, lds0 + (w + 32) * 1024);
    if (w < 5) glds16(wsrc + (w + 20) * 1024, lane * 16, lds0 + (w + 40) * 1024);
    else if (part && w < 7) { const int e = (w - 5) * 512 + lane * 8; glds16(P.gw, (e > 632 ? 632 : e) * 2, lds0 + (w + 40) * 1024); }
  }
  STMP(13);
  u32 tv = 0;
  if (w == 3 || w == 4) {
    const int t = ((w - 3) * 64 + lane) % 100;
    tv = *(const GAS u32*)((const GAS char*)P.wq + (20 * idx + t / 5) * 1280 + part * 640 + (t % 5) * 128);
    wait_vm_bar<1>();
  } else
  wait_vm_bar<0>();
  STMP(14);
  const rsrc_t rs = ws_rsrc(P.ws);
  const int s = kg & 1;
  const u32 tile = WS_KV + (kg >> 1) * KVT;
  if (w < 2 + part) {
    const char* xa = smem + l15 * 128;
    const int sw = (l15 >> 1) & 7;
    const char* aw = smem + RW + (8 * g + (l15 >> 2)) * 40 + (lane & 3) * 8 + w * 32;
    f4v a0 = {}, a1 = {};
    auto run = [&](auto GATE, auto KPART) {
      u4v xf[2][4], wf[2][4];
      auto rd = [&](int b) {
#pragma unroll
        for (int e = 0; e < 4; e++) {
          const int ks = 4 * b + e;
          xf[b & 1][e] = lds128(xa + (ks >> 1) * 2048 + (((4 * (ks & 1) + g) ^ sw) << 4));
          if constexpr (GATE) wf[b & 1][e] = lds128(smem + RG + (32 * ks + 8 * g) * 2);
          else wf[b & 1][e] = cat(ldstr(aw + ks * 32 * 40), ldstr(aw + ks * 32 * 40 + 160));
        }
      };
      rd(0);
#pragma unroll
      for (int b = 0; b < 5; b++) {
        if (b < 4) rd(b + 1);
        __builtin_amdgcn_sched_barrier(0);
#pragma unroll
        for (int e = 0; e < 4; e++) {
          f4v& a = (e & 1) ? a1 : a0;
          a = KPART ? mfma16(wf[b & 1][e], xf[b & 1][e], a) : mfma16(xf[b & 1][e], wf[b & 1][e], a);
        }
        __builtin_amdgcn_sched_barrier(0);
      }
    };
    if (part == 0) run(std::false_type{}, std::true_type{});
    else if (w < 2) run(std::false_type{}, std::false_type{});
    else run(std::true_type{}, std::false_type{});
    const f4v a = a0 + a1;
    STMP(15);
    if (part == 0) {
      const u2v v = {pk2(a[0], a[1]), pk2(a[2], a[3])};
      float n2 = a[0] * a[0] + a[1] * a[1] + a[2] * a[2] + a[3] * a[3];
      if (w == 0) {
        st64c(rs, tile + ((g & 1) * 32 + 16 * s + l15) * 16 + (g >> 1) * 8, v);
        n2 += xor16(n2); n2 += xor32(n2);
      } else if (g == 0) st64c(rs, tile + 1024 + (16 * s + l15) * 8, v);
      n2 = max16(n2);
      if (lane == 0) st32c(rs, WS_KM + w * 512 + kg * 4, __builtin_bit_cast(u32, n2));
    } else if (w < 2) {
      u2v v = {pk2(a[0], a[1]), pk2(a[2], a[3])};
      if (w == 1 && l15 == 4) v = u2v{0x3f803f80u, 0x3f803f80u};
      if (w == 1 && l15 == 5) v = u2v{0u, 0u};
      if (w == 0 || l15 <= 5) st64c(rs, tile + 1280 + (2 * s + (g & 1)) * 352 + (g >> 1) * 8 + (16 * w + l15) * 16, v);
    } else if (l15 == 0) {
      u4v o;
#pragma unroll
      for (int i = 0; i < 4; i++) o[i] = __builtin_bit_cast(u32, 1.f / (1.f + __expf(-a[i])));
      st128c(rs, WS_G + (16 * kg + 4 * g) * 4, o);
    }
  }
  asm volatile("" ::"v"(tv));
}

struct WtUnit { u4v re, im; };
DEVI WtUnit wt_load(const Params& P, int u, int lane) {
  const int k2 = u / 20, f0 = 16 * (u % 20) + 8 * (lane >> 5), row = 32 * k2 + (lane & 31);
  WtUnit t;
  __builtin_memcpy(&t.re, P.wr + row * 321 + f0, 16);
  __builtin_memcpy(&t.im, P.wi + row * 321 + f0, 16);
  return t;
}
DEVI void wt_store(const Params& P, int u, int lane, const WtUnit& t) {
  const int k2 = u / 20, f0 = 16 * (u % 20) + 8 * (lane >> 5);
  const rsrc_t rs = ws_rsrc(P.ws);
  const u32 o = WS_WT + f0 * 8192 + k2 * 128 + (lane & 31) * 4;
#pragma unroll
  for (int j = 0; j < 4; j++) {
    st32c(rs, o + (2 * j) * 8192, (t.re[j] & 0xffffu) | (t.im[j] << 16));
    st32c(rs, o + (2 * j + 1) * 8192, (t.re[j] >> 16) | (t.im[j] & 0xffff0000u));
  }
}

template <bool COH> DEVI void b_piece(const char* base, u32 vo, int adv, u32 lds0, int w, int t) {
  if (COH) glds16c(base + t * adv, vo, lds0 + (t & 7) * 8192 + w * 1024);
  else glds16(base + t * adv, vo, lds0 + (t & 7) * 8192 + w * 1024);
}
template <int KIND> DEVI u4v b_frag(const char* smem, const int (&ao)[4], int n, int mb) {
  if (KIND == 2) { const char* p = smem + ao[mb] + (n & 15) * 4096; return cat(ldstr(p), ldstr(p + 1024)); }
  return lds128(smem + ao[n & 1] + ((n >> 1) & 7) * 8192 + mb * 2048);
}
template <int N, class F> DEVI void forN(F&& f) { if constexpr (N > 0) { forN<N - 1>(f); f(std::integral_constant<int, N - 1>{}); } }
template <class F> DEVI void call0(F& f) { if constexpr (std::is_invocable_v<F&>) f(); }
template <int J, class F> DEVI void call1(F& f) { if constexpr (std::is_invocable_v<F&, std::integral_constant<int, J>>) f(std::integral_constant<int, J>{}); }
constexpr int b_landed(int n, int npre) {
  const int m1 = npre == 20 ? 3 : -1;
  const int la = n < m1 ? 1 : 2, lap = (n - 1) < m1 ? 1 : 2;
  const int need = (n + la) >> 1, had = n == 0 ? 0 : (n - 1 + lap) >> 1;
  return need > had ? need : had;
}
template <int KIND, bool COH, bool MAIN, int NPRE, int NSIDE, class SIDE>
DEVI void b_gemm_w(const char* smem, u4v (&W)[20], const u4v* tab, f4v (&acc)[4], const int (&ao)[4],
                   const char* base, u32 vo, int adv, u32 lds0, int w, SIDE&& side STAMP_ARG) {
  STMP(16 + 4 * KIND);
#pragma unroll
  for (int t = 0; t < 5; t++) b_piece<COH>(base, vo, adv, lds0, w, t);
  STMP(17 + 4 * KIND);
  vm_bar<4>();
  STMP(18 + 4 * KIND);
  constexpr int N1 = (MAIN && NPRE == 20) ? 3 : -1;
  const int kq = w & 3;
  u4v a[2][4];
  if constexpr (MAIN) {
#pragma unroll
    for (int i = 0; i < (NPRE == 20 ? 1 : 2); i++)
#pragma unroll
      for (int mb = 0; mb < 4; mb++) a[i][mb] = b_frag<KIND>(smem, ao, i, mb);
  } else if (kq < 2) {
#pragma unroll
    for (int mb = 0; mb < 4; mb++) a[0][mb] = kq ? b_frag<KIND>(smem, ao, 1, mb) : b_frag<KIND>(smem, ao, 0, mb);
  }
  forN<20>([&](auto NN) {
    constexpr int n = NN, T = n >> 1;
    constexpr int M1 = NPRE == 20 ? 3 : -1;
    constexpr int LA = n < M1 ? 1 : 2, LAP = (n - 1) < M1 ? 1 : 2;
    constexpr int need = (n + LA) >> 1, had = n == 0 ? 0 : (n - 1 + LAP) >> 1;
    if constexpr (n == 10) { STMP(19 + 4 * KIND); }
    if constexpr (MAIN && NPRE == 12 && (n == 2 || n == 6)) {
#pragma unroll
      for (int i = 0; i < 4; i++) W[(n == 2 ? 12 : 16) + i] = tab[((n == 2 ? 12 : 16) + i) * 64];
    }
    if constexpr (!(n & 1) && n < 10) b_piece<COH>(base, vo, adv, lds0, w, 5 + T);
    if constexpr (!MAIN && n == 8) call0(side);
    if constexpr (MAIN && NSIDE >= 0 && n == 12) call0(side);
    if constexpr (MAIN && NSIDE < 0 && n >= 10 && n <= 15) call1<n - 10>(side);
    if constexpr (need > had && need <= 9) {
      constexpr int issued = 5 + (T + 1 < 5 ? T + 1 : 5);
      constexpr int wy = MAIN ? (NPRE == 12 ? ((n >= 2 && need <= 5) ? 4 : 0) + ((n >= 6 && need <= 7) ? 4 : 0) : 0) + (NSIDE >= 0 ? (n >= 12 ? NSIDE : 0) : (n >= 10 ? 2 * ((n < 15 ? n : 15) - 9) : 0)) : (n >= 8 ? NSIDE : 0);
      vm_bar<(issued - need - 1 + wy)>();
    }
    if constexpr (MAIN) {
      constexpr int cur = n <= N1 ? 0 : (n & 1);
#pragma unroll
      for (int mb = 0; mb < 4; mb++) {
        const u4v x = a[cur][mb];
        acc[mb] = KIND == 0 ? mfma16(x, W[n], acc[mb]) : mfma16(W[n], x, acc[mb]);
        if constexpr (n == N1) { a[(n + 1) & 1][mb] = b_frag<KIND>(smem, ao, n + 1, mb); a[n & 1][mb] = b_frag<KIND>(smem, ao, n + 2, mb); }
        else if constexpr (n + LA < 20) a[LA == 1 ? 0 : (n & 1)][mb] = b_frag<KIND>(smem, ao, n + LA, mb);
      }
    } else {
      if ((n & 3) == kq) {
#pragma unroll
        for (int mb = 0; mb < 4; mb++) acc[mb] = KIND == 0 ? mfma16(a[0][mb], W[n >> 2], acc[mb]) : mfma16(W[n >> 2], a[0][mb], acc[mb]);
      }
      constexpr bool now2 = n + 2 < 20 && ((n + 2) >> 1) <= b_landed(n, NPRE);
      constexpr bool late1 = n >= 1 && ((n + 1) >> 1) > b_landed(n - 1, NPRE);
      if constexpr (late1) {
        if (((n + 1) & 3) == kq) {
#pragma unroll
          for (int mb = 0; mb < 4; mb++) a[0][mb] = b_frag<KIND>(smem, ao, n + 1, mb);
        }
      }
      if constexpr (now2) {
        if (((n + 2) & 3) == kq) {
#pragma unroll
          for (int mb = 0; mb < 4; mb++) a[0][mb] = b_frag<KIND>(smem, ao, n + 2, mb);
        }
      }
    }
    __builtin_amdgcn_sched_barrier(0);
  });
}
template <bool MAIN>
DEVI void b_gemm_res(const char* smem, const u4v (&W)[20], f4v (&acc)[4], const int (&ao)[4], int w) {
  auto frag = [&](int n, int mb) { const char* p = smem + ao[mb] + n * 4096; return cat(ldstr(p), ldstr(p + 1024)); };
  if constexpr (MAIN) {
    u4v a[2][4];
#pragma unroll
    for (int mb = 0; mb < 4; mb++) a[0][mb] = frag(0, mb);
    forN<20>([&](auto NN) {
      constexpr int n = NN, cur = n <= 3 ? 0 : (n & 1);
#pragma unroll
      for (int mb = 0; mb < 4; mb++) {
        acc[mb] = mfma16(W[n], a[cur][mb], acc[mb]);
        if constexpr (n < 3) a[0][mb] = frag(n + 1, mb);
        else if constexpr (n == 3) { a[0][mb] = frag(4, mb); a[1][mb] = frag(5, mb); }
        else if constexpr (n + 2 < 20) a[n & 1][mb] = frag(n + 2, mb);
      }
      __builtin_amdgcn_sched_barrier(0);
    });
  } else {
    const int kq = w & 3;
    const char* sm = smem + kq * 4096;
    auto fr = [&](int j, int mb) { const char* p = sm + ao[mb] + j * 16384; return cat(ldstr(p), ldstr(p + 1024)); };
    u4v a[2][4];
#pragma unroll
    for (int mb = 0; mb < 4; mb++) a[0][mb] = fr(0, mb);
#pragma unroll
    for (int j = 0; j < 5; j++) {
      if (j < 4) {
#pragma unroll
        for (int mb = 0; mb < 4; mb++) a[(j + 1) & 1][mb] = fr(j + 1, mb);
      }
#pragma unroll
      for (int mb = 0; mb < 4; mb++) acc[mb] = mfma16(W[j], a[j & 1][mb], acc[mb]);
      __builtin_amdgcn_sched_barrier(0);
    }
  }
}
template <bool MAIN> DEVI f4v b_reduce(char* xa, const f4v (&acc)[4], int w, int lane) {
  lds_bar();
  const int kq = w & 3;
  if constexpr (!MAIN) {
#pragma unroll
    for (int mb = 0; mb < 4; mb++) *(LAS f4v*)(LAS char*)(xa + (kq * 4 + mb) * 1024 + lane * 16) = acc[mb];
  }
  lds_bar();
  f4v r = {};
  if constexpr (!MAIN) {
#pragma unroll
    for (int q = 0; q < 4; q++) r += *(const LAS f4v*)(const LAS char*)(xa + (q * 4 + kq) * 1024 + lane * 16);
  }
  return r;
}
DEVI u32 rows_off(int p0, bool perm, int w, int lane) {
  const int row = w * 8 + (lane >> 3), ch = (lane & 7) ^ ((row >> 1) & 7);
  return ((perm ? s_of_p(p0 + row) : p0 + row) * 640 + ch * 8) * 2;
}
DEVI void mk_ro(int (&ao)[4], int l15, int g) {
  ao[0] = l15 * 128 + ((g ^ ((l15 >> 1) & 7)) << 4);
  ao[1] = l15 * 128 + (((4 + g) ^ ((l15 >> 1) & 7)) << 4);
  ao[2] = ao[3] = 0;
}

template <bool FUSED>
DEVI void b2_fwd(const Params& P, char* smem, int mt, int nt, int i, u64 tag STAMP_ARG) {
  const int tid = fresh_tid(), lane = tid & 63, w = uni(tid >> 6), l15 = lane & 15, g = lane >> 4;
  const u32 lds0 = (u32)(uintptr_t)(LAS char*)smem;
  const u32 vo = rows_off(mt * 64, true, w, lane);
  f4v acc[4] = {};
  u4v W[20];
  int ao[4];
  mk_ro(ao, l15, g);
  char* stg = smem + 16384;
  auto put = [&](int blk, int mb, const f4v& v) {
    sts64(stg + blk * 2304 + l15 * 144 + (16 * mb + 4 * g) * 2, u2v{pk2(v[0], v[1]), pk2(v[2], v[3])});
  };
  auto go = [&] {
    if (FUSED) wait_flags<1>(P, FL_SA + i * 8, tag);
    STMP(9);
  };
  go();
  if (w < 4) {
    const u4v* tf = (const u4v*)&gTF + (nt * 5 + w) * 1280 + lane;
#pragma unroll
    for (int k = 0; k < 12; k++) W[k] = tf[k * 64];
    b_gemm_w<0, false, true, 12, 0>(smem, W, tf, acc, ao, (const char*)P.x, vo, 128, lds0, w, [] {} STAMP_PASS);
    STMP(11);
    b_reduce<true>(smem, acc, w, lane);
#pragma unroll
    for (int mb = 0; mb < 4; mb++) put(w, mb, acc[mb]);
    lds_bar();
  } else {
    const u4v* tf = (const u4v*)&gTF + (nt * 5 + 4) * 1280 + (w & 3) * 64 + lane;
#pragma unroll
    for (int k = 0; k < 5; k++) W[k] = tf[k * 256];
    const int u0 = 5 * i + 2 * (w > 4 ? w - 5 : 0), u1 = w < 7 ? u0 + 1 : u0;
    WtUnit t0, t1;
    u32 ny = 0, tv = 0;
    auto side = [&] {
      t0 = wt_load(P, u0, lane);
      t1 = wt_load(P, u1, lane);
      const int row = 32 * (i & 63) + (lane & 31);
      ny = (lane >> 5) ? P.wi[row * 321 + 320] : P.wr[row * 321 + 320];
    };
    b_gemm_w<0, false, false, 12, 5>(smem, W, nullptr, acc, ao, (const char*)P.x, vo, 128, lds0, w, side STAMP_PASS);
    asm volatile("" ::"v"(tv));
    const f4v r = b_reduce<false>(smem, acc, w, lane);
    put(4, w & 3, r);
    if (w > 4) {
      wt_store(P, u0, lane, t0);
      if (w < 7) wt_store(P, u1, lane, t1);
      else if (i < 64) {
        const u32 o = __shfl_xor(ny, 32);
        if (lane < 32) st32c(ws_rsrc(P.ws), WS_WT + 320 * 8192 + i * 128 + lane * 4, ny | (o << 16));
      }
    }
    lds_bar();
  }
  if (w < 5) {
    const rsrc_t rs = ws_rsrc(P.ws);
#pragma unroll
    for (int k = 0; k < 2; k++) {
      const int row = 8 * k + (lane >> 3), c = lane & 7;
      st128c(rs, WS_X1 + ((80 * nt + 16 * w + row) * 2048 + mt * 64 + 8 * c) * 2, lds128(stg + w * 2304 + row * 144 + c * 16));
    }
  }
}

constexpr int CT_A1 = 0, CT_A1C = 4096, CT_A2 = 8192, CT_TW1 = 24576, CT_TW2 = 40960, CT_XCH = 57344;
static_assert(sizeof(ColTabD) == CT_XCH, "table image");
DEVI u4v pk8(const f16v& a, int s) {
  u4v v = {pk2(a[8 * s], a[8 * s + 1]), pk2(a[8 * s + 2], a[8 * s + 3]), pk2(a[8 * s + 4], a[8 * s + 5]), pk2(a[8 * s + 6], a[8 * s + 7])};
  return v;
}
DEVI u4v neg8(u4v v) { return v ^ u4v{0x80008000u, 0x80008000u, 0x80008000u, 0x80008000u}; }
DEVI void column_tables(char* smem) {
  const int tid = fresh_tid(), lane = tid & 63, w = uni(tid >> 6);
  const u32 lds0 = (u32)(uintptr_t)(LAS char*)smem;
#pragma unroll
  for (int j = 0; j < 7; j++) glds16((const char*)&gCT + (w + 8 * j) * 1024, lane * 16, lds0 + (w + 8 * j) * 1024);
}
DEVI f2v lo2(f4v c) { return f2v{c[0], c[1]}; }
DEVI f2v hi2(f4v c) { return f2v{c[2], c[3]}; }
template <int SG> DEVI void twiddle(f16v& re, f16v& im, const char* tab) {
#pragma unroll
  for (int hq = 0; hq < 2; hq++) {
    f4v c[4];
#pragma unroll
    for (int q = 0; q < 4; q++) c[q] = __builtin_bit_cast(f4v, lds128(tab + (4 * hq + q) * 1024));
#pragma unroll
    for (int q = 0; q < 4; q++) {
      const int k = 2 * (4 * hq + q);
      const f2v a = {re[k], re[k + 1]}, b = {im[k], im[k + 1]}, cc = lo2(c[q]), ss = hi2(c[q]);
      const f2v nr = SG > 0 ? a * cc - b * ss : a * cc + b * ss;
      const f2v ni = SG > 0 ? b * cc + a * ss : b * cc - a * ss;
      re[k] = nr[0]; re[k + 1] = nr[1]; im[k] = ni[0]; im[k + 1] = ni[1];
    }
  }
}
DEVI void column_job(const Params& P, char* smem, int col, int f, int t, int lane) {
  const int r = lane & 31, h = lane >> 5;
  const rsrc_t rs = ws_rsrc(P.ws);
  const int cr = f == 0 ? 0 : f == 320 ? 1 : 2 * f;
  const bool real_in = (f == 0 || f == 320);
  const u32 zo = WS_X1 + (cr * 2048 + (32 * t + r) * 32 + 8 * h) * 2;
  u4v Zr[2], Zi[2];
#pragma unroll
  for (int ks = 0; ks < 2; ks++) {
    Zr[ks] = ld128c(rs, zo + 32 * ks);
    Zi[ks] = ld128c(rs, zo + 4096 + 32 * ks);
  }
  u32 wv[16];
#pragma unroll
  for (int i = 0; i < 16; i++) wv[i] = ld32c(rs, WS_WT + f * 8192 + (32 * t + (i & 3) + 8 * (i >> 2) + 4 * h) * 128 + r * 4);
  if (real_in) { Zi[0] = u4v{0u, 0u, 0u, 0u}; Zi[1] = Zi[0]; }
  const char* tb = smem + lane * 16;
  char* xch = smem + CT_XCH + col * 8192 + lane * 16;
  f16v re = {}, im = {};
#pragma unroll
  for (int ks = 0; ks < 2; ks++) {
    u4v c = lds128(tb + CT_A1 + ks * 1024), ns = lds128(tb + CT_A1 + 2048 + ks * 1024);
    re = mfma32(Zr[ks], c, re); re = mfma32(neg8(Zi[ks]), ns, re);
    im = mfma32(Zr[ks], ns, im); im = mfma32(Zi[ks], c, im);
  }
  twiddle<-1>(re, im, tb + CT_TW1 + t * 8192);
  u4v yr[4], yi[4];
  auto exchange = [&]() {
    yr[0] = pk8(re, 0); yr[1] = pk8(re, 1); yi[0] = pk8(im, 0); yi[1] = pk8(im, 1);
    char* mine = xch + t * 4096;
    sts128(mine, yr[0]); sts128(mine + 1024, yr[1]); sts128(mine + 2048, yi[0]); sts128(mine + 3072, yi[1]);
    lds_bar();
    const char* oth = xch + (t ^ 1) * 4096;
    yr[2] = lds128(oth); yr[3] = lds128(oth + 1024); yi[2] = lds128(oth + 2048); yi[3] = lds128(oth + 3072);
  };
  const char* a2 = tb + CT_A2 + t * 8192;
  const int ko = t * 4096;
  exchange();
  re = f16v{}; im = f16v{};
#pragma unroll
  for (int kk = 0; kk < 4; kk++) {
    u4v c = lds128(a2 + ((kk * 2048) ^ ko)), ns = lds128(a2 + ((kk * 2048) ^ ko) + 1024);
    re = mfma32(c, yr[kk], re); re = mfma32(ns, neg8(yi[kk]), re);
    im = mfma32(ns, yr[kk], im); im = mfma32(c, yi[kk], im);
  }
  const float wsc = real_in ? 0.5f : 1.f;
#pragma unroll
  for (int i = 0; i < 16; i += 2) {
    const f2v wr_ = f2v{bflo(wv[i]), bflo(wv[i + 1])} * wsc, wi_ = f2v{bfhi(wv[i]), bfhi(wv[i + 1])} * wsc;
    const f2v a = {re[i], re[i + 1]}, b = {im[i], im[i + 1]};
    const f2v nr = a * wr_ - b * wi_, ni = a * wi_ + b * wr_;
    re[i] = nr[0]; re[i + 1] = nr[1]; im[i] = ni[0]; im[i + 1] = ni[1];
  }
  lds_bar();
  exchange();
  re = f16v{}; im = f16v{};
#pragma unroll
  for (int kk = 0; kk < 4; kk++) {
    u4v c = lds128(a2 + ((kk * 2048) ^ ko)), ns = lds128(a2 + ((kk * 2048) ^ ko) + 1024);
    re = mfma32(yr[kk], c, re); re = mfma32(yi[kk], ns, re);
    im = mfma32(neg8(yr[kk]), ns, im); im = mfma32(yi[kk], c, im);
  }
  twiddle<1>(re, im, tb + CT_TW2 + t * 8192);
  u4v gr0 = pk8(re, 0), gr1 = pk8(re, 1), gi0 = pk8(im, 0), gi1 = pk8(im, 1);
  re = f16v{}; im = f16v{};
#pragma unroll
  for (int ks = 0; ks < 2; ks++) {
    u4v c = lds128(tb + CT_A1C + ks * 1024), ns = lds128(tb + CT_A1C + 2048 + ks * 1024);
    u4v gr = ks ? gr1 : gr0, gi = ks ? gi1 : gi0;
    re = mfma32(c, gr, re); re = mfma32(ns, gi, re);
    im = mfma32(ns, neg8(gr), im); im = mfma32(c, gi, im);
  }
  const u32 dst = WS_X2 + (cr * 2048 + (32 * t + r) * 32 + 8 * h) * 2;
#pragma unroll
  for (int g = 0; g < 4; g += 2) {
    st128c(rs, dst + 16 * g, swap_pack(pk2(re[4 * g], re[4 * g + 1]), pk2(re[4 * g + 2], re[4 * g + 3]),
                                       pk2(re[4 * g + 4], re[4 * g + 5]), pk2(re[4 * g + 6], re[4 * g + 7])));
    u4v vi = swap_pack(pk2(im[4 * g], im[4 * g + 1]), pk2(im[4 * g + 2], im[4 * g + 3]),
                       pk2(im[4 * g + 4], im[4 * g + 5]), pk2(im[4 * g + 6], im[4 * g + 7]));
    if (!real_in) st128c(rs, dst + 4096 + 16 * g, vi);
  }
}
DEVI void wot_job(const Params& P, char* img, int fr, int lane) {
  const int cb = fr / 20, ks = fr % 20;
  glds16((const char*)P.wo + (32 * ks * 640 + 16 * cb) * 2, (lane >> 1) * 1280 + (lane & 1) * 16, (u32)(uintptr_t)(LAS char*)img);
  wait_vm<0>();
  const char* a = img + (8 * (lane >> 4) + ((lane & 15) >> 2)) * 32 + (lane & 3) * 8;
  st128c(ws_rsrc(P.ws), WS_WOT + fr * 1024 + lane * 16, cat(ldstr(a), ldstr(a + 128)));
}
template <bool FUSED>
DEVI void b3_columns(const Params& P, char* smem, int i, u64 tag STAMP_ARG) {
  lds_bar();
  column_tables(smem);
  if (FUSED) wait_flags<256>(P, FL_S2, tag); else wait_vm_bar<0>();
  STMP(12);
  const int tid = fresh_tid(), w = uni(tid >> 6), lane = tid & 63;
  const int f = (w >> 1) == 0 ? i : 256 + i;
  if (w < 4 && f <= 320) column_job(P, smem, w >> 1, f, w & 1, lane);
  else {
    if (w >= 4 && i + 256 * (w - 4) < 800) wot_job(P, smem + CT_XCH + 16384 + (w - 4) * 1024, i + 256 * (w - 4), lane);
    lds_bar(); lds_bar(); lds_bar();
  }
}

template <bool FUSED>
DEVI void b45_out(const Params& P, char* smem, int mt, int nt, int wgi, u64 tag STAMP_ARG) {
  const u32 lds0 = (u32)(uintptr_t)(LAS char*)smem;
  const int p0 = mt * 64, n0 = nt * 80;
  constexpr int STG = 65536;
  const int tid = fresh_tid(), lane = tid & 63, w = uni(tid >> 6), l15 = lane & 15, g = lane >> 4;
  auto synth_off = [&] { const int o = w * 1024 + lane * 16, rho = o >> 7, s16 = (o >> 4) & 7, mbs = (s16 >> 1) ^ ((rho >> 1) & 3);
                         return (u32)((swap23(rho) * 2048 + p0 + mbs * 16 + (s16 & 1) * 8) * 2); };
  auto fetch_b = [&] {
    if (FUSED) {
      wait_flags<256>(P, FL_S3, tag);
    } else lds_bar();
    const u32 vo = synth_off();
#pragma unroll
    for (int t = 0; t < 10; t++) glds16c(P.ws + WS_X2 + t * (64 * 4096), vo, lds0 + t * 8192 + w * 1024);
    if (FUSED) {
      wait_flags<256>(P, FL_S1, tag);
      wait_flags<1>(P, FL_SB + wgi * 8, tag);
    }
    wait_vm_bar<0>();
  };
  auto mk_co = [&](int (&ao)[4]) {
    const int rho0 = 16 * (g >> 1) + 4 * (g & 1) + (l15 >> 2);
#pragma unroll
    for (int mb = 0; mb < 4; mb++) ao[mb] = rho0 * 128 + ((mb ^ ((rho0 >> 1) & 3)) << 5) + (lane & 3) * 8;
  };
  const float sb = 2.f / (float)(S_ * D_);
  const int blk = w < 4 ? w : 4;
  auto stg = [&](int mb) { return smem + STG + (16 * mb + l15) * 176 + (16 * blk + 4 * g) * 2; };
  auto park = [&](int mb, const f4v& v) { sts64(stg(mb), u2v{pk2(v[0] * sb, v[1] * sb), pk2(v[2] * sb, v[3] * sb)}); };
  auto mix = [&](int mb, const f4v& v, float gg) {
    const float og = 1.f - gg;
    const u2v b = lds64(stg(mb));
    sts64(stg(mb), u2v{pk2(gg * v[0] + og * bflo(b[0]), gg * v[1] + og * bfhi(b[0])), pk2(gg * v[2] + og * bflo(b[1]), gg * v[3] + og * bfhi(b[1]))});
  };
  const rsrc_t rs = ws_rsrc(P.ws);
  if (w < 4) {
    u4v W[20], WO[20];
    f4v accb[4] = {}, acca[4] = {};
    int ao[4];
    mk_co(ao);
    const u4v* ti = (const u4v*)&gTI + (nt * 5 + w) * 1280 + lane;
#pragma unroll
    for (int i = 0; i < 20; i++) W[i] = ti[i * 64];
    fetch_b();
    STMP(4);
    __builtin_amdgcn_s_waitcnt(0x0F70);
    b_gemm_res<true>(smem, W, accb, ao, w);
    STMP(27);
    lds_bar();
#pragma unroll
    for (int mb = 0; mb < 4; mb++) park(mb, accb[mb]);
    const u32 woff = WS_WOT + (nt * 5 + w) * 20480 + lane * 16;
#pragma unroll
    for (int i = 0; i < 20; i++) WO[i] = ld128c(rs, woff + i * 1024);
    lds_bar();
    STMP(5);
    mk_ro(ao, l15, g);
    const u32 vo = rows_off(p0, false, w, lane);
    if (FUSED) wait_flags<8, 1>(P, FL_S4 + mt * 64, tag); else lds_bar();
    STMP(7);
    __builtin_amdgcn_s_waitcnt(0x0F70);
    float gate[4];
    b_gemm_w<1, true, true, 20, 4>(smem, WO, nullptr, acca, ao, P.ws + WS_O, vo, 128, lds0, w, [&] {
#pragma unroll
      for (int mb = 0; mb < 4; mb++) gate[mb] = __builtin_bit_cast(float, ld32c(rs, WS_G + (p0 + 16 * mb + l15) * 4));
    } STAMP_PASS);
#pragma unroll
    for (int mb = 0; mb < 4; mb++) mix(mb, acca[mb], gate[mb]);
    b_reduce<true>(smem, acca, w, lane);
  } else {
    const int kq = w & 3;
    u4v W[20];
    f4v acc[4] = {};
    int ao[4];
    mk_co(ao);
    const u4v* ti = (const u4v*)&gTI + (nt * 5 + 4) * 1280 + kq * 64 + lane;
#pragma unroll
    for (int i = 0; i < 5; i++) W[i] = ti[i * 256];
    fetch_b();
    __builtin_amdgcn_s_waitcnt(0x0F70);
    b_gemm_res<false>(smem, W, acc, ao, w);
    const u32 woff = WS_WOT + (nt * 5 + 4) * 20480 + kq * 1024 + lane * 16;
#pragma unroll
    for (int i = 0; i < 5; i++) W[i] = ld128c(rs, woff + i * 4096);
    const float gate = __builtin_bit_cast(float, ld32c(rs, WS_G + (p0 + 16 * kq + l15) * 4));
    park(kq, b_reduce<false>(smem, acc, w, lane));
#pragma unroll
    for (int mb = 0; mb < 4; mb++) acc[mb] = f4v{};
    mk_ro(ao, l15, g);
    const u32 vo = rows_off(p0, false, w, lane);
    if (FUSED) wait_flags<8, 1>(P, FL_S4 + mt * 64, tag); else lds_bar();
    __builtin_amdgcn_s_waitcnt(0x0F70);
    b_gemm_w<1, true, false, 20, 0>(smem, W, nullptr, acc, ao, P.ws + WS_O, vo, 128, lds0, w, [] {} STAMP_PASS);
    mix(kq, b_reduce<false>(smem, acc, w, lane), gate);
  }
  lds_bar();
  {
    const int t2 = fresh_tid();
    u4v v[2];
    u16* d[2];
#pragma unroll
    for (int k = 0; k < 2; k++) {
      const int ch = t2 + NTHR * k < 640 ? t2 + NTHR * k : t2, row = ch / 10, c = ch % 10;
      v[k] = lds128(smem + STG + row * 176 + c * 16);
      d[k] = P.out + s_of_p(p0 + row) * 640 + n0 + 8 * c;
    }
    *(u4v*)d[0] = v[0];
    if (t2 < 128) *(u4v*)d[1] = v[1];
  }
}

template <int PH>
__global__ void __launch_bounds__(NTHR, 4) mega(Params P) {
  extern __shared__ __attribute__((aligned(16))) char smem[];
  STAMP_DECL()
  asm volatile("" ::"s"(P.x), "s"(P.wq), "s"(P.wk), "s"(P.wv), "s"(P.wo), "s"(P.wr), "s"(P.wi), "s"(P.gw), "s"(P.out), "s"(P.ws));
  constexpr bool FUSED = PH == 0;
  const int i = blockIdx.x & 255, xcd = i & 7, idx = i >> 3;
  const int mt = (xcd >> 1) * 8 + (idx >> 2), nt = (xcd & 1) * 4 + (idx & 3);
  if (blockIdx.x >= 256) {
    if (PH == 0 || PH == 2) a_wg<FUSED>(P, smem, mt, nt, i STAMP_PASS);
    STAMP_END(P, PH);
    return;
  }
#define BPRIO1 3
  __builtin_amdgcn_s_setprio(BPRIO1);
  u64 tag = 0, epoch = 0;
  if (FUSED) epoch = read_epoch(P);
  if (PH == 0 || PH == 1) {
    b1_kv(P, smem, (xcd >> 1) * 32 + idx, xcd & 1, idx STAMP_PASS);
    STMP(8);
    if (FUSED) tag = tag_of(epoch);
    if (FUSED) publish(P, FL_S1 + i * 8, tag); else lds_bar();
    STMP(1);
#define BPRIO 0
#define BPRIO2 3
#define BPRIO4 2
    __builtin_amdgcn_s_setprio(BPRIO2);
    b2_fwd<FUSED>(P, smem, mt, nt, i, tag STAMP_PASS);
    if (FUSED) publish(P, FL_S2 + i * 8, tag);
    STMP(2);
  }
  __builtin_amdgcn_s_setprio(BPRIO);
  if (PH == 0 || PH == 2) {
    b3_columns<FUSED>(P, smem, i, tag STAMP_PASS);
    if (FUSED) publish(P, FL_S3 + i * 8, tag);
    STMP(3);
  }
  __builtin_amdgcn_s_setprio(BPRIO4);
  if (FUSED && i == 0 && uni(threadIdx.x >> 6) == 7) {
    spin_flags<256>(P, FL_SA, tag, threadIdx.x & 63);
    if ((threadIdx.x & 63) == 0) put_flag(P, FL_E, tag);
  }
  if (PH == 0 || PH == 3) b45_out<FUSED>(P, smem, mt, nt, i, tag STAMP_PASS);
  STAMP_END(P, PH);
}

extern "C" void kernel_launch(void* const* d_in, const int* in_sizes, int n_in, void* d_out, int out_size, void* d_ws,
                              size_t ws_size, hipStream_t stream) {
  Params P;
  P.x = (const u16*)d_in[0];
  P.wq = (const u16*)d_in[1];
  P.wk = (const u16*)d_in[2];
  P.wv = (const u16*)d_in[3];
  P.wo = (const u16*)d_in[4];
  P.wr = (const u16*)d_in[6];
  P.wi = (const u16*)d_in[7];
  P.gw = (const u16*)d_in[8];
  P.out = (u16*)d_out;
  P.ws = (char*)d_ws;
  (void)hipFuncSetAttribute((const void*)mega<0>, hipFuncAttributeMaxDynamicSharedMemorySize, LDS_BYTES);
  mega<0><<<512, NTHR, LDS_BYTES, stream>>>(P);
}
```

```cpp
#include <hip/hip_runtime.h>
#include <stdint.h>
#include <type_traits>
#include <utility>

#define WQTOUCH
#define MODE_SPLIT 0

typedef unsigned short u16;
typedef unsigned int u32;
typedef unsigned long long u64;
typedef __attribute__((ext_vector_type(8))) __bf16 bf8;
typedef __attribute__((ext_vector_type(4))) short s4;
typedef __attribute__((ext_vector_type(16))) float f16v;
typedef __attribute__((ext_vector_type(4))) float f4v;
typedef __attribute__((ext_vector_type(2))) float f2v;
typedef __attribute__((ext_vector_type(2))) __bf16 bf2;
typedef __attribute__((ext_vector_type(4))) u32 u4v;
typedef __attribute__((ext_vector_type(2))) u32 u2v;
#define LAS __attribute__((address_space(3)))
#define GAS __attribute__((address_space(1)))
#define DEVI __device__ __forceinline__

constexpr int S_ = 2048, D_ = 640;
constexpr int NTHR = 512, LDS_BYTES = 81920;

constexpr u32 WS_X1 = 0x000000;
constexpr u32 WS_X2 = 0x280000;
constexpr u32 WS_O = 0x500000;
constexpr u32 WS_WT = 0x780000;
constexpr u32 WS_KV = 0xA10000;
constexpr u32 WS_KM = 0xA3A000;
constexpr u32 WS_G = 0xA3B000;
constexpr u32 WS_FL = 0xA40000;
constexpr u32 WS_WOT = 0xA50000;
constexpr u32 FL_E = 0, FL_S1 = 0x800, FL_S2 = 0x1000, FL_S3 = 0x1800, FL_S4 = 0x2000, FL_SA = 0x2800, FL_SB = 0x3000;
constexpr int KVT = 2688;

constexpr double kPi = 3.14159265358979323846;
constexpr double cx_sin(double x) { double t = x, s = x; for (int i = 1; i < 10; i++) { t *= -x * x / ((2 * i) * (2 * i + 1)); s += t; } return s; }
constexpr double cx_cos(double x) { double t = 1, s = 1; for (int i = 1; i < 10; i++) { t *= -x * x / ((2 * i - 1) * (2 * i)); s += t; } return s; }
constexpr double cos2pi(int m, int N) {
  m %= N; if (m < 0) m += N;
  if (2 * m > N) m = N - m;
  if (4 * m > N) return -cos2pi(N / 2 - m, N);
  if (8 * m > N) return cx_sin(2 * kPi * (N / 4 - m) / N);
  return cx_cos(2 * kPi * m / N);
}
constexpr double sin2pi(int m, int N) { return cos2pi(m + 3 * (N / 4), N); }
constexpr u16 to_bf16(double d) { float f = (float)d; u32 u = __builtin_bit_cast(u32, f); u += 0x7fffu + ((u >> 16) & 1); return (u16)(u >> 16); }
constexpr int crow_c(int r, int h) { return (r & 3) + 8 * (r >> 2) + 4 * h; }
constexpr int kperm_c(int ks, int h, int j) { return 16 * ks + 8 * (j >> 2) + 4 * h + (j & 3); }

struct FragB { u16 v[20][64][8]; };
struct FragAll { FragB c[40]; };
struct A1T { u16 a1[2][2][64][8]; u16 a1c[2][2][64][8]; };
struct A2T { u16 v[4][2][64][8]; };
struct TWT { float v[8][64][4]; };
struct ColTabD { A1T a1; A2T a2[2]; TWT tw1[2]; TWT tw2[2]; };

#if defined(__HIP_DEVICE_COMPILE__) && __HIP_DEVICE_COMPILE__
struct Base640 { u16 c[640]; u16 ns[640]; };
constexpr Base640 mk_base() { Base640 b{}; for (int i = 0; i < 640; i++) { b.c[i] = to_bf16(cos2pi(i, 640)); b.ns[i] = to_bf16(-sin2pi(i, 640)); } return b; }
constexpr Base640 kBase = mk_base();
constexpr u16 Fval(int d, int comp) {
  return comp == 0 ? kBase.c[0] : comp == 1 ? kBase.c[(320 * d) % 640] : (comp & 1) ? kBase.ns[((comp >> 1) * d) % 640] : kBase.c[((comp >> 1) * d) % 640];
}
constexpr FragB mk_frag(int cb, int inv) {
  FragB r{};
  for (int ks = 0; ks < 20; ks++) for (int l = 0; l < 64; l++) for (int j = 0; j < 8; j++) {
    int n = 16 * cb + (l & 15), k = 32 * ks + 8 * (l >> 4) + j;
    r.v[ks][l][j] = inv ? Fval(n, k) : Fval(k, n);
  }
  return r;
}
template <int C, int I> constexpr FragB cFrag = mk_frag(C, I);
template <int INV, int... I> constexpr FragAll mk_fragall(std::integer_sequence<int, I...>) { return FragAll{{cFrag<I, INV>...}}; }
__device__ const FragAll gTF = mk_fragall<0>(std::make_integer_sequence<int, 40>{});
__device__ const FragAll gTI = mk_fragall<1>(std::make_integer_sequence<int, 40>{});

struct Trig2048 { double c[2048]; };
constexpr Trig2048 mk_trig() { Trig2048 t{}; for (int i = 0; i < 2048; i++) t.c[i] = cos2pi(i, 2048); return t; }
constexpr Trig2048 kTrig = mk_trig();
constexpr double C2K(int m) { return kTrig.c[m & 2047]; }
constexpr double S2K(int m) { return kTrig.c[(m + 1536) & 2047]; }
constexpr A1T mk_a1() {
  A1T t{};
  for (int ks = 0; ks < 2; ks++) for (int l = 0; l < 64; l++) for (int j = 0; j < 8; j++) {
    int h = l >> 5, r = l & 31;
    int n1 = 16 * ks + 8 * h + j;
    double c = C2K(64 * n1 * r), s = S2K(64 * n1 * r);
    t.a1[0][ks][l][j] = to_bf16(c); t.a1[1][ks][l][j] = to_bf16(-s);
    int k1 = kperm_c(ks, h, j);
    c = C2K(64 * k1 * r); s = S2K(64 * k1 * r);
    t.a1c[0][ks][l][j] = to_bf16(c); t.a1c[1][ks][l][j] = to_bf16(-s);
  }
  return t;
}
constexpr A2T mk_a2(int u) {
  A2T t{};
  for (int kk = 0; kk < 4; kk++) for (int l = 0; l < 64; l++) for (int j = 0; j < 8; j++) {
    int h = l >> 5, row = 32 * u + (l & 31), col = 32 * (kk >> 1) + kperm_c(kk & 1, h, j);
    double c = C2K(32 * row * col), s = S2K(32 * row * col);
    t.v[kk][0][l][j] = to_bf16(c); t.v[kk][1][l][j] = to_bf16(-s);
  }
  return t;
}
constexpr TWT mk_tw(int t, int inv) {
  TWT o{};
  for (int q = 0; q < 8; q++) for (int l = 0; l < 64; l++) for (int e = 0; e < 2; e++) {
    int h = l >> 5, r = 2 * q + e;
    int n2 = inv ? 32 * t + (l & 31) : 32 * t + crow_c(r, h);
    int k1 = inv ? crow_c(r, h) : (l & 31);
    o.v[q][l][e] = (float)C2K(n2 * k1); o.v[q][l][2 + e] = (float)S2K(n2 * k1);
  }
  return o;
}
constexpr A1T cA1 = mk_a1();
template <int U> constexpr A2T cA2 = mk_a2(U);
template <int T, int I> constexpr TWT cTW = mk_tw(T, I);
__device__ const ColTabD gCT = {cA1, {cA2<0>, cA2<1>}, {cTW<0, 0>, cTW<1, 0>}, {cTW<0, 1>, cTW<1, 1>}};
#else
extern __device__ const FragAll gTF;
extern __device__ const FragAll gTI;
extern __device__ const ColTabD gCT;
#endif

DEVI f16v mfma32(u4v a, u4v b, f16v c) { return __builtin_amdgcn_mfma_f32_32x32x16_bf16(__builtin_bit_cast(bf8, a), __builtin_bit_cast(bf8, b), c, 0, 0, 0); }
DEVI f16v mfma32k8(u2v a, u2v b, f16v c) { return __builtin_amdgcn_mfma_f32_32x32x8bf16_1k(__builtin_bit_cast(s4, a), __builtin_bit_cast(s4, b), c, 0, 0, 0); }
DEVI f4v mfma16(u4v a, u4v b, f4v c) { return __builtin_amdgcn_mfma_f32_16x16x32_bf16(__builtin_bit_cast(bf8, a), __builtin_bit_cast(bf8, b), c, 0, 0, 0); }
DEVI u32 pk2(float a, float b) { f2v v = {a, b}; return __builtin_bit_cast(u32, __builtin_convertvector(v, bf2)); }
DEVI float bflo(u32 w) { return __builtin_bit_cast(float, w << 16); }
DEVI float bfhi(u32 w) { return __builtin_bit_cast(float, w & 0xffff0000u); }
DEVI u4v lds128(const char* p) { return *(const LAS u4v*)(const LAS char*)p; }
DEVI u2v lds64(const char* p) { return *(const LAS u2v*)(const LAS char*)p; }
DEVI void sts128(char* p, u4v v) { *(LAS u4v*)(LAS char*)p = v; }
DEVI void sts64(char* p, u2v v) { *(LAS u2v*)(LAS char*)p = v; }
DEVI u2v ldstr(const char* p) { return __builtin_bit_cast(u2v, __builtin_amdgcn_ds_read_tr16_b64_v4i16((LAS s4*)(LAS char*)p)); }
DEVI u4v cat(u2v a, u2v b) { u4v r = {a.x, a.y, b.x, b.y}; return r; }
DEVI void glds16(const void* base, u32 voff, u32 lds) {
  u32 sv;
  asm volatile("s_mov_b32 %0, m0\n\ts_mov_b32 m0, %3\n\ts_nop 0\n\tglobal_load_lds_dwordx4 %1, %2\n\ts_mov_b32 m0, %0" : "=&s"(sv) : "v"(voff), "s"(base), "s"(lds) : "memory");
}
DEVI void glds16c(const void* base, u32 voff, u32 lds) {
  u32 sv;
  asm volatile("s_mov_b32 %0, m0\n\ts_mov_b32 m0, %3\n\ts_nop 0\n\tglobal_load_lds_dwordx4 %1, %2 sc1\n\ts_mov_b32 m0, %0" : "=&s"(sv) : "v"(voff), "s"(base), "s"(lds) : "memory");
}
template <int N> DEVI void wait_vm() { asm volatile("s_waitcnt vmcnt(%0)" ::"i"(N) : "memory"); }
template <int N> DEVI void wait_vm_bar() { asm volatile("s_waitcnt vmcnt(%0) lgkmcnt(0)\n\ts_barrier" ::"i"(N) : "memory"); }
template <int N> DEVI void vm_bar() { asm volatile("s_waitcnt vmcnt(%0)\n\ts_barrier" ::"i"(N) : "memory"); }
DEVI void raw_bar() { asm volatile("s_barrier" ::: "memory"); }
DEVI void lds_bar() { asm volatile("s_waitcnt lgkmcnt(0)\n\ts_barrier" ::: "memory"); }
DEVI u32 uni(u32 v) { return __builtin_amdgcn_readfirstlane(v); }
DEVI int fresh_tid() { int t = threadIdx.x; asm volatile("" : "+v"(t)); return t; }
DEVI u4v swap_pack(u32 a0, u32 a1, u32 b0, u32 b1) {
  auto r0 = __builtin_amdgcn_permlane32_swap(a0, b0, false, false);
  auto r1 = __builtin_amdgcn_permlane32_swap(a1, b1, false, false);
  u4v o = {r0[0], r1[0], r0[1], r1[1]};
  return o;
}
template <int CTRL> DEVI float dpp(float v) { return __builtin_bit_cast(float, __builtin_amdgcn_update_dpp(0, __builtin_bit_cast(int, v), CTRL, 0xf, 0xf, true)); }
DEVI float xor16(float v) { auto r = __builtin_amdgcn_permlane16_swap(__builtin_bit_cast(u32, v), __builtin_bit_cast(u32, v), false, false); return __builtin_bit_cast(float, (threadIdx.x & 16) ? r[0] : r[1]); }
DEVI float xor32(float v) { auto r = __builtin_amdgcn_permlane32_swap(__builtin_bit_cast(u32, v), __builtin_bit_cast(u32, v), false, false); return __builtin_bit_cast(float, (threadIdx.x & 32) ? r[0] : r[1]); }
DEVI float nmax(float a, float b) { return (a > b || a != a) ? a : b; }
DEVI float max16(float v) {
  v = nmax(v, dpp<0xB1>(v)); v = nmax(v, dpp<0x4E>(v)); v = nmax(v, dpp<0x141>(v)); return nmax(v, dpp<0x140>(v));
}
DEVI float max64(float v) { v = max16(v); v = nmax(v, xor16(v)); return nmax(v, xor32(v)); }
DEVI int swap23(int k) { return (k & ~12) | ((k & 4) << 1) | ((k & 8) >> 1); }
DEVI int s_of_p(int p) { return 64 * (p & 31) + (p >> 5); }

typedef __amdgpu_buffer_rsrc_t rsrc_t;
constexpr int SC1 = 16;
DEVI rsrc_t ws_rsrc(const char* ws) { return __builtin_amdgcn_make_buffer_rsrc((void*)ws, 0, 0x1000000, 0x00020000); }
DEVI void st128c(rsrc_t r, u32 off, u4v v) { __builtin_amdgcn_raw_buffer_store_b128(v, r, off, 0, SC1); }
DEVI void st64c(rsrc_t r, u32 off, u2v v) { __builtin_amdgcn_raw_buffer_store_b64(v, r, off, 0, SC1); }
DEVI void st32c(rsrc_t r, u32 off, u32 v) { __builtin_amdgcn_raw_buffer_store_b32(v, r, off, 0, SC1); }
DEVI u4v ld128c(rsrc_t r, u32 off) { return __builtin_amdgcn_raw_buffer_load_b128(r, off, 0, SC1); }
DEVI u32 ld32c(rsrc_t r, u32 off) { return __builtin_amdgcn_raw_buffer_load_b32(r, off, 0, SC1); }

struct Params {
  const u16 *x, *wq, *wk, *wv, *wo, *wr, *wi, *gw;
  u16* out;
  char* ws;
};

#define STAMP_DECL()
#define STMP(n)
#define STAMP_ARG
#define STAMP_PASS
#define STAMP_END(P, K)

DEVI u64 read_epoch(const Params& P) { return __hip_atomic_load((GAS u64*)(P.ws + WS_FL + FL_E), __ATOMIC_RELAXED, __HIP_MEMORY_SCOPE_AGENT); }
DEVI u64 tag_of(u64 e) { return (((u64)uni((u32)(e >> 32)) << 32) | uni((u32)e)) + 1; }
DEVI void put_flag(const Params& P, u32 off, u64 tag) {
  __hip_atomic_store((GAS u64*)(P.ws + WS_FL + off), tag, __ATOMIC_RELAXED, __HIP_MEMORY_SCOPE_AGENT);
}
DEVI void publish(const Params& P, u32 off, u64 tag) {
  asm volatile("s_waitcnt vmcnt(0) lgkmcnt(0)\n\ts_barrier" ::: "memory");
  if (threadIdx.x == 0) put_flag(P, off, tag);
}
template <int N> DEVI bool test_flags(const Params& P, u32 off, u64 tag, int lane) {
  const GAS u64* f = (const GAS u64*)(P.ws + WS_FL + off);
  bool ok = true;
#pragma unroll
  for (int i = 0; i < (N + 63) / 64; i++) {
    const int idx = N >= 64 ? lane + 64 * i : (lane & (N - 1));
    ok &= __hip_atomic_load(f + idx, __ATOMIC_RELAXED, __HIP_MEMORY_SCOPE_AGENT) == tag;
  }
  return __all(ok);
}
#define PSLEEP 2
template <int N, int SLEEP = PSLEEP> DEVI void spin_flags(const Params& P, u32 off, u64 tag, int lane) {
  for (int it = 0; it < (1 << 20); it++) {
    if (test_flags<N>(P, off, tag, lane)) break;
    __builtin_amdgcn_s_sleep(SLEEP);
  }
}
template <int N, int SLEEP = PSLEEP> DEVI void wait_flags(const Params& P, u32 off, u64 tag) {
  if (uni(threadIdx.x >> 6) == 0) spin_flags<N, SLEEP>(P, off, tag, threadIdx.x & 63);
  asm volatile("s_waitcnt vmcnt(0) lgkmcnt(0)\n\ts_barrier" ::: "memory");
}

constexpr int CHUNK = 8 * KVT;
constexpr int KV_BASE = LDS_BYTES - 3 * CHUNK;
#define SA_T 7
#define HINT_CHUNK 7
#define HINT_SLEEP 40
constexpr float kBound2 = 48.f * 48.f;

DEVI void kv_fetch(const char* src, u32 dst, int lw, int lane) {
#pragma unroll
  for (int j = 0; j < 5; j++) glds16c(src + (lw + 4 * j) * 1024, lane * 16, dst + (lw + 4 * j) * 1024);
  if (lw == 0) glds16c(src + 20 * 1024, lane * 16, dst + 20 * 1024);
}
DEVI void kv_chunk0_4(const char* kvg, u32 lds0, int lw, int lane) { kv_fetch(kvg, lds0 + KV_BASE + 2 * CHUNK, lw, lane); }
template <bool FUSED>
DEVI void kv_loader(const Params& P, const char* kvg, u32 lds0, int lw, int lane, int wgi, u64 tag STAMP_ARG) {
  for (int c = 0; c < 8; c++) {
    if (c < 7) kv_fetch(kvg + (c + 1) * CHUNK, lds0 + KV_BASE + (c % 3) * CHUNK, lw, lane);
    if (FUSED && c == HINT_CHUNK && lw == 0) {
      __builtin_amdgcn_s_sleep(HINT_SLEEP);
      if (lane == 0) put_flag(P, FL_SB + wgi * 8, tag);
    }
    wait_vm_bar<0>();
  }
  if (FUSED && HINT_CHUNK == 8 && lw == 0 && lane == 0) put_flag(P, FL_SB + wgi * 8, tag);
}

DEVI void attn_loop_safe(char* smem, int lane, u4v qa0, u2v qb0, u4v qa1, u2v qb1, f16v& o0, f16v& o1) {
  const int r = lane & 31, h = lane >> 5;
  const int koff = lane * 16, k1off = 1024 + r * 8;
  const int voff = 1280 + h * 352 + (r <= 20 ? r : 21) * 16;
  float m0 = -__builtin_inff(), m1 = -__builtin_inff();
  auto one = [&](u4v ka, u2v kb, u4v v0, u4v v1, u4v qa, u2v qb, float& mrun, f16v& o) {
    f16v s = {};
    s = mfma32(ka, qa, s);
    s = mfma32k8(kb, qb, s);
    float mx = s[0];
#pragma unroll
    for (int i = 1; i < 16; i++) mx = fmaxf(mx, s[i]);
    mx = fmaxf(mx, __shfl_xor(mx, 32));
    const float mn = fmaxf(mrun, mx), al = __builtin_amdgcn_exp2f(mrun - mn);
    mrun = mn;
    u4v p0, p1;
#pragma unroll
    for (int i = 0; i < 16; i++) o[i] *= al;
#pragma unroll
    for (int i = 0; i < 4; i++) {
      p0[i] = pk2(__builtin_amdgcn_exp2f(s[2 * i] - mn), __builtin_amdgcn_exp2f(s[2 * i + 1] - mn));
      p1[i] = pk2(__builtin_amdgcn_exp2f(s[8 + 2 * i] - mn), __builtin_amdgcn_exp2f(s[9 + 2 * i] - mn));
    }
    o = mfma32(v0, p0, o);
    o = mfma32(v1, p1, o);
  };
  for (int c = 0; c < 8; c++) {
    for (int i = 0; i < 8; i++) {
      if (i == 7) lds_bar();
      const char* p = smem + KV_BASE + ((c + 2) % 3) * CHUNK + i * KVT;
      u4v ka = lds128(p + koff), v0 = lds128(p + voff), v1 = lds128(p + voff + 704);
      u2v kb = lds64(p + k1off);
      one(ka, kb, v0, v1, qa0, qb0, m0, o0);
      one(ka, kb, v0, v1, qa1, qb1, m1, o1);
    }
  }
}

DEVI void attn_loop_fast(u32 lds0, int lane, u4v qa0, u2v qb0, u4v qa1, u2v qb1, f16v& o0, f16v& o1) {
  const int r = lane & 31, h = lane >> 5;
  const u32 base = lds0 + KV_BASE;
  const u32 kb = base + lane * 16, k1b = base + 1024 + r * 8, vb = base + 1280 + h * 352 + (r <= 20 ? r : 21) * 16;
  u32 s_c;
  asm volatile(R"ASM(
.macro EXP r
  v_exp_f32 v[\r], v[\r]
.endm
.macro CVT d, a
  v_cvt_pk_bf16_f32 v[72+\d], v[\a], v[\a+1]
.endm
; tile t, query tile A: scores B of tile t are produced; K(t+1), V(t) keys 16..31 and V(t+1) keys 0..15 are fetched
.macro STEPA cur, nxt, v0c, v0n, bar
  .if \bar
  s_barrier
  .endif
  v_mfma_f32_32x32x16_bf16 v[56:71], v[80:83], %[qa1], 0
  ds_read_b128 v[80:83], %[kb] offset:\nxt
  EXP 40
  EXP 41
  EXP 42
  EXP 43
  v_mfma_f32_32x32x16_bf16 %[o1], v[90:93], v[76:79], %[o1]
  ds_read_b128 v[90:93], %[vb] offset:\cur+704
  EXP 44
  CVT 0, 40
  EXP 45
  CVT 1, 42
  EXP 46
  v_mfma_f32_32x32x8_bf16 v[56:71], v[84:85], %[qb1], v[56:71]
  ds_read_b64 v[84:85], %[k1b] offset:\nxt
  EXP 47
  EXP 48
  CVT 2, 44
  EXP 49
  CVT 3, 46
  EXP 50
  EXP 51
  v_mfma_f32_32x32x16_bf16 %[o0], v[\v0c:\v0c+3], v[72:75], %[o0]
  ds_read_b128 v[\v0n:\v0n+3], %[vb] offset:\nxt
  EXP 52
  EXP 53
  CVT 4, 48
  EXP 54
  CVT 5, 50
  EXP 55
  CVT 6, 52
  s_waitcnt lgkmcnt(1)
  CVT 7, 54
.endm
; tile t, query tile B: scores A of tile t+1 are produced
.macro STEPB v0c
  v_mfma_f32_32x32x16_bf16 v[40:55], v[80:83], %[qa0], 0
  EXP 56
  EXP 57
  EXP 58
  EXP 59
  v_mfma_f32_32x32x16_bf16 %[o0], v[90:93], v[76:79], %[o0]
  EXP 60
  CVT 0, 56
  EXP 61
  CVT 1, 58
  EXP 62
  v_mfma_f32_32x32x8_bf16 v[40:55], v[84:85], %[qb0], v[40:55]
  EXP 63
  EXP 64
  CVT 2, 60
  EXP 65
  CVT 3, 62
  EXP 66
  EXP 67
  v_mfma_f32_32x32x16_bf16 %[o1], v[\v0c:\v0c+3], v[72:75], %[o1]
  EXP 68
  EXP 69
  CVT 4, 64
  EXP 70
  CVT 5, 66
  EXP 71
  CVT 6, 68
  s_waitcnt lgkmcnt(0)
  CVT 7, 70
.endm
.macro PAIR cur, nxt, v0c, v0n, bar
  STEPA \cur, \nxt, \v0c, \v0n, \bar
  STEPB \v0c
.endm
; one chunk of 8 key tiles in the ring slot at \s; the next chunk sits at \n
.macro CHUNK s, n
  PAIR \s+0*2688, \s+1*2688, 86, 94, 0
  PAIR \s+1*2688, \s+2*2688, 94, 86, 0
  PAIR \s+2*2688, \s+3*2688, 86, 94, 0
  PAIR \s+3*2688, \s+4*2688, 94, 86, 0
  PAIR \s+4*2688, \s+5*2688, 86, 94, 0
  PAIR \s+5*2688, \s+6*2688, 94, 86, 0
  PAIR \s+6*2688, \s+7*2688, 86, 94, 0
  PAIR \s+7*2688, \n, 94, 86, 1
.endm
  ; chunk c sits in slot (c+2)%3
  ds_read_b128 v[80:83], %[kb] offset:43008
  ds_read_b64 v[84:85], %[k1b] offset:43008
  ds_read_b128 v[86:89], %[vb] offset:43008
  v_mov_b32 v90, 0
  v_mov_b32 v91, 0
  v_mov_b32 v92, 0
  v_mov_b32 v93, 0
  v_mov_b32 v76, 0
  v_mov_b32 v77, 0
  v_mov_b32 v78, 0
  v_mov_b32 v79, 0
  s_mov_b32 %[sc_], 0
  s_waitcnt lgkmcnt(0)
  v_mfma_f32_32x32x16_bf16 v[40:55], v[80:83], %[qa0], 0
  v_mfma_f32_32x32x8_bf16 v[40:55], v[84:85], %[qb0], v[40:55]
  s_nop 15
  s_nop 7
3:
  CHUNK 43008, 0
  CHUNK 0, 21504
  s_cmp_eq_u32 %[sc_], 2
  s_cbranch_scc1 4f
  CHUNK 21504, 43008
  s_add_u32 %[sc_], %[sc_], 1
  s_branch 3b
4:
  v_mfma_f32_32x32x16_bf16 %[o1], v[90:93], v[76:79], %[o1]
  s_waitcnt lgkmcnt(0)
  s_nop 15
  s_nop 7
.purgem EXP
.purgem CVT
.purgem STEPA
.purgem STEPB
.purgem PAIR
.purgem CHUNK
)ASM"
      : [o0] "+v"(o0), [o1] "+v"(o1), [sc_] "=&s"(s_c)
      : [qa0] "v"(qa0), [qb0] "v"(qb0), [qa1] "v"(qa1), [qb1] "v"(qb1), [kb] "v"(kb), [k1b] "v"(k1b), [vb] "v"(vb)
      : "memory", "scc", "v40", "v41", "v42", "v43", "v44", "v45", "v46", "v47", "v48", "v49", "v50", "v51", "v52", "v53", "v54", "v55",
        "v56", "v57", "v58", "v59", "v60", "v61", "v62", "v63", "v64", "v65", "v66", "v67", "v68", "v69", "v70", "v71", "v72", "v73", "v74", "v75",
        "v76", "v77", "v78", "v79", "v80", "v81", "v82", "v83", "v84", "v85", "v86", "v87", "v88", "v89", "v90", "v91", "v92", "v93",
        "v94", "v95", "v96", "v97");
}

template <bool FUSED>
DEVI void a_wg(const Params& P, char* smem, int mt, int nt, int wgi STAMP_ARG) {
  const int tid = fresh_tid(), lane = tid & 63, w = uni(tid >> 6), r = lane & 31, h = lane >> 5;
  const u32 lds0 = (u32)(uintptr_t)(LAS char*)smem;
  const int p0 = mt * 64;
#define APRIO0 0
  __builtin_amdgcn_s_setprio(APRIO0);
  u64 epoch = 0;
  if (FUSED) epoch = read_epoch(P);
  const char* kvg = P.ws + WS_KV;
  u64 tag = 0;
  constexpr int QST = 18432;
  u4v qa[2]; u2v qb[2];
  float qn;
  auto qproj = [&](auto NPc) {
    constexpr int NP = NPc;
    u32 xoff, voff[NP - 1];
    {
      const int row = w * 8 + (lane >> 3), ch = (lane & 7) ^ ((row >> 1) & 7);
      xoff = (s_of_p(p0 + row) * 640 + ch * 8) * 2;
    }
    const int wp0 = NP == 3 ? 2 * w : w + 2;
#pragma unroll
    for (int j = 0; j < NP - 1; j++) {
      const int o = (wp0 + j) * 1024 + lane * 16, row = o / 160, c = (o % 160) >> 4;
      voff[j] = (row * 640 + 80 * nt + 8 * c) * 2;
    }
    auto xpiece = [&](int t) { glds16((const char*)P.x + t * 128, xoff, lds0 + (t & 3) * QST + w * 1024); };
    auto wpiece = [&](int t, int j) { glds16((const char*)P.wq + t * (64 * 1280), voff[j], lds0 + (t & 3) * QST + 8192 + (wp0 + j) * 1024); };
#pragma unroll
    for (int t = 0; t < 3; t++) {
      xpiece(t);
#pragma unroll
      for (int j = 0; j < NP - 1; j++) wpiece(t, j);
    }
    int xo[4];
#pragma unroll
    for (int ks = 0; ks < 4; ks++) xo[ks] = (w >> 2) * 4096 + r * 128 + (((2 * ks + h) ^ ((r >> 1) & 7)) << 4);
    const int woff = 8192 + (8 * h + ((lane & 15) >> 2)) * 160 + 40 * (w & 3) + ((lane >> 4) & 1) * 32 + (lane & 3) * 8;
    struct Fr { u4v x, wf; };
    auto rd = [&](const char* sl, int ks) {
      Fr f;
      f.x = lds128(sl + xo[ks]);
      const char* a = sl + woff + ks * 16 * 160;
      f.wf = cat(ldstr(a), ldstr(a + 4 * 160));
      return f;
    };
    f16v q = {};
    STMP(8);
    vm_bar<2 * NP>();
    STMP(9);
    if (FUSED) tag = tag_of(epoch);
    Fr fa = rd(smem, 0), fb = rd(smem, 1);
    auto stage = [&](int t, auto DMA, auto&& wait) {
      const char* sl = smem + (t & 3) * QST;
      const char* sl1 = smem + ((t + 1) & 3) * QST;
      q = mfma32(fa.wf, fa.x, q); fa = rd(sl, 2);
      if constexpr (DMA) xpiece(t + 3);
      __builtin_amdgcn_sched_barrier(0);
      q = mfma32(fb.wf, fb.x, q); fb = rd(sl, 3);
      if constexpr (DMA) wpiece(t + 3, 0);
      __builtin_amdgcn_sched_barrier(0);
      q = mfma32(fa.wf, fa.x, q);
      if constexpr (DMA && NP == 3) wpiece(t + 3, NP - 2);
      wait();
      fa = rd(sl1, 0);
      __builtin_amdgcn_sched_barrier(0);
      q = mfma32(fb.wf, fb.x, q); fb = rd(sl1, 1);
      __builtin_amdgcn_sched_barrier(0);
    };
#pragma unroll 1
    for (int t = 0; t < 7; t++) {
      stage(t, std::true_type{}, [&] { wait_vm_bar<2 * NP>(); });
      if (FUSED && t == SA_T && w == 4 && lane == 0) put_flag(P, FL_SA + wgi * 8, tag);
    }
    STMP(10);
    u64 fl[4];
    if (FUSED) {
#pragma unroll
      for (int k = 0; k < 4; k++) fl[k] = __hip_atomic_load((const GAS u64*)(P.ws + WS_FL + FL_S1) + lane + 64 * k, __ATOMIC_RELAXED, __HIP_MEMORY_SCOPE_AGENT);
    }
    stage(7, std::false_type{}, [&] { wait_vm_bar<NP + (FUSED ? 4 : 0)>(); });
    const bool got = !FUSED || __all(fl[0] == tag && fl[1] == tag && fl[2] == tag && fl[3] == tag);
    STMP(5);
    auto chunk0 = [&] {
#pragma unroll
      for (int j = 0; j < 2; j++) glds16c(kvg + (w + 8 * j) * 1024, lane * 16, lds0 + KV_BASE + 2 * CHUNK + (w + 8 * j) * 1024);
      if (w < 5) glds16c(kvg + (w + 16) * 1024, lane * 16, lds0 + KV_BASE + 2 * CHUNK + (w + 16) * 1024);
    };
    u32 kmr[4] = {};
    auto km_load = [&] {
      if (w == 5) {
        const rsrc_t r5 = ws_rsrc(P.ws);
#pragma unroll
        for (int k = 0; k < 4; k++) kmr[k] = ld32c(r5, WS_KM + k * 256 + lane * 4);
      }
    };
    if (got) { chunk0(); km_load(); }
    stage(8, std::false_type{}, [&] { if (FUSED) lds_bar(); else wait_vm_bar<2>(); });
    stage(9, std::false_type{}, [&] { lds_bar(); });
    STMP(6);
    if (FUSED && SA_T > 6 && w == 4 && lane == 0) put_flag(P, FL_SA + wgi * 8, tag);
    if (!got) { spin_flags<256, 1>(P, FL_S1, tag, lane); chunk0(); km_load(); }
    const float qs = 0.22360679774997896f * 1.4426950408889634f;
    float n = 0.f;
#pragma unroll
    for (int j = 0; j < 12; j++) { q[j] *= qs; if (j < 8 || h == 0) n += q[j] * q[j]; }
    qa[0] = u4v{pk2(q[0], q[1]), pk2(q[2], q[3]), pk2(q[4], q[5]), pk2(q[6], q[7])};
    qb[0] = h ? u2v{0u, 0u} : u2v{pk2(q[8], q[9]), pk2(q[10], q[11])};
    qn = n + xor32(n);
    if (w >= 4) {
      char* qx = smem + 1024 + (w & 3) * (7 * 256) + lane * 4;
#pragma unroll
      for (int j = 0; j < 4; j++) *(LAS u32*)(LAS char*)(qx + j * 256) = qa[0][j];
#pragma unroll
      for (int j = 0; j < 2; j++) *(LAS u32*)(LAS char*)(qx + (4 + j) * 256) = qb[0][j];
      *(LAS float*)(LAS char*)(qx + 6 * 256) = qn;
      if (w == 5) {
        const float km0 = nmax(__builtin_bit_cast(float, kmr[0]), __builtin_bit_cast(float, kmr[1]));
        const float km1 = nmax(__builtin_bit_cast(float, kmr[2]), __builtin_bit_cast(float, kmr[3]));
        const float kmx = max64(km0) + max64(km1);
        if (lane == 0) *(LAS float*)(LAS char*)smem = kmx;
      }
    }
  };
  if (w < 2) qproj(std::integral_constant<int, 3>{}); else qproj(std::integral_constant<int, 2>{});
  STMP(1);
  lds_bar();
  STMP(11);
  if (w >= 4) { wait_vm_bar<0>(); STMP(2); kv_loader<FUSED>(P, kvg, lds0, w - 4, lane, wgi, tag STAMP_PASS); return; }
  const rsrc_t rs = ws_rsrc(P.ws);
  const float km = *(const LAS float*)(const LAS char*)smem;
  {
    const char* qx = smem + 1024 + w * (7 * 256) + lane * 4;
#pragma unroll
    for (int j = 0; j < 4; j++) qa[1][j] = *(const LAS u32*)(const LAS char*)(qx + j * 256);
#pragma unroll
    for (int j = 0; j < 2; j++) qb[1][j] = *(const LAS u32*)(const LAS char*)(qx + (4 + j) * 256);
    qn = nmax(qn, *(const LAS float*)(const LAS char*)(qx + 6 * 256));
  }
  const bool fast = __all(qn * km * 1.05f <= kBound2);
#define APRIO 0
  __builtin_amdgcn_s_setprio(APRIO);
  f16v o0 = {}, o1 = {};
  wait_vm_bar<0>();
  STMP(2);
  if (fast) attn_loop_fast(lds0, lane, qa[0], qb[0], qa[1], qb[1], o0, o1);
  else attn_loop_safe(smem, lane, qa[0], qb[0], qa[1], qb[1], o0, o1);
  STMP(3);
  __builtin_amdgcn_s_setprio(3);
#pragma unroll
  for (int i = 0; i < 2; i++) {
    const f16v& o = i ? o1 : o0;
    const float up = xor32(o[8]);
    const float sc = __builtin_amdgcn_rcpf(h ? o[8] : up);
    char* dst = smem + (32 * i + r) * 176 + (20 * w + 4 * h) * 2;
    sts64(dst, u2v{pk2(o[0] * sc, o[1] * sc), pk2(o[2] * sc, o[3] * sc)});
    sts64(dst + 16, u2v{pk2(o[4] * sc, o[5] * sc), pk2(o[6] * sc, o[7] * sc)});
    if (h == 0) sts64(dst + 32, u2v{pk2(o[8] * sc, o[9] * sc), pk2(o[10] * sc, o[11] * sc)});
  }
  STMP(12);
  lds_bar();
  STMP(13);
  {
    u4v v[3];
    u32 o[3];
#pragma unroll
    for (int k = 0; k < 3; k++) {
      const int ch = tid + 256 * k < 640 ? tid + 256 * k : tid, row = ch / 10, c = ch % 10;
      v[k] = lds128(smem + row * 176 + c * 16);
      o[k] = WS_O + ((p0 + row) * 640 + 80 * nt + 8 * c) * 2;
    }
    st128c(rs, o[0], v[0]); st128c(rs, o[1], v[1]);
    if (w < 2) st128c(rs, o[2], v[2]);
  }
  STMP(14);
  if (FUSED) publish(P, FL_S4 + (mt * 8 + nt) * 8, tag_of(epoch));
  STMP(4);
}

DEVI void b1_kv(const Params& P, char* smem, int kg, int part, int idx STAMP_ARG) {
  const int tid = fresh_tid(), lane = tid & 63, w = uni(tid >> 6), l15 = lane & 15, g = lane >> 4;
  const u32 lds0 = (u32)(uintptr_t)(LAS char*)smem;
  constexpr int RW = 20480, RG = RW + 25600;
  const char* wsrc = (const char*)(part ? P.wv : P.wk);
  {
    const int row = (w & 1) * 8 + (lane >> 3), ch = (lane & 7) ^ ((row >> 1) & 7);
    const u32 xo = (s_of_p(16 * kg + row) * 640 + ch * 8) * 2;
    glds16((const char*)P.x + (w >> 1) * 128, xo, lds0 + w * 1024);
    glds16((const char*)P.x + ((w + 8) >> 1) * 128, xo, lds0 + (w + 8) * 1024);
    if (w < 4) glds16((const char*)P.x + ((w + 16) >> 1) * 128, xo, lds0 + (w + 16) * 1024);
    else glds16(wsrc + (w - 4) * 1024, lane * 16, lds0 + (w + 16) * 1024);
    glds16(wsrc + (w + 4) * 1024, lane * 16, lds0 + (w + 24) * 1024);
    glds16(wsrc + (w + 12) * 1024, lane * 16, lds0 + (w + 32) * 1024);
    if (w < 5) glds16(wsrc + (w + 20) * 1024, lane * 16, lds0 + (w + 40) * 1024);
    else if (part && w < 7) { const int e = (w - 5) * 512 + lane * 8; glds16(P.gw, (e > 632 ? 632 : e) * 2, lds0 + (w + 40) * 1024); }
  }
  STMP(13);
  u32 tv = 0;
  if (w == 3 || w == 4) {
    const int t = ((w - 3) * 64 + lane) % 100;
    tv = *(const GAS u32*)((const GAS char*)P.wq + (20 * idx + t / 5) * 1280 + part * 640 + (t % 5) * 128);
    wait_vm_bar<1>();
  } else
  wait_vm_bar<0>();
  STMP(14);
  const rsrc_t rs = ws_rsrc(P.ws);
  const int s = kg & 1;
  const u32 tile = WS_KV + (kg >> 1) * KVT;
  if (w < 2 + part) {
    const char* xa = smem + l15 * 128;
    const int sw = (l15 >> 1) & 7;
    const char* aw = smem + RW + (8 * g + (l15 >> 2)) * 40 + (lane & 3) * 8 + w * 32;
    f4v a0 = {}, a1 = {};
    auto run = [&](auto GATE, auto KPART) {
      u4v xf[2][4], wf[2][4];
      auto rd = [&](int b) {
#pragma unroll
        for (int e = 0; e < 4; e++) {
          const int ks = 4 * b + e;
          xf[b & 1][e] = lds128(xa + (ks >> 1) * 2048 + (((4 * (ks & 1) + g) ^ sw) << 4));
          if constexpr (GATE) wf[b & 1][e] = lds128(smem + RG + (32 * ks + 8 * g) * 2);
          else wf[b & 1][e] = cat(ldstr(aw + ks * 32 * 40), ldstr(aw + ks * 32 * 40 + 160));
        }
      };
      rd(0);
#pragma unroll
      for (int b = 0; b < 5; b++) {
        if (b < 4) rd(b + 1);
        __builtin_amdgcn_sched_barrier(0);
#pragma unroll
        for (int e = 0; e < 4; e++) {
          f4v& a = (e & 1) ? a1 : a0;
          a = KPART ? mfma16(wf[b & 1][e], xf[b & 1][e], a) : mfma16(xf[b & 1][e], wf[b & 1][e], a);
        }
        __builtin_amdgcn_sched_barrier(0);
      }
    };
    if (part == 0) run(std::false_type{}, std::true_type{});
    else if (w < 2) run(std::false_type{}, std::false_type{});
    else run(std::true_type{}, std::false_type{});
    const f4v a = a0 + a1;
    STMP(15);
    if (part == 0) {
      const u2v v = {pk2(a[0], a[1]), pk2(a[2], a[3])};
      float n2 = a[0] * a[0] + a[1] * a[1] + a[2] * a[2] + a[3] * a[3];
      if (w == 0) {
        st64c(rs, tile + ((g & 1) * 32 + 16 * s + l15) * 16 + (g >> 1) * 8, v);
        n2 += xor16(n2); n2 += xor32(n2);
      } else if (g == 0) st64c(rs, tile + 1024 + (16 * s + l15) * 8, v);
      n2 = max16(n2);
      if (lane == 0) st32c(rs, WS_KM + w * 512 + kg * 4, __builtin_bit_cast(u32, n2));
    } else if (w < 2) {
      u2v v = {pk2(a[0], a[1]), pk2(a[2], a[3])};
      if (w == 1 && l15 == 4) v = u2v{0x3f803f80u, 0x3f803f80u};
      if (w == 1 && l15 == 5) v = u2v{0u, 0u};
      if (w == 0 || l15 <= 5) st64c(rs, tile + 1280 + (2 * s + (g & 1)) * 352 + (g >> 1) * 8 + (16 * w + l15) * 16, v);
    } else if (l15 == 0) {
      u4v o;
#pragma unroll
      for (int i = 0; i < 4; i++) o[i] = __builtin_bit_cast(u32, 1.f / (1.f + __expf(-a[i])));
      st128c(rs, WS_G + (16 * kg + 4 * g) * 4, o);
    }
  }
  asm volatile("" ::"v"(tv));
}

struct WtUnit { u4v re, im; };
DEVI WtUnit wt_load(const Params& P, int u, int lane) {
  const int k2 = u / 20, f0 = 16 * (u % 20) + 8 * (lane >> 5), row = 32 * k2 + (lane & 31);
  WtUnit t;
  __builtin_memcpy(&t.re, P.wr + row * 321 + f0, 16);
  __builtin_memcpy(&t.im, P.wi + row * 321 + f0, 16);
  return t;
}
DEVI void wt_store(const Params& P, int u, int lane, const WtUnit& t) {
  const int k2 = u / 20, f0 = 16 * (u % 20) + 8 * (lane >> 5);
  const rsrc_t rs = ws_rsrc(P.ws);
  const u32 o = WS_WT + f0 * 8192 + k2 * 128 + (lane & 31) * 4;
#pragma unroll
  for (int j = 0; j < 4; j++) {
    st32c(rs, o + (2 * j) * 8192, (t.re[j] & 0xffffu) | (t.im[j] << 16));
    st32c(rs, o + (2 * j + 1) * 8192, (t.re[j] >> 16) | (t.im[j] & 0xffff0000u));
  }
}

template <bool COH> DEVI void b_piece(const char* base, u32 vo, int adv, u32 lds0, int w, int t) {
  if (COH) glds16c(base + t * adv, vo, lds0 + (t & 7) * 8192 + w * 1024);
  else glds16(base + t * adv, vo, lds0 + (t & 7) * 8192 + w * 1024);
}
template <int KIND> DEVI u4v b_frag(const char* smem, const int (&ao)[4], int n, int mb) {
  if (KIND == 2) { const char* p = smem + ao[mb] + (n & 15) * 4096; return cat(ldstr(p), ldstr(p + 1024)); }
  return lds128(smem + ao[n & 1] + ((n >> 1) & 7) * 8192 + mb * 2048);
}
template <int N, class F> DEVI void forN(F&& f) { if constexpr (N > 0) { forN<N - 1>(f); f(std::integral_constant<int, N - 1>{}); } }
template <class F> DEVI void call0(F& f) { if constexpr (std::is_invocable_v<F&>) f(); }
template <int J, class F> DEVI void call1(F& f) { if constexpr (std::is_invocable_v<F&, std::integral_constant<int, J>>) f(std::integral_constant<int, J>{}); }
constexpr int b_landed(int n, int npre) {
  const int m1 = npre == 20 ? 3 : -1;
  const int la = n < m1 ? 1 : 2, lap = (n - 1) < m1 ? 1 : 2;
  const int need = (n + la) >> 1, had = n == 0 ? 0 : (n - 1 + lap) >> 1;
  return need > had ? need : had;
}
template <int KIND, bool COH, bool MAIN, int NPRE, int NSIDE, class SIDE>
DEVI void b_gemm_w(const char* smem, u4v (&W)[20], const u4v* tab, f4v (&acc)[4], const int (&ao)[4],
                   const char* base, u32 vo, int adv, u32 lds0, int w, SIDE&& side STAMP_ARG) {
  constexpr int PRE = KIND == 1 ? 8 : 5;
  STMP(16 + 4 * KIND);
#pragma unroll
  for (int t = 0; t < PRE; t++) b_piece<COH>(base, vo, adv, lds0, w, t);
  STMP(17 + 4 * KIND);
  vm_bar<PRE - 1>();
  STMP(18 + 4 * KIND);
  constexpr int N1 = (MAIN && NPRE == 20) ? 3 : -1;
  const int kq = w & 3;
  u4v a[2][4];
  if constexpr (MAIN) {
#pragma unroll
    for (int i = 0; i < (NPRE == 20 ? 1 : 2); i++)
#pragma unroll
      for (int mb = 0; mb < 4; mb++) a[i][mb] = b_frag<KIND>(smem, ao, i, mb);
  } else if (kq < 2) {
#pragma unroll
    for (int mb = 0; mb < 4; mb++) a[0][mb] = kq ? b_frag<KIND>(smem, ao, 1, mb) : b_frag<KIND>(smem, ao, 0, mb);
  }
  forN<20>([&](auto NN) {
    constexpr int n = NN, T = n >> 1;
    constexpr int M1 = NPRE == 20 ? 3 : -1;
    constexpr int LA = n < M1 ? 1 : 2, LAP = (n - 1) < M1 ? 1 : 2;
    constexpr int need = (n + LA) >> 1, had = n == 0 ? 0 : (n - 1 + LAP) >> 1;
    if constexpr (n == 10) { STMP(19 + 4 * KIND); }
    if constexpr (MAIN && NPRE == 12 && (n == 2 || n == 6)) {
#pragma unroll
      for (int i = 0; i < 4; i++) W[(n == 2 ? 12 : 16) + i] = tab[((n == 2 ? 12 : 16) + i) * 64];
    }
    if constexpr (PRE == 5 && !(n & 1) && n < 10) b_piece<COH>(base, vo, adv, lds0, w, 5 + T);
    if constexpr (PRE == 8 && (n == 4 || n == 6)) b_piece<COH>(base, vo, adv, lds0, w, n == 4 ? 8 : 9);
    if constexpr (!MAIN && n == 8) call0(side);
    if constexpr (MAIN && NSIDE >= 0 && n == 12) call0(side);
    if constexpr (MAIN && NSIDE < 0 && n >= 10 && n <= 15) call1<n - 10>(side);
    if constexpr (need > had && need <= 9) {
      constexpr int issued = PRE == 5 ? 5 + (T + 1 < 5 ? T + 1 : 5) : 8 + (n >= 4 ? 1 : 0) + (n >= 6 ? 1 : 0);
      constexpr int wy = MAIN ? (NPRE == 12 ? ((n >= 2 && need <= 5) ? 4 : 0) + ((n >= 6 && need <= 7) ? 4 : 0) : 0) + (NSIDE >= 0 ? (n >= 12 ? NSIDE : 0) : (n >= 10 ? 2 * ((n < 15 ? n : 15) - 9) : 0)) : (n >= 8 ? NSIDE : 0);
      vm_bar<(issued - need - 1 + wy)>();
    }
    if constexpr (MAIN) {
      constexpr int cur = n <= N1 ? 0 : (n & 1);
#pragma unroll
      for (int mb = 0; mb < 4; mb++) {
        const u4v x = a[cur][mb];
        acc[mb] = KIND == 0 ? mfma16(x, W[n], acc[mb]) : mfma16(W[n], x, acc[mb]);
        if constexpr (n == N1) { a[(n + 1) & 1][mb] = b_frag<KIND>(smem, ao, n + 1, mb); a[n & 1][mb] = b_frag<KIND>(smem, ao, n + 2, mb); }
        else if constexpr (n + LA < 20) a[LA == 1 ? 0 : (n & 1)][mb] = b_frag<KIND>(smem, ao, n + LA, mb);
      }
    } else {
      if ((n & 3) == kq) {
#pragma unroll
        for (int mb = 0; mb < 4; mb++) acc[mb] = KIND == 0 ? mfma16(a[0][mb], W[n >> 2], acc[mb]) : mfma16(W[n >> 2], a[0][mb], acc[mb]);
      }
      constexpr bool now2 = n + 2 < 20 && ((n + 2) >> 1) <= b_landed(n, NPRE);
      constexpr bool late1 = n >= 1 && ((n + 1) >> 1) > b_landed(n - 1, NPRE);
      if constexpr (late1) {
        if (((n + 1) & 3) == kq) {
#pragma unroll
          for (int mb = 0; mb < 4; mb++) a[0][mb] = b_frag<KIND>(smem, ao, n + 1, mb);
        }
      }
      if constexpr (now2) {
        if (((n + 2) & 3) == kq) {
#pragma unroll
          for (int mb = 0; mb < 4; mb++) a[0][mb] = b_frag<KIND>(smem, ao, n + 2, mb);
        }
      }
    }
    __builtin_amdgcn_sched_barrier(0);
  });
}
template <bool MAIN, class SIDE>
DEVI void b_gemm_res(const char* smem, const u4v (&W)[20], f4v (&acc)[4], const int (&ao)[4], int w, SIDE&& side) {
  auto frag = [&](int n, int mb) { const char* p = smem + ao[mb] + n * 4096; return cat(ldstr(p), ldstr(p + 1024)); };
  if constexpr (MAIN) {
    u4v a[2][4];
#pragma unroll
    for (int mb = 0; mb < 4; mb++) a[0][mb] = frag(0, mb);
    forN<20>([&](auto NN) {
      constexpr int n = NN, cur = n <= 3 ? 0 : (n & 1);
#pragma unroll
      for (int mb = 0; mb < 4; mb++) {
        acc[mb] = mfma16(W[n], a[cur][mb], acc[mb]);
        if constexpr (n < 3) a[0][mb] = frag(n + 1, mb);
        else if constexpr (n == 3) { a[0][mb] = frag(4, mb); a[1][mb] = frag(5, mb); }
        else if constexpr (n + 2 < 20) a[n & 1][mb] = frag(n + 2, mb);
      }
      if constexpr (n >= 6) call1<n - 6>(side);
      __builtin_amdgcn_sched_barrier(0);
    });
  } else {
    const int kq = w & 3;
    const char* sm = smem + kq * 4096;
    auto fr = [&](int j, int mb) { const char* p = sm + ao[mb] + j * 16384; return cat(ldstr(p), ldstr(p + 1024)); };
    u4v a[2][4];
#pragma unroll
    for (int mb = 0; mb < 4; mb++) a[0][mb] = fr(0, mb);
#pragma unroll
    for (int j = 0; j < 5; j++) {
      if (j < 4) {
#pragma unroll
        for (int mb = 0; mb < 4; mb++) a[(j + 1) & 1][mb] = fr(j + 1, mb);
      }
#pragma unroll
      for (int mb = 0; mb < 4; mb++) acc[mb] = mfma16(W[j], a[j & 1][mb], acc[mb]);
      __builtin_amdgcn_sched_barrier(0);
    }
  }
}
template <bool MAIN> DEVI f4v b_reduce(char* xa, const f4v (&acc)[4], int w, int lane) {
  lds_bar();
  const int kq = w & 3;
  if constexpr (!MAIN) {
#pragma unroll
    for (int mb = 0; mb < 4; mb++) *(LAS f4v*)(LAS char*)(xa + (kq * 4 + mb) * 1024 + lane * 16) = acc[mb];
  }
  lds_bar();
  f4v r = {};
  if constexpr (!MAIN) {
#pragma unroll
    for (int q = 0; q < 4; q++) r += *(const LAS f4v*)(const LAS char*)(xa + (q * 4 + kq) * 1024 + lane * 16);
  }
  return r;
}
DEVI u32 rows_off(int p0, bool perm, int w, int lane) {
  const int row = w * 8 + (lane >> 3), ch = (lane & 7) ^ ((row >> 1) & 7);
  return ((perm ? s_of_p(p0 + row) : p0 + row) * 640 + ch * 8) * 2;
}
DEVI void mk_ro(int (&ao)[4], int l15, int g) {
  ao[0] = l15 * 128 + ((g ^ ((l15 >> 1) & 7)) << 4);
  ao[1] = l15 * 128 + (((4 + g) ^ ((l15 >> 1) & 7)) << 4);
  ao[2] = ao[3] = 0;
}

template <bool FUSED>
DEVI void b2_fwd(const Params& P, char* smem, int mt, int nt, int i, u64 tag STAMP_ARG) {
  const int tid = fresh_tid(), lane = tid & 63, w = uni(tid >> 6), l15 = lane & 15, g = lane >> 4;
  const u32 lds0 = (u32)(uintptr_t)(LAS char*)smem;
  const u32 vo = rows_off(mt * 64, true, w, lane);
  f4v acc[4] = {};
  u4v W[20];
  int ao[4];
  mk_ro(ao, l15, g);
  char* stg = smem + 16384;
  auto put = [&](int blk, int mb, const f4v& v) {
    sts64(stg + blk * 2304 + l15 * 144 + (16 * mb + 4 * g) * 2, u2v{pk2(v[0], v[1]), pk2(v[2], v[3])});
  };
  auto go = [&] {
    if (FUSED) wait_flags<1>(P, FL_SA + i * 8, tag);
    STMP(9);
  };
  go();
  if (w < 4) {
    const u4v* tf = (const u4v*)&gTF + (nt * 5 + w) * 1280 + lane;
#pragma unroll
    for (int k = 0; k < 12; k++) W[k] = tf[k * 64];
    b_gemm_w<0, false, true, 12, 0>(smem, W, tf, acc, ao, (const char*)P.x, vo, 128, lds0, w, [] {} STAMP_PASS);
    STMP(11);
    b_reduce<true>(smem, acc, w, lane);
#pragma unroll
    for (int mb = 0; mb < 4; mb++) put(w, mb, acc[mb]);
    lds_bar();
  } else {
    const u4v* tf = (const u4v*)&gTF + (nt * 5 + 4) * 1280 + (w & 3) * 64 + lane;
#pragma unroll
    for (int k = 0; k < 5; k++) W[k] = tf[k * 256];
    const int u0 = 5 * i + 2 * (w > 4 ? w - 5 : 0), u1 = w < 7 ? u0 + 1 : u0;
    WtUnit t0, t1;
    u32 ny = 0, tv = 0;
    auto side = [&] {
      t0 = wt_load(P, u0, lane);
      t1 = wt_load(P, u1, lane);
      const int row = 32 * (i & 63) + (lane & 31);
      ny = (lane >> 5) ? P.wi[row * 321 + 320] : P.wr[row * 321 + 320];
    };
    b_gemm_w<0, false, false, 12, 5>(smem, W, nullptr, acc, ao, (const char*)P.x, vo, 128, lds0, w, side STAMP_PASS);
    asm volatile("" ::"v"(tv));
    const f4v r = b_reduce<false>(smem, acc, w, lane);
    put(4, w & 3, r);
    if (w > 4) {
      wt_store(P, u0, lane, t0);
      if (w < 7) wt_store(P, u1, lane, t1);
      else if (i < 64) {
        const u32 o = __shfl_xor(ny, 32);
        if (lane < 32) st32c(ws_rsrc(P.ws), WS_WT + 320 * 8192 + i * 128 + lane * 4, ny | (o << 16));
      }
    }
    lds_bar();
  }
  if (w < 5) {
    const rsrc_t rs = ws_rsrc(P.ws);
#pragma unroll
    for (int k = 0; k < 2; k++) {
      const int row = 8 * k + (lane >> 3), c = lane & 7;
      st128c(rs, WS_X1 + ((80 * nt + 16 * w + row) * 2048 + mt * 64 + 8 * c) * 2, lds128(stg + w * 2304 + row * 144 + c * 16));
    }
  }
}

constexpr int CT_A1 = 0, CT_A1C = 4096, CT_A2 = 8192, CT_TW1 = 24576, CT_TW2 = 40960, CT_XCH = 57344;
static_assert(sizeof(ColTabD) == CT_XCH, "table image");
DEVI u4v pk8(const f16v& a, int s) {
  u4v v = {pk2(a[8 * s], a[8 * s + 1]), pk2(a[8 * s + 2], a[8 * s + 3]), pk2(a[8 * s + 4], a[8 * s + 5]), pk2(a[8 * s + 6], a[8 * s + 7])};
  return v;
}
DEVI u4v neg8(u4v v) { return v ^ u4v{0x80008000u, 0x80008000u, 0x80008000u, 0x80008000u}; }
DEVI void column_tables(char* smem) {
  const int tid = fresh_tid(), lane = tid & 63, w = uni(tid >> 6);
  const u32 lds0 = (u32)(uintptr_t)(LAS char*)smem;
#pragma unroll
  for (int j = 0; j < 7; j++) glds16((const char*)&gCT + (w + 8 * j) * 1024, lane * 16, lds0 + (w + 8 * j) * 1024);
}
DEVI f2v lo2(f4v c) { return f2v{c[0], c[1]}; }
DEVI f2v hi2(f4v c) { return f2v{c[2], c[3]}; }
template <int SG> DEVI void twiddle(f16v& re, f16v& im, const char* tab) {
#pragma unroll
  for (int hq = 0; hq < 2; hq++) {
    f4v c[4];
#pragma unroll
    for (int q = 0; q < 4; q++) c[q] = __builtin_bit_cast(f4v, lds128(tab + (4 * hq + q) * 1024));
#pragma unroll
    for (int q = 0; q < 4; q++) {
      const int k = 2 * (4 * hq + q);
      const f2v a = {re[k], re[k + 1]}, b = {im[k], im[k + 1]}, cc = lo2(c[q]), ss = hi2(c[q]);
      const f2v nr = SG > 0 ? a * cc - b * ss : a * cc + b * ss;
      const f2v ni = SG > 0 ? b * cc + a * ss : b * cc - a * ss;
      re[k] = nr[0]; re[k + 1] = nr[1]; im[k] = ni[0]; im[k + 1] = ni[1];
    }
  }
}
DEVI void column_job(const Params& P, char* smem, int col, int f, int t, int lane) {
  const int r = lane & 31, h = lane >> 5;
  const rsrc_t rs = ws_rsrc(P.ws);
  const int cr = f == 0 ? 0 : f == 320 ? 1 : 2 * f;
  const bool real_in = (f == 0 || f == 320);
  const u32 zo = WS_X1 + (cr * 2048 + (32 * t + r) * 32 + 8 * h) * 2;
  u4v Zr[2], Zi[2];
#pragma unroll
  for (int ks = 0; ks < 2; ks++) {
    Zr[ks] = ld128c(rs, zo + 32 * ks);
    Zi[ks] = ld128c(rs, zo + 4096 + 32 * ks);
  }
  u32 wv[16];
#pragma unroll
  for (int i = 0; i < 16; i++) wv[i] = ld32c(rs, WS_WT + f * 8192 + (32 * t + (i & 3) + 8 * (i >> 2) + 4 * h) * 128 + r * 4);
  if (real_in) { Zi[0] = u4v{0u, 0u, 0u, 0u}; Zi[1] = Zi[0]; }
  const char* tb = smem + lane * 16;
  char* xch = smem + CT_XCH + col * 8192 + lane * 16;
  f16v re = {}, im = {};
#pragma unroll
  for (int ks = 0; ks < 2; ks++) {
    u4v c = lds128(tb + CT_A1 + ks * 1024), ns = lds128(tb + CT_A1 + 2048 + ks * 1024);
    re = mfma32(Zr[ks], c, re); re = mfma32(neg8(Zi[ks]), ns, re);
    im = mfma32(Zr[ks], ns, im); im = mfma32(Zi[ks], c, im);
  }
  twiddle<-1>(re, im, tb + CT_TW1 + t * 8192);
  u4v yr[4], yi[4];
  auto exchange = [&]() {
    yr[0] = pk8(re, 0); yr[1] = pk8(re, 1); yi[0] = pk8(im, 0); yi[1] = pk8(im, 1);
    char* mine = xch + t * 4096;
    sts128(mine, yr[0]); sts128(mine + 1024, yr[1]); sts128(mine + 2048, yi[0]); sts128(mine + 3072, yi[1]);
    lds_bar();
    const char* oth = xch + (t ^ 1) * 4096;
    yr[2] = lds128(oth); yr[3] = lds128(oth + 1024); yi[2] = lds128(oth + 2048); yi[3] = lds128(oth + 3072);
  };
  const char* a2 = tb + CT_A2 + t * 8192;
  const int ko = t * 4096;
  exchange();
  re = f16v{}; im = f16v{};
#pragma unroll
  for (int kk = 0; kk < 4; kk++) {
    u4v c = lds128(a2 + ((kk * 2048) ^ ko)), ns = lds128(a2 + ((kk * 2048) ^ ko) + 1024);
    re = mfma32(c, yr[kk], re); re = mfma32(ns, neg8(yi[kk]), re);
    im = mfma32(ns, yr[kk], im); im = mfma32(c, yi[kk], im);
  }
  const float wsc = real_in ? 0.5f : 1.f;
#pragma unroll
  for (int i = 0; i < 16; i += 2) {
    const f2v wr_ = f2v{bflo(wv[i]), bflo(wv[i + 1])} * wsc, wi_ = f2v{bfhi(wv[i]), bfhi(wv[i + 1])} * wsc;
    const f2v a = {re[i], re[i + 1]}, b = {im[i], im[i + 1]};
    const f2v nr = a * wr_ - b * wi_, ni = a * wi_ + b * wr_;
    re[i] = nr[0]; re[i + 1] = nr[1]; im[i] = ni[0]; im[i + 1] = ni[1];
  }
  lds_bar();
  exchange();
  re = f16v{}; im = f16v{};
#pragma unroll
  for (int kk = 0; kk < 4; kk++) {
    u4v c = lds128(a2 + ((kk * 2048) ^ ko)), ns = lds128(a2 + ((kk * 2048) ^ ko) + 1024);
    re = mfma32(yr[kk], c, re); re = mfma32(yi[kk], ns, re);
    im = mfma32(neg8(yr[kk]), ns, im); im = mfma32(yi[kk], c, im);
  }
  twiddle<1>(re, im, tb + CT_TW2 + t * 8192);
  u4v gr0 = pk8(re, 0), gr1 = pk8(re, 1), gi0 = pk8(im, 0), gi1 = pk8(im, 1);
  re = f16v{}; im = f16v{};
#pragma unroll
  for (int ks = 0; ks < 2; ks++) {
    u4v c = lds128(tb + CT_A1C + ks * 1024), ns = lds128(tb + CT_A1C + 2048 + ks * 1024);
    u4v gr = ks ? gr1 : gr0, gi = ks ? gi1 : gi0;
    re = mfma32(c, gr, re); re = mfma32(ns, gi, re);
    im = mfma32(ns, neg8(gr), im); im = mfma32(c, gi, im);
  }
  const u32 dst = WS_X2 + (cr * 2048 + (32 * t + r) * 32 + 8 * h) * 2;
#pragma unroll
  for (int g = 0; g < 4; g += 2) {
    st128c(rs, dst + 16 * g, swap_pack(pk2(re[4 * g], re[4 * g + 1]), pk2(re[4 * g + 2], re[4 * g + 3]),
                                       pk2(re[4 * g + 4], re[4 * g + 5]), pk2(re[4 * g + 6], re[4 * g + 7])));
    u4v vi = swap_pack(pk2(im[4 * g], im[4 * g + 1]), pk2(im[4 * g + 2], im[4 * g + 3]),
                       pk2(im[4 * g + 4], im[4 * g + 5]), pk2(im[4 * g + 6], im[4 * g + 7]));
    if (!real_in) st128c(rs, dst + 4096 + 16 * g, vi);
  }
}
DEVI void wot_job(const Params& P, char* img, int fr, int lane) {
  const int cb = fr / 20, ks = fr % 20;
  glds16((const char*)P.wo + (32 * ks * 640 + 16 * cb) * 2, (lane >> 1) * 1280 + (lane & 1) * 16, (u32)(uintptr_t)(LAS char*)img);
  wait_vm<0>();
  const char* a = img + (8 * (lane >> 4) + ((lane & 15) >> 2)) * 32 + (lane & 3) * 8;
  st128c(ws_rsrc(P.ws), WS_WOT + fr * 1024 + lane * 16, cat(ldstr(a), ldstr(a + 128)));
}
template <bool FUSED>
DEVI void b3_columns(const Params& P, char* smem, int i, u64 tag STAMP_ARG) {
  lds_bar();
  column_tables(smem);
  if (FUSED) wait_flags<256>(P, FL_S2, tag); else wait_vm_bar<0>();
  STMP(12);
  const int tid = fresh_tid(), w = uni(tid >> 6), lane = tid & 63;
  const int f = (w >> 1) == 0 ? i : 256 + i;
  if (w < 4 && f <= 320) column_job(P, smem, w >> 1, f, w & 1, lane);
  else {
    if (w >= 4 && i + 256 * (w - 4) < 800) wot_job(P, smem + CT_XCH + 16384 + (w - 4) * 1024, i + 256 * (w - 4), lane);
    lds_bar(); lds_bar(); lds_bar();
  }
}

template <bool FUSED>
DEVI void b45_out(const Params& P, char* smem, int mt, int nt, int wgi, u64 tag STAMP_ARG) {
  const u32 lds0 = (u32)(uintptr_t)(LAS char*)smem;
  const int p0 = mt * 64, n0 = nt * 80;
  constexpr int STG = 65536;
  const int tid = fresh_tid(), lane = tid & 63, w = uni(tid >> 6), l15 = lane & 15, g = lane >> 4;
  auto synth_off = [&] { const int o = w * 1024 + lane * 16, rho = o >> 7, s16 = (o >> 4) & 7, mbs = (s16 >> 1) ^ ((rho >> 1) & 3);
                         return (u32)((swap23(rho) * 2048 + p0 + mbs * 16 + (s16 & 1) * 8) * 2); };
  auto fetch_b = [&] {
    if (FUSED) {
      wait_flags<256>(P, FL_S3, tag);
    } else lds_bar();
    const u32 vo = synth_off();
#pragma unroll
    for (int t = 0; t < 10; t++) glds16c(P.ws + WS_X2 + t * (64 * 4096), vo, lds0 + t * 8192 + w * 1024);
    if (FUSED) {
      wait_flags<256>(P, FL_S1, tag);
      wait_flags<1>(P, FL_SB + wgi * 8, tag);
    }
    wait_vm_bar<0>();
  };
  auto mk_co = [&](int (&ao)[4]) {
    const int rho0 = 16 * (g >> 1) + 4 * (g & 1) + (l15 >> 2);
#pragma unroll
    for (int mb = 0; mb < 4; mb++) ao[mb] = rho0 * 128 + ((mb ^ ((rho0 >> 1) & 3)) << 5) + (lane & 3) * 8;
  };
  const float sb = 2.f / (float)(S_ * D_);
  const int blk = w < 4 ? w : 4;
  auto stg = [&](int mb) { return smem + STG + (16 * mb + l15) * 176 + (16 * blk + 4 * g) * 2; };
  auto park = [&](int mb, const f4v& v) { sts64(stg(mb), u2v{pk2(v[0] * sb, v[1] * sb), pk2(v[2] * sb, v[3] * sb)}); };
  auto mix = [&](int mb, const f4v& v, float gg) {
    const float og = 1.f - gg;
    const u2v b = lds64(stg(mb));
    sts64(stg(mb), u2v{pk2(gg * v[0] + og * bflo(b[0]), gg * v[1] + og * bfhi(b[0])), pk2(gg * v[2] + og * bflo(b[1]), gg * v[3] + og * bfhi(b[1]))});
  };
  const rsrc_t rs = ws_rsrc(P.ws);
  if (w < 4) {
    u4v W[20], WO[20];
    f4v accb[4] = {}, acca[4] = {};
    int ao[4];
    mk_co(ao);
    const u4v* ti = (const u4v*)&gTI + (nt * 5 + w) * 1280 + lane;
#pragma unroll
    for (int i = 0; i < 20; i++) W[i] = ti[i * 64];
    fetch_b();
    STMP(4);
    __builtin_amdgcn_s_waitcnt(0x0F70);
    const u32 woff = WS_WOT + (nt * 5 + w) * 20480 + lane * 16;
    b_gemm_res<true>(smem, W, accb, ao, w, [&](auto J) { WO[J] = ld128c(rs, woff + J * 1024); });
    STMP(27);
    lds_bar();
#pragma unroll
    for (int mb = 0; mb < 4; mb++) park(mb, accb[mb]);
#pragma unroll
    for (int i = 14; i < 20; i++) WO[i] = ld128c(rs, woff + i * 1024);
    lds_bar();
    STMP(5);
    mk_ro(ao, l15, g);
    const u32 vo = rows_off(p0, false, w, lane);
    if (FUSED) wait_flags<8, 1>(P, FL_S4 + mt * 64, tag); else lds_bar();
    STMP(7);
    __builtin_amdgcn_s_waitcnt(0x0F70);
    float gate[4];
    b_gemm_w<1, true, true, 20, 4>(smem, WO, nullptr, acca, ao, P.ws + WS_O, vo, 128, lds0, w, [&] {
#pragma unroll
      for (int mb = 0; mb < 4; mb++) gate[mb] = __builtin_bit_cast(float, ld32c(rs, WS_G + (p0 + 16 * mb + l15) * 4));
    } STAMP_PASS);
#pragma unroll
    for (int mb = 0; mb < 4; mb++) mix(mb, acca[mb], gate[mb]);
    b_reduce<true>(smem, acca, w, lane);
  } else {
    const int kq = w & 3;
    u4v W[20];
    f4v acc[4] = {};
    int ao[4];
    mk_co(ao);
    const u4v* ti = (const u4v*)&gTI + (nt * 5 + 4) * 1280 + kq * 64 + lane;
#pragma unroll
    for (int i = 0; i < 5; i++) W[i] = ti[i * 256];
    fetch_b();
    __builtin_amdgcn_s_waitcnt(0x0F70);
    b_gemm_res<false>(smem, W, acc, ao, w, [] {});
    const u32 woff = WS_WOT + (nt * 5 + 4) * 20480 + kq * 1024 + lane * 16;
#pragma unroll
    for (int i = 0; i < 5; i++) W[i] = ld128c(rs, woff + i * 4096);
    const float gate = __builtin_bit_cast(float, ld32c(rs, WS_G + (p0 + 16 * kq + l15) * 4));
    park(kq, b_reduce<false>(smem, acc, w, lane));
#pragma unroll
    for (int mb = 0; mb < 4; mb++) acc[mb] = f4v{};
    mk_ro(ao, l15, g);
    const u32 vo = rows_off(p0, false, w, lane);
    if (FUSED) wait_flags<8, 1>(P, FL_S4 + mt * 64, tag); else lds_bar();
    __builtin_amdgcn_s_waitcnt(0x0F70);
    b_gemm_w<1, true, false, 20, 0>(smem, W, nullptr, acc, ao, P.ws + WS_O, vo, 128, lds0, w, [] {} STAMP_PASS);
    mix(kq, b_reduce<false>(smem, acc, w, lane), gate);
  }
  lds_bar();
  {
    const int t2 = fresh_tid();
    u4v v[2];
    u16* d[2];
#pragma unroll
    for (int k = 0; k < 2; k++) {
      const int ch = t2 + NTHR * k < 640 ? t2 + NTHR * k : t2, row = ch / 10, c = ch % 10;
      v[k] = lds128(smem + STG + row * 176 + c * 16);
      d[k] = P.out + s_of_p(p0 + row) * 640 + n0 + 8 * c;
    }
    *(u4v*)d[0] = v[0];
    if (t2 < 128) *(u4v*)d[1] = v[1];
  }
}

template <int PH>
__global__ void __launch_bounds__(NTHR, 4) mega(Params P) {
  extern __shared__ __attribute__((aligned(16))) char smem[];
  STAMP_DECL()
  asm volatile("" ::"s"(P.x), "s"(P.wq), "s"(P.wk), "s"(P.wv), "s"(P.wo), "s"(P.wr), "s"(P.wi), "s"(P.gw), "s"(P.out), "s"(P.ws));
  constexpr bool FUSED = PH == 0;
  const int i = blockIdx.x & 255, xcd = i & 7, idx = i >> 3;
  const int mt = (xcd >> 1) * 8 + (idx >> 2), nt = (xcd & 1) * 4 + (idx & 3);
  if (blockIdx.x >= 256) {
    if (PH == 0 || PH == 2) a_wg<FUSED>(P, smem, mt, nt, i STAMP_PASS);
    STAMP_END(P, PH);
    return;
  }
#define BPRIO1 3
  __builtin_amdgcn_s_setprio(BPRIO1);
  u64 tag = 0, epoch = 0;
  if (FUSED) epoch = read_epoch(P);
  if (PH == 0 || PH == 1) {
    b1_kv(P, smem, (xcd >> 1) * 32 + idx, xcd & 1, idx STAMP_PASS);
    STMP(8);
    if (FUSED) tag = tag_of(epoch);
    if (FUSED) publish(P, FL_S1 + i * 8, tag); else lds_bar();
    STMP(1);
#define BPRIO 0
#define BPRIO2 3
#define BPRIO4 2
    __builtin_amdgcn_s_setprio(BPRIO2);
    b2_fwd<FUSED>(P, smem, mt, nt, i, tag STAMP_PASS);
    if (FUSED) publish(P, FL_S2 + i * 8, tag);
    STMP(2);
  }
  __builtin_amdgcn_s_setprio(BPRIO);
  if (PH == 0 || PH == 2) {
    b3_columns<FUSED>(P, smem, i, tag STAMP_PASS);
    if (FUSED) publish(P, FL_S3 + i * 8, tag);
    STMP(3);
  }
  __builtin_amdgcn_s_setprio(BPRIO4);
  if (FUSED && i == 0 && uni(threadIdx.x >> 6) == 7) {
    spin_flags<256>(P, FL_SA, tag, threadIdx.x & 63);
    if ((threadIdx.x & 63) == 0) put_flag(P, FL_E, tag);
  }
  if (PH == 0 || PH == 3) b45_out<FUSED>(P, smem, mt, nt, i, tag STAMP_PASS);
  STAMP_END(P, PH);
}

extern "C" void kernel_launch(void* const* d_in, const int* in_sizes, int n_in, void* d_out, int out_size, void* d_ws,
                              size_t ws_size, hipStream_t stream) {
  Params P;
  P.x = (const u16*)d_in[0];
  P.wq = (const u16*)d_in[1];
  P.wk = (const u16*)d_in[2];
  P.wv = (const u16*)d_in[3];
  P.wo = (const u16*)d_in[4];
  P.wr = (const u16*)d_in[6];
  P.wi = (const u16*)d_in[7];
  P.gw = (const u16*)d_in[8];
  P.out = (u16*)d_out;
  P.ws = (char*)d_ws;
  (void)hipFuncSetAttribute((const void*)mega<0>, hipFuncAttributeMaxDynamicSharedMemorySize, LDS_BYTES);
  mega<0><<<512, NTHR, LDS_BYTES, stream>>>(P);
}
```

```cpp
#include <hip/hip_runtime.h>
#include <stdint.h>
#include <type_traits>
#include <utility>

typedef unsigned short u16;
typedef unsigned int u32;
typedef unsigned long long u64;
typedef __attribute__((ext_vector_type(8))) __bf16 bf8;
typedef __attribute__((ext_vector_type(4))) short s4;
typedef __attribute__((ext_vector_type(16))) float f16v;
typedef __attribute__((ext_vector_type(4))) float f4v;
typedef __attribute__((ext_vector_type(2))) float f2v;
typedef __attribute__((ext_vector_type(2))) __bf16 bf2;
typedef __attribute__((ext_vector_type(4))) u32 u4v;
typedef __attribute__((ext_vector_type(2))) u32 u2v;
#define LAS __attribute__((address_space(3)))
#define GAS __attribute__((address_space(1)))
#define DEVI __device__ __forceinline__

constexpr int S_ = 2048, D_ = 640;
constexpr int NTHR = 512, LDS_BYTES = 81920;
constexpr int kPollSleep = 2;
constexpr int kHintSleep = 40;

constexpr u32 WS_X1 = 0x000000;
constexpr u32 WS_X2 = 0x280000;
constexpr u32 WS_O = 0x500000;
constexpr u32 WS_WT = 0x780000;
constexpr u32 WS_KV = 0xA10000;
constexpr u32 WS_KM = 0xA3A000;
constexpr u32 WS_G = 0xA3B000;
constexpr u32 WS_FL = 0xA40000;
constexpr u32 WS_WOT = 0xA50000;
constexpr u32 FL_E = 0, FL_S1 = 0x800, FL_S2 = 0x1000, FL_S3 = 0x1800, FL_S4 = 0x2000, FL_SA = 0x2800, FL_SB = 0x3000;
constexpr int KVT = 2688;

constexpr double kPi = 3.14159265358979323846;
constexpr double cx_sin(double x) { double t = x, s = x; for (int i = 1; i < 10; i++) { t *= -x * x / ((2 * i) * (2 * i + 1)); s += t; } return s; }
constexpr double cx_cos(double x) { double t = 1, s = 1; for (int i = 1; i < 10; i++) { t *= -x * x / ((2 * i - 1) * (2 * i)); s += t; } return s; }
constexpr double cos2pi(int m, int N) {
  m %= N; if (m < 0) m += N;
  if (2 * m > N) m = N - m;
  if (4 * m > N) return -cos2pi(N / 2 - m, N);
  if (8 * m > N) return cx_sin(2 * kPi * (N / 4 - m) / N);
  return cx_cos(2 * kPi * m / N);
}
constexpr double sin2pi(int m, int N) { return cos2pi(m + 3 * (N / 4), N); }
constexpr u16 to_bf16(double d) { float f = (float)d; u32 u = __builtin_bit_cast(u32, f); u += 0x7fffu + ((u >> 16) & 1); return (u16)(u >> 16); }
constexpr int crow_c(int r, int h) { return (r & 3) + 8 * (r >> 2) + 4 * h; }
constexpr int kperm_c(int ks, int h, int j) { return 16 * ks + 8 * (j >> 2) + 4 * h + (j & 3); }

struct FragB { u16 v[20][64][8]; };
struct FragAll { FragB c[40]; };
struct A1T { u16 a1[2][2][64][8]; u16 a1c[2][2][64][8]; };
struct A2T { u16 v[4][2][64][8]; };
struct TWT { float v[8][64][4]; };
struct ColTabD { A1T a1; A2T a2[2]; TWT tw1[2]; TWT tw2[2]; };

#if defined(__HIP_DEVICE_COMPILE__) && __HIP_DEVICE_COMPILE__
struct Base640 { u16 c[640]; u16 ns[640]; };
constexpr Base640 mk_base() { Base640 b{}; for (int i = 0; i < 640; i++) { b.c[i] = to_bf16(cos2pi(i, 640)); b.ns[i] = to_bf16(-sin2pi(i, 640)); } return b; }
constexpr Base640 kBase = mk_base();
constexpr u16 Fval(int d, int comp) {
  return comp == 0 ? kBase.c[0] : comp == 1 ? kBase.c[(320 * d) % 640] : (comp & 1) ? kBase.ns[((comp >> 1) * d) % 640] : kBase.c[((comp >> 1) * d) % 640];
}
constexpr FragB mk_frag(int cb, int inv) {
  FragB r{};
  for (int ks = 0; ks < 20; ks++) for (int l = 0; l < 64; l++) for (int j = 0; j < 8; j++) {
    int n = 16 * cb + (l & 15), k = 32 * ks + 8 * (l >> 4) + j;
    r.v[ks][l][j] = inv ? Fval(n, k) : Fval(k, n);
  }
  return r;
}
template <int C, int I> constexpr FragB cFrag = mk_frag(C, I);
template <int INV, int... I> constexpr FragAll mk_fragall(std::integer_sequence<int, I...>) { return FragAll{{cFrag<I, INV>...}}; }
__device__ const FragAll gTF = mk_fragall<0>(std::make_integer_sequence<int, 40>{});
__device__ const FragAll gTI = mk_fragall<1>(std::make_integer_sequence<int, 40>{});

struct Trig2048 { double c[2048]; };
constexpr Trig2048 mk_trig() { Trig2048 t{}; for (int i = 0; i < 2048; i++) t.c[i] = cos2pi(i, 2048); return t; }
constexpr Trig2048 kTrig = mk_trig();
constexpr double C2K(int m) { return kTrig.c[m & 2047]; }
constexpr double S2K(int m) { return kTrig.c[(m + 1536) & 2047]; }
constexpr A1T mk_a1() {
  A1T t{};
  for (int ks = 0; ks < 2; ks++) for (int l = 0; l < 64; l++) for (int j = 0; j < 8; j++) {
    int h = l >> 5, r = l & 31;
    int n1 = 16 * ks + 8 * h + j;
    double c = C2K(64 * n1 * r), s = S2K(64 * n1 * r);
    t.a1[0][ks][l][j] = to_bf16(c); t.a1[1][ks][l][j] = to_bf16(-s);
    int k1 = kperm_c(ks, h, j);
    c = C2K(64 * k1 * r); s = S2K(64 * k1 * r);
    t.a1c[0][ks][l][j] = to_bf16(c); t.a1c[1][ks][l][j] = to_bf16(-s);
  }
  return t;
}
constexpr A2T mk_a2(int u) {
  A2T t{};
  for (int kk = 0; kk < 4; kk++) for (int l = 0; l < 64; l++) for (int j = 0; j < 8; j++) {
    int h = l >> 5, row = 32 * u + (l & 31), col = 32 * (kk >> 1) + kperm_c(kk & 1, h, j);
    double c = C2K(32 * row * col), s = S2K(32 * row * col);
    t.v[kk][0][l][j] = to_bf16(c); t.v[kk][1][l][j] = to_bf16(-s);
  }
  return t;
}
constexpr TWT mk_tw(int t, int inv) {
  TWT o{};
  for (int q = 0; q < 8; q++) for (int l = 0; l < 64; l++) for (int e = 0; e < 2; e++) {
    int h = l >> 5, r = 2 * q + e;
    int n2 = inv ? 32 * t + (l & 31) : 32 * t + crow_c(r, h);
    int k1 = inv ? crow_c(r, h) : (l & 31);
    o.v[q][l][e] = (float)C2K(n2 * k1); o.v[q][l][2 + e] = (float)S2K(n2 * k1);
  }
  return o;
}
constexpr A1T cA1 = mk_a1();
template <int U> constexpr A2T cA2 = mk_a2(U);
template <int T, int I> constexpr TWT cTW = mk_tw(T, I);
__device__ const ColTabD gCT = {cA1, {cA2<0>, cA2<1>}, {cTW<0, 0>, cTW<1, 0>}, {cTW<0, 1>, cTW<1, 1>}};
#else
extern __device__ const FragAll gTF;
extern __device__ const FragAll gTI;
extern __device__ const ColTabD gCT;
#endif

DEVI f16v mfma32(u4v a, u4v b, f16v c) { return __builtin_amdgcn_mfma_f32_32x32x16_bf16(__builtin_bit_cast(bf8, a), __builtin_bit_cast(bf8, b), c, 0, 0, 0); }
DEVI f16v mfma32k8(u2v a, u2v b, f16v c) { return __builtin_amdgcn_mfma_f32_32x32x8bf16_1k(__builtin_bit_cast(s4, a), __builtin_bit_cast(s4, b), c, 0, 0, 0); }
DEVI f4v mfma16(u4v a, u4v b, f4v c) { return __builtin_amdgcn_mfma_f32_16x16x32_bf16(__builtin_bit_cast(bf8, a), __builtin_bit_cast(bf8, b), c, 0, 0, 0); }
DEVI u32 pk2(float a, float b) { f2v v = {a, b}; return __builtin_bit_cast(u32, __builtin_convertvector(v, bf2)); }
DEVI float bflo(u32 w) { return __builtin_bit_cast(float, w << 16); }
DEVI float bfhi(u32 w) { return __builtin_bit_cast(float, w & 0xffff0000u); }
DEVI u4v lds128(const char* p) { return *(const LAS u4v*)(const LAS char*)p; }
DEVI u2v lds64(const char* p) { return *(const LAS u2v*)(const LAS char*)p; }
DEVI void sts128(char* p, u4v v) { *(LAS u4v*)(LAS char*)p = v; }
DEVI void sts64(char* p, u2v v) { *(LAS u2v*)(LAS char*)p = v; }
DEVI u2v ldstr(const char* p) { return __builtin_bit_cast(u2v, __builtin_amdgcn_ds_read_tr16_b64_v4i16((LAS s4*)(LAS char*)p)); }
DEVI u4v cat(u2v a, u2v b) { u4v r = {a.x, a.y, b.x, b.y}; return r; }
DEVI void glds16(const void* base, u32 voff, u32 lds) {
  u32 sv;
  asm volatile("s_mov_b32 %0, m0\n\ts_mov_b32 m0, %3\n\ts_nop 0\n\tglobal_load_lds_dwordx4 %1, %2\n\ts_mov_b32 m0, %0" : "=&s"(sv) : "v"(voff), "s"(base), "s"(lds) : "memory");
}
DEVI void glds4(const void* base, u32 voff, u32 lds) {
  u32 sv;
  asm volatile("s_mov_b32 %0, m0\n\ts_mov_b32 m0, %3\n\ts_nop 0\n\tglobal_load_lds_dword %1, %2\n\ts_mov_b32 m0, %0" : "=&s"(sv) : "v"(voff), "s"(base), "s"(lds) : "memory");
}
DEVI void glds16c(const void* base, u32 voff, u32 lds) {
  u32 sv;
  asm volatile("s_mov_b32 %0, m0\n\ts_mov_b32 m0, %3\n\ts_nop 0\n\tglobal_load_lds_dwordx4 %1, %2 sc1\n\ts_mov_b32 m0, %0" : "=&s"(sv) : "v"(voff), "s"(base), "s"(lds) : "memory");
}
template <int N> DEVI void wait_vm() { asm volatile("s_waitcnt vmcnt(%0)" ::"i"(N) : "memory"); }
template <int N> DEVI void wait_vm_bar() { asm volatile("s_waitcnt vmcnt(%0) lgkmcnt(0)\n\ts_barrier" ::"i"(N) : "memory"); }
template <int N> DEVI void vm_bar() { asm volatile("s_waitcnt vmcnt(%0)\n\ts_barrier" ::"i"(N) : "memory"); }
DEVI void raw_bar() { asm volatile("s_barrier" ::: "memory"); }
DEVI void lds_bar() { asm volatile("s_waitcnt lgkmcnt(0)\n\ts_barrier" ::: "memory"); }
DEVI u32 uni(u32 v) { return __builtin_amdgcn_readfirstlane(v); }
DEVI int fresh_tid() { int t = threadIdx.x; asm volatile("" : "+v"(t)); return t; }
DEVI u4v swap_pack(u32 a0, u32 a1, u32 b0, u32 b1) {
  auto r0 = __builtin_amdgcn_permlane32_swap(a0, b0, false, false);
  auto r1 = __builtin_amdgcn_permlane32_swap(a1, b1, false, false);
  u4v o = {r0[0], r1[0], r0[1], r1[1]};
  return o;
}
template <int CTRL> DEVI float dpp(float v) { return __builtin_bit_cast(float, __builtin_amdgcn_update_dpp(0, __builtin_bit_cast(int, v), CTRL, 0xf, 0xf, true)); }
DEVI float xor16(float v) { auto r = __builtin_amdgcn_permlane16_swap(__builtin_bit_cast(u32, v), __builtin_bit_cast(u32, v), false, false); return __builtin_bit_cast(float, (threadIdx.x & 16) ? r[0] : r[1]); }
DEVI float xor32(float v) { auto r = __builtin_amdgcn_permlane32_swap(__builtin_bit_cast(u32, v), __builtin_bit_cast(u32, v), false, false); return __builtin_bit_cast(float, (threadIdx.x & 32) ? r[0] : r[1]); }
DEVI float nmax(float a, float b) { return (a > b || a != a) ? a : b; }
DEVI float max16(float v) {
  v = nmax(v, dpp<0xB1>(v)); v = nmax(v, dpp<0x4E>(v)); v = nmax(v, dpp<0x141>(v)); return nmax(v, dpp<0x140>(v));
}
DEVI float max64(float v) { v = max16(v); v = nmax(v, xor16(v)); return nmax(v, xor32(v)); }
DEVI int swap23(int k) { return (k & ~12) | ((k & 4) << 1) | ((k & 8) >> 1); }
DEVI int s_of_p(int p) { return 64 * (p & 31) + (p >> 5); }

typedef __amdgpu_buffer_rsrc_t rsrc_t;
constexpr int SC1 = 16;
DEVI rsrc_t ws_rsrc(const char* ws) { return __builtin_amdgcn_make_buffer_rsrc((void*)ws, 0, 0x1000000, 0x00020000); }
DEVI void st128c(rsrc_t r, u32 off, u4v v) { __builtin_amdgcn_raw_buffer_store_b128(v, r, off, 0, SC1); }
DEVI void st64c(rsrc_t r, u32 off, u2v v) { __builtin_amdgcn_raw_buffer_store_b64(v, r, off, 0, SC1); }
DEVI void st32c(rsrc_t r, u32 off, u32 v) { __builtin_amdgcn_raw_buffer_store_b32(v, r, off, 0, SC1); }
DEVI u4v ld128c(rsrc_t r, u32 off) { return __builtin_amdgcn_raw_buffer_load_b128(r, off, 0, SC1); }
DEVI u32 ld32c(rsrc_t r, u32 off) { return __builtin_amdgcn_raw_buffer_load_b32(r, off, 0, SC1); }

struct Params {
  const u16 *x, *wq, *wk, *wv, *wo, *wr, *wi, *gw;
  u16* out;
  char* ws;
};

DEVI u64 read_epoch(const Params& P) { return __hip_atomic_load((GAS u64*)(P.ws + WS_FL + FL_E), __ATOMIC_RELAXED, __HIP_MEMORY_SCOPE_AGENT); }
DEVI u64 tag_of(u64 e) { return (((u64)uni((u32)(e >> 32)) << 32) | uni((u32)e)) + 1; }
DEVI void put_flag(const Params& P, u32 off, u64 tag) {
  __hip_atomic_store((GAS u64*)(P.ws + WS_FL + off), tag, __ATOMIC_RELAXED, __HIP_MEMORY_SCOPE_AGENT);
}
DEVI void publish(const Params& P, u32 off, u64 tag) {
  asm volatile("s_waitcnt vmcnt(0) lgkmcnt(0)\n\ts_barrier" ::: "memory");
  if (threadIdx.x == 0) put_flag(P, off, tag);
}
template <int N> DEVI bool test_flags(const Params& P, u32 off, u64 tag, int lane) {
  const GAS u64* f = (const GAS u64*)(P.ws + WS_FL + off);
  bool ok = true;
#pragma unroll
  for (int i = 0; i < (N + 63) / 64; i++) {
    const int idx = N >= 64 ? lane + 64 * i : (lane & (N - 1));
    ok &= __hip_atomic_load(f + idx, __ATOMIC_RELAXED, __HIP_MEMORY_SCOPE_AGENT) == tag;
  }
  return __all(ok);
}
template <int N, int SLEEP = kPollSleep> DEVI void spin_flags(const Params& P, u32 off, u64 tag, int lane) {
  for (int it = 0; it < (1 << 20); it++) {
    if (test_flags<N>(P, off, tag, lane)) break;
    __builtin_amdgcn_s_sleep(SLEEP);
  }
}
template <int N, int SLEEP = kPollSleep> DEVI void wait_flags(const Params& P, u32 off, u64 tag) {
  if (uni(threadIdx.x >> 6) == 0) spin_flags<N, SLEEP>(P, off, tag, threadIdx.x & 63);
  asm volatile("s_waitcnt vmcnt(0) lgkmcnt(0)\n\ts_barrier" ::: "memory");
}

constexpr int CHUNK = 8 * KVT;
constexpr int KV_BASE = LDS_BYTES - 3 * CHUNK;
constexpr float kBound2 = 48.f * 48.f;

DEVI void kv_loader(const Params& P, const char* kvg, u32 lds0, int lw, int lane, int wgi, u64 tag) {
  for (int c = 0; c < 8; c++) {
    if (c < 7) {
      const char* src = kvg + (c + 1) * CHUNK;
      const u32 dst = lds0 + KV_BASE + (c % 3) * CHUNK;
#pragma unroll
      for (int j = 0; j < 5; j++) glds16c(src + (lw + 4 * j) * 1024, lane * 16, dst + (lw + 4 * j) * 1024);
      if (lw == 0) glds16c(src + 20 * 1024, lane * 16, dst + 20 * 1024);
    } else if (lw == 0) {
      __builtin_amdgcn_s_sleep(kHintSleep);
      if (lane == 0) put_flag(P, FL_SB + wgi * 8, tag);
    }
    wait_vm_bar<0>();
  }
}

DEVI void attn_loop_safe(char* smem, int lane, u4v qa0, u2v qb0, u4v qa1, u2v qb1, f16v& o0, f16v& o1) {
  const int r = lane & 31, h = lane >> 5;
  const int koff = lane * 16, k1off = 1024 + r * 8;
  const int voff = 1280 + h * 352 + (r <= 20 ? r : 21) * 16;
  float m0 = -__builtin_inff(), m1 = -__builtin_inff();
  auto one = [&](u4v ka, u2v kb, u4v v0, u4v v1, u4v qa, u2v qb, float& mrun, f16v& o) {
    f16v s = {};
    s = mfma32(ka, qa, s);
    s = mfma32k8(kb, qb, s);
    float mx = s[0];
#pragma unroll
    for (int i = 1; i < 16; i++) mx = fmaxf(mx, s[i]);
    mx = fmaxf(mx, __shfl_xor(mx, 32));
    const float mn = fmaxf(mrun, mx), al = __builtin_amdgcn_exp2f(mrun - mn);
    mrun = mn;
    u4v p0, p1;
#pragma unroll
    for (int i = 0; i < 16; i++) o[i] *= al;
#pragma unroll
    for (int i = 0; i < 4; i++) {
      p0[i] = pk2(__builtin_amdgcn_exp2f(s[2 * i] - mn), __builtin_amdgcn_exp2f(s[2 * i + 1] - mn));
      p1[i] = pk2(__builtin_amdgcn_exp2f(s[8 + 2 * i] - mn), __builtin_amdgcn_exp2f(s[9 + 2 * i] - mn));
    }
    o = mfma32(v0, p0, o);
    o = mfma32(v1, p1, o);
  };
  for (int c = 0; c < 8; c++) {
    for (int i = 0; i < 8; i++) {
      if (i == 7) lds_bar();
      const char* p = smem + KV_BASE + ((c + 2) % 3) * CHUNK + i * KVT;
      u4v ka = lds128(p + koff), v0 = lds128(p + voff), v1 = lds128(p + voff + 704);
      u2v kb = lds64(p + k1off);
      one(ka, kb, v0, v1, qa0, qb0, m0, o0);
      one(ka, kb, v0, v1, qa1, qb1, m1, o1);
    }
  }
}

DEVI void attn_loop_fast(u32 lds0, int lane, u4v qa0, u2v qb0, u4v qa1, u2v qb1, f16v& o0, f16v& o1) {
  const int r = lane & 31, h = lane >> 5;
  const u32 base = lds0 + KV_BASE;
  const u32 kb = base + lane * 16, k1b = base + 1024 + r * 8, vb = base + 1280 + h * 352 + (r <= 20 ? r : 21) * 16;
  u32 s_c;
  asm volatile(R"ASM(
.macro EXP r
  v_exp_f32 v[\r], v[\r]
.endm
.macro CVT d, a
  v_cvt_pk_bf16_f32 v[72+\d], v[\a], v[\a+1]
.endm
; tile t, query tile A: scores B of tile t are produced; K(t+1), V(t) keys 16..31 and V(t+1) keys 0..15 are fetched
.macro STEPA cur, nxt, v0c, v0n, bar
  .if \bar
  s_barrier
  .endif
  v_mfma_f32_32x32x16_bf16 v[56:71], v[80:83], %[qa1], 0
  ds_read_b128 v[80:83], %[kb] offset:\nxt
  EXP 40
  EXP 41
  EXP 42
  EXP 43
  v_mfma_f32_32x32x16_bf16 %[o1], v[90:93], v[76:79], %[o1]
  ds_read_b128 v[90:93], %[vb] offset:\cur+704
  EXP 44
  CVT 0, 40
  EXP 45
  CVT 1, 42
  EXP 46
  v_mfma_f32_32x32x8_bf16 v[56:71], v[84:85], %[qb1], v[56:71]
  ds_read_b64 v[84:85], %[k1b] offset:\nxt
  EXP 47
  EXP 48
  CVT 2, 44
  EXP 49
  CVT 3, 46
  EXP 50
  EXP 51
  v_mfma_f32_32x32x16_bf16 %[o0], v[\v0c:\v0c+3], v[72:75], %[o0]
  ds_read_b128 v[\v0n:\v0n+3], %[vb] offset:\nxt
  EXP 52
  EXP 53
  CVT 4, 48
  EXP 54
  CVT 5, 50
  EXP 55
  CVT 6, 52
  s_waitcnt lgkmcnt(1)
  CVT 7, 54
.endm
; tile t, query tile B: scores A of tile t+1 are produced
.macro STEPB v0c
  v_mfma_f32_32x32x16_bf16 v[40:55], v[80:83], %[qa0], 0
  EXP 56
  EXP 57
  EXP 58
  EXP 59
  v_mfma_f32_32x32x16_bf16 %[o0], v[90:93], v[76:79], %[o0]
  EXP 60
  CVT 0, 56
  EXP 61
  CVT 1, 58
  EXP 62
  v_mfma_f32_32x32x8_bf16 v[40:55], v[84:85], %[qb0], v[40:55]
  EXP 63
  EXP 64
  CVT 2, 60
  EXP 65
  CVT 3, 62
  EXP 66
  EXP 67
  v_mfma_f32_32x32x16_bf16 %[o1], v[\v0c:\v0c+3], v[72:75], %[o1]
  EXP 68
  EXP 69
  CVT 4, 64
  EXP 70
  CVT 5, 66
  EXP 71
  CVT 6, 68
  s_waitcnt lgkmcnt(0)
  CVT 7, 70
.endm
.macro PAIR cur, nxt, v0c, v0n, bar
  STEPA \cur, \nxt, \v0c, \v0n, \bar
  STEPB \v0c
.endm
; one chunk of 8 key tiles in the ring slot at \s; the next chunk sits at \n
.macro CHUNK s, n
  PAIR \s+0*2688, \s+1*2688, 86, 94, 0
  PAIR \s+1*2688, \s+2*2688, 94, 86, 0
  PAIR \s+2*2688, \s+3*2688, 86, 94, 0
  PAIR \s+3*2688, \s+4*2688, 94, 86, 0
  PAIR \s+4*2688, \s+5*2688, 86, 94, 0
  PAIR \s+5*2688, \s+6*2688, 94, 86, 0
  PAIR \s+6*2688, \s+7*2688, 86, 94, 0
  PAIR \s+7*2688, \n, 94, 86, 1
.endm
  ; chunk c sits in slot (c+2)%3
  ds_read_b128 v[80:83], %[kb] offset:43008
  ds_read_b64 v[84:85], %[k1b] offset:43008
  ds_read_b128 v[86:89], %[vb] offset:43008
  v_mov_b32 v90, 0
  v_mov_b32 v91, 0
  v_mov_b32 v92, 0
  v_mov_b32 v93, 0
  v_mov_b32 v76, 0
  v_mov_b32 v77, 0
  v_mov_b32 v78, 0
  v_mov_b32 v79, 0
  s_mov_b32 %[sc_], 0
  s_waitcnt lgkmcnt(0)
  v_mfma_f32_32x32x16_bf16 v[40:55], v[80:83], %[qa0], 0
  v_mfma_f32_32x32x8_bf16 v[40:55], v[84:85], %[qb0], v[40:55]
  s_nop 15
  s_nop 7
3:
  CHUNK 43008, 0
  CHUNK 0, 21504
  s_cmp_eq_u32 %[sc_], 2
  s_cbranch_scc1 4f
  CHUNK 21504, 43008
  s_add_u32 %[sc_], %[sc_], 1
  s_branch 3b
4:
  v_mfma_f32_32x32x16_bf16 %[o1], v[90:93], v[76:79], %[o1]
  s_waitcnt lgkmcnt(0)
  s_nop 15
  s_nop 7
.purgem EXP
.purgem CVT
.purgem STEPA
.purgem STEPB
.purgem PAIR
.purgem CHUNK
)ASM"
      : [o0] "+v"(o0), [o1] "+v"(o1), [sc_] "=&s"(s_c)
      : [qa0] "v"(qa0), [qb0] "v"(qb0), [qa1] "v"(qa1), [qb1] "v"(qb1), [kb] "v"(kb), [k1b] "v"(k1b), [vb] "v"(vb)
      : "memory", "scc", "v40", "v41", "v42", "v43", "v44", "v45", "v46", "v47", "v48", "v49", "v50", "v51", "v52", "v53", "v54", "v55",
        "v56", "v57", "v58", "v59", "v60", "v61", "v62", "v63", "v64", "v65", "v66", "v67", "v68", "v69", "v70", "v71", "v72", "v73", "v74", "v75",
        "v76", "v77", "v78", "v79", "v80", "v81", "v82", "v83", "v84", "v85", "v86", "v87", "v88", "v89", "v90", "v91", "v92", "v93",
        "v94", "v95", "v96", "v97");
}

DEVI void a_wg(const Params& P, char* smem, int mt, int nt, int wgi) {
  const int tid = fresh_tid(), lane = tid & 63, w = uni(tid >> 6), r = lane & 31, h = lane >> 5;
  const u32 lds0 = (u32)(uintptr_t)(LAS char*)smem;
  const int p0 = mt * 64;
  const u64 epoch = read_epoch(P);
  const char* kvg = P.ws + WS_KV;
  u64 tag = 0;
  constexpr int QST = 18432;
  u4v qa[2]; u2v qb[2];
  float qn;
  auto qproj = [&](auto NPc) {
    constexpr int NP = NPc;
    u32 xoff, voff[NP - 1];
    {
      const int row = w * 8 + (lane >> 3), ch = (lane & 7) ^ ((row >> 1) & 7);
      xoff = (s_of_p(p0 + row) * 640 + ch * 8) * 2;
    }
    const int wp0 = NP == 3 ? 2 * w : w + 2;
#pragma unroll
    for (int j = 0; j < NP - 1; j++) {
      const int o = (wp0 + j) * 1024 + lane * 16, row = o / 160, c = (o % 160) >> 4;
      voff[j] = (row * 640 + 80 * nt + 8 * c) * 2;
    }
    auto xpiece = [&](int t) { glds16((const char*)P.x + t * 128, xoff, lds0 + (t & 3) * QST + w * 1024); };
    auto wpiece = [&](int t, int j) { glds16((const char*)P.wq + t * (64 * 1280), voff[j], lds0 + (t & 3) * QST + 8192 + (wp0 + j) * 1024); };
#pragma unroll
    for (int t = 0; t < 3; t++) {
      xpiece(t);
#pragma unroll
      for (int j = 0; j < NP - 1; j++) wpiece(t, j);
    }
    int xo[4];
#pragma unroll
    for (int ks = 0; ks < 4; ks++) xo[ks] = (w >> 2) * 4096 + r * 128 + (((2 * ks + h) ^ ((r >> 1) & 7)) << 4);
    const int woff = 8192 + (8 * h + ((lane & 15) >> 2)) * 160 + 40 * (w & 3) + ((lane >> 4) & 1) * 32 + (lane & 3) * 8;
    struct Fr { u4v x, wf; };
    auto rd = [&](const char* sl, int ks) {
      Fr f;
      f.x = lds128(sl + xo[ks]);
      const char* a = sl + woff + ks * 16 * 160;
      f.wf = cat(ldstr(a), ldstr(a + 4 * 160));
      return f;
    };
    f16v q = {};
    vm_bar<2 * NP>();
    tag = tag_of(epoch);
    Fr fa = rd(smem, 0), fb = rd(smem, 1);
    auto stage = [&](int t, auto DMA, auto&& wait) {
      const char* sl = smem + (t & 3) * QST;
      const char* sl1 = smem + ((t + 1) & 3) * QST;
      q = mfma32(fa.wf, fa.x, q); fa = rd(sl, 2);
      if constexpr (DMA) xpiece(t + 3);
      __builtin_amdgcn_sched_barrier(0);
      q = mfma32(fb.wf, fb.x, q); fb = rd(sl, 3);
      if constexpr (DMA) wpiece(t + 3, 0);
      __builtin_amdgcn_sched_barrier(0);
      q = mfma32(fa.wf, fa.x, q);
      if constexpr (DMA && NP == 3) wpiece(t + 3, NP - 2);
      wait();
      fa = rd(sl1, 0);
      __builtin_amdgcn_sched_barrier(0);
      q = mfma32(fb.wf, fb.x, q); fb = rd(sl1, 1);
      __builtin_amdgcn_sched_barrier(0);
    };
#pragma unroll 1
    for (int t = 0; t < 7; t++) stage(t, std::true_type{}, [&] { wait_vm_bar<2 * NP>(); });
    u64 fl[4];
#pragma unroll
    for (int k = 0; k < 4; k++) fl[k] = __hip_atomic_load((const GAS u64*)(P.ws + WS_FL + FL_S1) + lane + 64 * k, __ATOMIC_RELAXED, __HIP_MEMORY_SCOPE_AGENT);
    stage(7, std::false_type{}, [&] { wait_vm_bar<NP + 4>(); });
    const bool got = __all(fl[0] == tag && fl[1] == tag && fl[2] == tag && fl[3] == tag);
    auto chunk0 = [&] {
#pragma unroll
      for (int j = 0; j < 2; j++) glds16c(kvg + (w + 8 * j) * 1024, lane * 16, lds0 + KV_BASE + 2 * CHUNK + (w + 8 * j) * 1024);
      if (w < 5) glds16c(kvg + (w + 16) * 1024, lane * 16, lds0 + KV_BASE + 2 * CHUNK + (w + 16) * 1024);
    };
    u32 kmr[4] = {};
    auto km_load = [&] {
      if (w == 5) {
        const rsrc_t r5 = ws_rsrc(P.ws);
#pragma unroll
        for (int k = 0; k < 4; k++) kmr[k] = ld32c(r5, WS_KM + k * 256 + lane * 4);
      }
    };
    if (got) { chunk0(); km_load(); }
    stage(8, std::false_type{}, [&] { lds_bar(); });
    stage(9, std::false_type{}, [&] { lds_bar(); });
    if (w == 4 && lane == 0) put_flag(P, FL_SA + wgi * 8, tag);
    if (!got) { spin_flags<256, 1>(P, FL_S1, tag, lane); chunk0(); km_load(); }
    const float qs = 0.22360679774997896f * 1.4426950408889634f;
    float n = 0.f;
#pragma unroll
    for (int j = 0; j < 12; j++) { q[j] *= qs; if (j < 8 || h == 0) n += q[j] * q[j]; }
    qa[0] = u4v{pk2(q[0], q[1]), pk2(q[2], q[3]), pk2(q[4], q[5]), pk2(q[6], q[7])};
    qb[0] = h ? u2v{0u, 0u} : u2v{pk2(q[8], q[9]), pk2(q[10], q[11])};
    qn = n + xor32(n);
    if (w >= 4) {
      char* qx = smem + 1024 + (w & 3) * (7 * 256) + lane * 4;
#pragma unroll
      for (int j = 0; j < 4; j++) *(LAS u32*)(LAS char*)(qx + j * 256) = qa[0][j];
#pragma unroll
      for (int j = 0; j < 2; j++) *(LAS u32*)(LAS char*)(qx + (4 + j) * 256) = qb[0][j];
      *(LAS float*)(LAS char*)(qx + 6 * 256) = qn;
      if (w == 5) {
        const float km0 = nmax(__builtin_bit_cast(float, kmr[0]), __builtin_bit_cast(float, kmr[1]));
        const float km1 = nmax(__builtin_bit_cast(float, kmr[2]), __builtin_bit_cast(float, kmr[3]));
        const float kmx = max64(km0) + max64(km1);
        if (lane == 0) *(LAS float*)(LAS char*)smem = kmx;
      }
    }
  };
  if (w < 2) qproj(std::integral_constant<int, 3>{}); else qproj(std::integral_constant<int, 2>{});
  lds_bar();
  if (w >= 4) { wait_vm_bar<0>(); kv_loader(P, kvg, lds0, w - 4, lane, wgi, tag); return; }
  const rsrc_t rs = ws_rsrc(P.ws);
  const float km = *(const LAS float*)(const LAS char*)smem;
  {
    const char* qx = smem + 1024 + w * (7 * 256) + lane * 4;
#pragma unroll
    for (int j = 0; j < 4; j++) qa[1][j] = *(const LAS u32*)(const LAS char*)(qx + j * 256);
#pragma unroll
    for (int j = 0; j < 2; j++) qb[1][j] = *(const LAS u32*)(const LAS char*)(qx + (4 + j) * 256);
    qn = nmax(qn, *(const LAS float*)(const LAS char*)(qx + 6 * 256));
  }
  const bool fast = __all(qn * km * 1.05f <= kBound2);
  f16v o0 = {}, o1 = {};
  wait_vm_bar<0>();
  if (fast) attn_loop_fast(lds0, lane, qa[0], qb[0], qa[1], qb[1], o0, o1);
  else attn_loop_safe(smem, lane, qa[0], qb[0], qa[1], qb[1], o0, o1);
  __builtin_amdgcn_s_setprio(3);
#pragma unroll
  for (int i = 0; i < 2; i++) {
    const f16v& o = i ? o1 : o0;
    const float up = xor32(o[8]);
    const float sc = __builtin_amdgcn_rcpf(h ? o[8] : up);
    char* dst = smem + (32 * i + r) * 176 + (20 * w + 4 * h) * 2;
    sts64(dst, u2v{pk2(o[0] * sc, o[1] * sc), pk2(o[2] * sc, o[3] * sc)});
    sts64(dst + 16, u2v{pk2(o[4] * sc, o[5] * sc), pk2(o[6] * sc, o[7] * sc)});
    if (h == 0) sts64(dst + 32, u2v{pk2(o[8] * sc, o[9] * sc), pk2(o[10] * sc, o[11] * sc)});
  }
  lds_bar();
  {
    u4v v[3];
    u32 o[3];
#pragma unroll
    for (int k = 0; k < 3; k++) {
      const int ch = tid + 256 * k < 640 ? tid + 256 * k : tid, row = ch / 10, c = ch % 10;
      v[k] = lds128(smem + row * 176 + c * 16);
      o[k] = WS_O + ((p0 + row) * 640 + 80 * nt + 8 * c) * 2;
    }
    st128c(rs, o[0], v[0]); st128c(rs, o[1], v[1]);
    if (w < 2) st128c(rs, o[2], v[2]);
  }
  publish(P, FL_S4 + (mt * 8 + nt) * 8, tag_of(epoch));
}

DEVI void b1_kv(const Params& P, char* smem, int kg, int part, int idx) {
  const int tid = fresh_tid(), lane = tid & 63, w = uni(tid >> 6), l15 = lane & 15, g = lane >> 4;
  const u32 lds0 = (u32)(uintptr_t)(LAS char*)smem;
  constexpr int RW = 20480, RG = RW + 25600;
  const char* wsrc = (const char*)(part ? P.wv : P.wk);
  {
    const int row = (w & 1) * 8 + (lane >> 3), ch = (lane & 7) ^ ((row >> 1) & 7);
    const u32 xo = (s_of_p(16 * kg + row) * 640 + ch * 8) * 2;
    glds16((const char*)P.x + (w >> 1) * 128, xo, lds0 + w * 1024);
    glds16((const char*)P.x + ((w + 8) >> 1) * 128, xo, lds0 + (w + 8) * 1024);
    if (w < 4) glds16((const char*)P.x + ((w + 16) >> 1) * 128, xo, lds0 + (w + 16) * 1024);
    else glds16(wsrc + (w - 4) * 1024, lane * 16, lds0 + (w + 16) * 1024);
    glds16(wsrc + (w + 4) * 1024, lane * 16, lds0 + (w + 24) * 1024);
    glds16(wsrc + (w + 12) * 1024, lane * 16, lds0 + (w + 32) * 1024);
    if (w < 5) glds16(wsrc + (w + 20) * 1024, lane * 16, lds0 + (w + 40) * 1024);
    else if (part && w < 7) { const int e = (w - 5) * 512 + lane * 8; glds16(P.gw, (e > 632 ? 632 : e) * 2, lds0 + (w + 40) * 1024); }
  }
  if (w == 3 || w == 4) {
    const int t = ((w - 3) * 64 + lane) % 100;
    glds4((const char*)P.wq, (20 * idx + t / 5) * 1280 + part * 640 + (t % 5) * 128, lds0 + 65536 + (w - 3) * 256);
    wait_vm_bar<1>();
  } else {
    wait_vm_bar<0>();
  }
  const rsrc_t rs = ws_rsrc(P.ws);
  const int s = kg & 1;
  const u32 tile = WS_KV + (kg >> 1) * KVT;
  if (w < 2 + part) {
    const char* xa = smem + l15 * 128;
    const int sw = (l15 >> 1) & 7;
    const char* aw = smem + RW + (8 * g + (l15 >> 2)) * 40 + (lane & 3) * 8 + w * 32;
    f4v a0 = {}, a1 = {};
    auto run = [&](auto GATE, auto KPART) {
      u4v xf[2][4], wf[2][4];
      auto rd = [&](int b) {
#pragma unroll
        for (int e = 0; e < 4; e++) {
          const int ks = 4 * b + e;
          xf[b & 1][e] = lds128(xa + (ks >> 1) * 2048 + (((4 * (ks & 1) + g) ^ sw) << 4));
          if constexpr (GATE) wf[b & 1][e] = lds128(smem + RG + (32 * ks + 8 * g) * 2);
          else wf[b & 1][e] = cat(ldstr(aw + ks * 32 * 40), ldstr(aw + ks * 32 * 40 + 160));
        }
      };
      rd(0);
#pragma unroll
      for (int b = 0; b < 5; b++) {
        if (b < 4) rd(b + 1);
        __builtin_amdgcn_sched_barrier(0);
#pragma unroll
        for (int e = 0; e < 4; e++) {
          f4v& a = (e & 1) ? a1 : a0;
          a = KPART ? mfma16(wf[b & 1][e], xf[b & 1][e], a) : mfma16(xf[b & 1][e], wf[b & 1][e], a);
        }
        __builtin_amdgcn_sched_barrier(0);
      }
    };
    if (part == 0) run(std::false_type{}, std::true_type{});
    else if (w < 2) run(std::false_type{}, std::false_type{});
    else run(std::true_type{}, std::false_type{});
    const f4v a = a0 + a1;
    if (part == 0) {
      const u2v v = {pk2(a[0], a[1]), pk2(a[2], a[3])};
      float n2 = a[0] * a[0] + a[1] * a[1] + a[2] * a[2] + a[3] * a[3];
      if (w == 0) {
        st64c(rs, tile + ((g & 1) * 32 + 16 * s + l15) * 16 + (g >> 1) * 8, v);
        n2 += xor16(n2); n2 += xor32(n2);
      } else if (g == 0) st64c(rs, tile + 1024 + (16 * s + l15) * 8, v);
      n2 = max16(n2);
      if (lane == 0) st32c(rs, WS_KM + w * 512 + kg * 4, __builtin_bit_cast(u32, n2));
    } else if (w < 2) {
      u2v v = {pk2(a[0], a[1]), pk2(a[2], a[3])};
      if (w == 1 && l15 == 4) v = u2v{0x3f803f80u, 0x3f803f80u};
      if (w == 1 && l15 == 5) v = u2v{0u, 0u};
      if (w == 0 || l15 <= 5) st64c(rs, tile + 1280 + (2 * s + (g & 1)) * 352 + (g >> 1) * 8 + (16 * w + l15) * 16, v);
    } else if (l15 == 0) {
      u4v o;
#pragma unroll
      for (int i = 0; i < 4; i++) o[i] = __builtin_bit_cast(u32, 1.f / (1.f + __expf(-a[i])));
      st128c(rs, WS_G + (16 * kg + 4 * g) * 4, o);
    }
  }
}

struct WtUnit { u4v re, im; };
DEVI WtUnit wt_load(const Params& P, int u, int lane) {
  const int k2 = u / 20, f0 = 16 * (u % 20) + 8 * (lane >> 5), row = 32 * k2 + (lane & 31);
  WtUnit t;
  __builtin_memcpy(&t.re, P.wr + row * 321 + f0, 16);
  __builtin_memcpy(&t.im, P.wi + row * 321 + f0, 16);
  return t;
}
DEVI void wt_store(const Params& P, int u, int lane, const WtUnit& t) {
  const int k2 = u / 20, f0 = 16 * (u % 20) + 8 * (lane >> 5);
  const rsrc_t rs = ws_rsrc(P.ws);
  const u32 o = WS_WT + f0 * 8192 + k2 * 128 + (lane & 31) * 4;
#pragma unroll
  for (int j = 0; j < 4; j++) {
    st32c(rs, o + (2 * j) * 8192, (t.re[j] & 0xffffu) | (t.im[j] << 16));
    st32c(rs, o + (2 * j + 1) * 8192, (t.re[j] >> 16) | (t.im[j] & 0xffff0000u));
  }
}

template <bool COH> DEVI void b_piece(const char* base, u32 vo, int adv, u32 lds0, int w, int t) {
  if (COH) glds16c(base + t * adv, vo, lds0 + (t & 7) * 8192 + w * 1024);
  else glds16(base + t * adv, vo, lds0 + (t & 7) * 8192 + w * 1024);
}
template <int KIND> DEVI u4v b_frag(const char* smem, const int (&ao)[4], int n, int mb) {
  return lds128(smem + ao[n & 1] + ((n >> 1) & 7) * 8192 + mb * 2048);
}
template <int N, class F> DEVI void forN(F&& f) { if constexpr (N > 0) { forN<N - 1>(f); f(std::integral_constant<int, N - 1>{}); } }
template <class F> DEVI void call0(F& f) { if constexpr (std::is_invocable_v<F&>) f(); }
template <int J, class F> DEVI void call1(F& f) { if constexpr (std::is_invocable_v<F&, std::integral_constant<int, J>>) f(std::integral_constant<int, J>{}); }
constexpr int b_landed(int n, int npre) {
  const int m1 = npre == 20 ? 3 : -1;
  const int la = n < m1 ? 1 : 2, lap = (n - 1) < m1 ? 1 : 2;
  const int need = (n + la) >> 1, had = n == 0 ? 0 : (n - 1 + lap) >> 1;
  return need > had ? need : had;
}
template <int KIND, bool COH, bool MAIN, int NPRE, int NSIDE, class SIDE>
DEVI void b_gemm_w(const char* smem, u4v (&W)[20], const void* tab, u32 toff, f4v (&acc)[4], const int (&ao)[4],
                   const char* base, u32 vo, int adv, u32 lds0, int w, SIDE&& side) {
  constexpr int PRE = KIND == 1 ? 8 : 5;
#pragma unroll
  for (int t = 0; t < PRE; t++) b_piece<COH>(base, vo, adv, lds0, w, t);
  vm_bar<PRE - 1>();
  constexpr int N1 = (MAIN && NPRE == 20) ? 3 : -1;
  const int kq = w & 3;
  u4v a[2][4];
  if constexpr (MAIN) {
#pragma unroll
    for (int i = 0; i < (NPRE == 20 ? 1 : 2); i++)
#pragma unroll
      for (int mb = 0; mb < 4; mb++) a[i][mb] = b_frag<KIND>(smem, ao, i, mb);
  } else if (kq < 2) {
#pragma unroll
    for (int mb = 0; mb < 4; mb++) a[0][mb] = kq ? b_frag<KIND>(smem, ao, 1, mb) : b_frag<KIND>(smem, ao, 0, mb);
  }
  forN<20>([&](auto NN) {
    constexpr int n = NN, T = n >> 1;
    constexpr int M1 = NPRE == 20 ? 3 : -1;
    constexpr int LA = n < M1 ? 1 : 2, LAP = (n - 1) < M1 ? 1 : 2;
    constexpr int need = (n + LA) >> 1, had = n == 0 ? 0 : (n - 1 + LAP) >> 1;
    if constexpr (MAIN && NPRE == 12 && (n == 2 || n == 6)) {
#pragma unroll
      for (int i = 0; i < 4; i++)
        W[(n == 2 ? 12 : 16) + i] = __builtin_amdgcn_raw_buffer_load_b128(__builtin_amdgcn_make_buffer_rsrc((void*)tab, 0, 819200, 0x00020000),
                                                                          toff + ((n == 2 ? 12 : 16) + i) * 1024, 0, 0);
    }
    if constexpr (PRE == 5 && !(n & 1) && n < 10) b_piece<COH>(base, vo, adv, lds0, w, 5 + T);
    if constexpr (PRE == 8 && (n == 4 || n == 6)) b_piece<COH>(base, vo, adv, lds0, w, n == 4 ? 8 : 9);
    if constexpr (!MAIN && n == 8) call0(side);
    if constexpr (MAIN && n == 12) call0(side);
    if constexpr (need > had && need <= 9) {
      constexpr int issued = PRE == 5 ? 5 + (T + 1 < 5 ? T + 1 : 5) : 8 + (n >= 4 ? 1 : 0) + (n >= 6 ? 1 : 0);
      constexpr int wy = MAIN ? (NPRE == 12 ? ((n >= 2 && need <= 5) ? 4 : 0) + ((n >= 6 && need <= 7) ? 4 : 0) : 0) + (n >= 12 ? NSIDE : 0) : (n >= 8 ? NSIDE : 0);
      vm_bar<(issued - need - 1 + wy)>();
    }
    if constexpr (MAIN) {
      constexpr int cur = n <= N1 ? 0 : (n & 1);
#pragma unroll
      for (int mb = 0; mb < 4; mb++) {
        const u4v x = a[cur][mb];
        acc[mb] = KIND == 0 ? mfma16(x, W[n], acc[mb]) : mfma16(W[n], x, acc[mb]);
        if constexpr (n == N1) { a[(n + 1) & 1][mb] = b_frag<KIND>(smem, ao, n + 1, mb); a[n & 1][mb] = b_frag<KIND>(smem, ao, n + 2, mb); }
        else if constexpr (n + LA < 20) a[LA == 1 ? 0 : (n & 1)][mb] = b_frag<KIND>(smem, ao, n + LA, mb);
      }
    } else {
      if ((n & 3) == kq) {
#pragma unroll
        for (int mb = 0; mb < 4; mb++) acc[mb] = KIND == 0 ? mfma16(a[0][mb], W[n >> 2], acc[mb]) : mfma16(W[n >> 2], a[0][mb], acc[mb]);
      }
      constexpr bool now2 = n + 2 < 20 && ((n + 2) >> 1) <= b_landed(n, NPRE);
      constexpr bool late1 = n >= 1 && ((n + 1) >> 1) > b_landed(n - 1, NPRE);
      if constexpr (late1) {
        if (((n + 1) & 3) == kq) {
#pragma unroll
          for (int mb = 0; mb < 4; mb++) a[0][mb] = b_frag<KIND>(smem, ao, n + 1, mb);
        }
      }
      if constexpr (now2) {
        if (((n + 2) & 3) == kq) {
#pragma unroll
          for (int mb = 0; mb < 4; mb++) a[0][mb] = b_frag<KIND>(smem, ao, n + 2, mb);
        }
      }
    }
    __builtin_amdgcn_sched_barrier(0);
  });
}
template <bool MAIN, class SIDE>
DEVI void b_gemm_res(const char* smem, const u4v (&W)[20], f4v (&acc)[4], const int (&ao)[4], int w, SIDE&& side) {
  auto frag = [&](int n, int mb) { const char* p = smem + ao[mb] + n * 4096; return cat(ldstr(p), ldstr(p + 1024)); };
  if constexpr (MAIN) {
    u4v a[2][4];
#pragma unroll
    for (int mb = 0; mb < 4; mb++) a[0][mb] = frag(0, mb);
    forN<20>([&](auto NN) {
      constexpr int n = NN, cur = n <= 3 ? 0 : (n & 1);
#pragma unroll
      for (int mb = 0; mb < 4; mb++) {
        acc[mb] = mfma16(W[n], a[cur][mb], acc[mb]);
        if constexpr (n < 3) a[0][mb] = frag(n + 1, mb);
        else if constexpr (n == 3) { a[0][mb] = frag(4, mb); a[1][mb] = frag(5, mb); }
        else if constexpr (n + 2 < 20) a[n & 1][mb] = frag(n + 2, mb);
      }
      if constexpr (n >= 6) call1<n - 6>(side);
      __builtin_amdgcn_sched_barrier(0);
    });
  } else {
    const int kq = w & 3;
    const char* sm = smem + kq * 4096;
    auto fr = [&](int j, int mb) { const char* p = sm + ao[mb] + j * 16384; return cat(ldstr(p), ldstr(p + 1024)); };
    u4v a[2][4];
#pragma unroll
    for (int mb = 0; mb < 4; mb++) a[0][mb] = fr(0, mb);
#pragma unroll
    for (int j = 0; j < 5; j++) {
      if (j < 4) {
#pragma unroll
        for (int mb = 0; mb < 4; mb++) a[(j + 1) & 1][mb] = fr(j + 1, mb);
      }
#pragma unroll
      for (int mb = 0; mb < 4; mb++) acc[mb] = mfma16(W[j], a[j & 1][mb], acc[mb]);
      __builtin_amdgcn_sched_barrier(0);
    }
  }
}
template <bool MAIN> DEVI f4v b_reduce(char* xa, const f4v (&acc)[4], int w, int lane) {
  lds_bar();
  const int kq = w & 3;
  if constexpr (!MAIN) {
#pragma unroll
    for (int mb = 0; mb < 4; mb++) *(LAS f4v*)(LAS char*)(xa + (kq * 4 + mb) * 1024 + lane * 16) = acc[mb];
  }
  lds_bar();
  f4v r = {};
  if constexpr (!MAIN) {
#pragma unroll
    for (int q = 0; q < 4; q++) r += *(const LAS f4v*)(const LAS char*)(xa + (q * 4 + kq) * 1024 + lane * 16);
  }
  return r;
}
DEVI u32 rows_off(int p0, bool perm, int w, int lane) {
  const int row = w * 8 + (lane >> 3), ch = (lane & 7) ^ ((row >> 1) & 7);
  return ((perm ? s_of_p(p0 + row) : p0 + row) * 640 + ch * 8) * 2;
}
DEVI void mk_ro(int (&ao)[4], int l15, int g) {
  ao[0] = l15 * 128 + ((g ^ ((l15 >> 1) & 7)) << 4);
  ao[1] = l15 * 128 + (((4 + g) ^ ((l15 >> 1) & 7)) << 4);
  ao[2] = ao[3] = 0;
}

DEVI void b2_fwd(const Params& P, char* smem, int mt, int nt, int i, u64 tag) {
  const int tid = fresh_tid(), lane = tid & 63, w = uni(tid >> 6), l15 = lane & 15, g = lane >> 4;
  const u32 lds0 = (u32)(uintptr_t)(LAS char*)smem;
  const u32 vo = rows_off(mt * 64, true, w, lane);
  f4v acc[4] = {};
  u4v W[20];
  int ao[4];
  mk_ro(ao, l15, g);
  char* stg = smem + 16384;
  auto put = [&](int blk, int mb, const f4v& v) {
    sts64(stg + blk * 2304 + l15 * 144 + (16 * mb + 4 * g) * 2, u2v{pk2(v[0], v[1]), pk2(v[2], v[3])});
  };
  wait_flags<1>(P, FL_SA + i * 8, tag);
  if (w < 4) {
    const u4v* tf = (const u4v*)&gTF + (nt * 5 + w) * 1280 + lane;
#pragma unroll
    for (int k = 0; k < 12; k++) W[k] = tf[k * 64];
    b_gemm_w<0, false, true, 12, 0>(smem, W, &gTF, ((nt * 5 + w) * 1280 + lane) * 16, acc, ao, (const char*)P.x, vo, 128, lds0, w, [] {});
    b_reduce<true>(smem, acc, w, lane);
#pragma unroll
    for (int mb = 0; mb < 4; mb++) put(w, mb, acc[mb]);
    lds_bar();
  } else {
    const u4v* tf = (const u4v*)&gTF + (nt * 5 + 4) * 1280 + (w & 3) * 64 + lane;
#pragma unroll
    for (int k = 0; k < 5; k++) W[k] = tf[k * 256];
    const int u0 = 5 * i + 2 * (w > 4 ? w - 5 : 0), u1 = w < 7 ? u0 + 1 : u0;
    WtUnit t0, t1;
    u32 ny = 0;
    auto side = [&] {
      t0 = wt_load(P, u0, lane);
      t1 = wt_load(P, u1, lane);
      const int row = 32 * (i & 63) + (lane & 31);
      ny = (lane >> 5) ? P.wi[row * 321 + 320] : P.wr[row * 321 + 320];
    };
    b_gemm_w<0, false, false, 12, 5>(smem, W, nullptr, 0, acc, ao, (const char*)P.x, vo, 128, lds0, w, side);
    const f4v r = b_reduce<false>(smem, acc, w, lane);
    put(4, w & 3, r);
    if (w > 4) {
      wt_store(P, u0, lane, t0);
      if (w < 7) wt_store(P, u1, lane, t1);
      else if (i < 64) {
        const u32 o = __shfl_xor(ny, 32);
        if (lane < 32) st32c(ws_rsrc(P.ws), WS_WT + 320 * 8192 + i * 128 + lane * 4, ny | (o << 16));
      }
    }
    lds_bar();
  }
  if (w < 5) {
    const rsrc_t rs = ws_rsrc(P.ws);
#pragma unroll
    for (int k = 0; k < 2; k++) {
      const int row = 8 * k + (lane >> 3), c = lane & 7;
      st128c(rs, WS_X1 + ((80 * nt + 16 * w + row) * 2048 + mt * 64 + 8 * c) * 2, lds128(stg + w * 2304 + row * 144 + c * 16));
    }
  }
}

constexpr int CT_A1 = 0, CT_A1C = 4096, CT_A2 = 8192, CT_TW1 = 24576, CT_TW2 = 40960, CT_XCH = 57344;
static_assert(sizeof(ColTabD) == CT_XCH, "table image");
DEVI u4v pk8(const f16v& a, int s) {
  u4v v = {pk2(a[8 * s], a[8 * s + 1]), pk2(a[8 * s + 2], a[8 * s + 3]), pk2(a[8 * s + 4], a[8 * s + 5]), pk2(a[8 * s + 6], a[8 * s + 7])};
  return v;
}
DEVI u4v neg8(u4v v) { return v ^ u4v{0x80008000u, 0x80008000u, 0x80008000u, 0x80008000u}; }
DEVI void column_tables(char* smem) {
  const int tid = fresh_tid(), lane = tid & 63, w = uni(tid >> 6);
  const u32 lds0 = (u32)(uintptr_t)(LAS char*)smem;
#pragma unroll
  for (int j = 0; j < 7; j++) glds16((const char*)&gCT + (w + 8 * j) * 1024, lane * 16, lds0 + (w + 8 * j) * 1024);
}
DEVI f2v lo2(f4v c) { return f2v{c[0], c[1]}; }
DEVI f2v hi2(f4v c) { return f2v{c[2], c[3]}; }
template <int SG> DEVI void twiddle(f16v& re, f16v& im, const char* tab) {
#pragma unroll
  for (int hq = 0; hq < 2; hq++) {
    f4v c[4];
#pragma unroll
    for (int q = 0; q < 4; q++) c[q] = __builtin_bit_cast(f4v, lds128(tab + (4 * hq + q) * 1024));
#pragma unroll
    for (int q = 0; q < 4; q++) {
      const int k = 2 * (4 * hq + q);
      const f2v a = {re[k], re[k + 1]}, b = {im[k], im[k + 1]}, cc = lo2(c[q]), ss = hi2(c[q]);
      const f2v nr = SG > 0 ? a * cc - b * ss : a * cc + b * ss;
      const f2v ni = SG > 0 ? b * cc + a * ss : b * cc - a * ss;
      re[k] = nr[0]; re[k + 1] = nr[1]; im[k] = ni[0]; im[k + 1] = ni[1];
    }
  }
}
DEVI void column_job(const Params& P, char* smem, int col, int f, int t, int lane) {
  const int r = lane & 31, h = lane >> 5;
  const rsrc_t rs = ws_rsrc(P.ws);
  const int cr = f == 0 ? 0 : f == 320 ? 1 : 2 * f;
  const bool real_in = (f == 0 || f == 320);
  const u32 zo = WS_X1 + (cr * 2048 + (32 * t + r) * 32 + 8 * h) * 2;
  u4v Zr[2], Zi[2];
#pragma unroll
  for (int ks = 0; ks < 2; ks++) {
    Zr[ks] = ld128c(rs, zo + 32 * ks);
    Zi[ks] = ld128c(rs, zo + 4096 + 32 * ks);
  }
  u32 wv[16];
#pragma unroll
  for (int i = 0; i < 16; i++) wv[i] = ld32c(rs, WS_WT + f * 8192 + (32 * t + (i & 3) + 8 * (i >> 2) + 4 * h) * 128 + r * 4);
  if (real_in) { Zi[0] = u4v{0u, 0u, 0u, 0u}; Zi[1] = Zi[0]; }
  const char* tb = smem + lane * 16;
  char* xch = smem + CT_XCH + col * 8192 + lane * 16;
  f16v re = {}, im = {};
#pragma unroll
  for (int ks = 0; ks < 2; ks++) {
    u4v c = lds128(tb + CT_A1 + ks * 1024), ns = lds128(tb + CT_A1 + 2048 + ks * 1024);
    re = mfma32(Zr[ks], c, re); re = mfma32(neg8(Zi[ks]), ns, re);
    im = mfma32(Zr[ks], ns, im); im = mfma32(Zi[ks], c, im);
  }
  twiddle<-1>(re, im, tb + CT_TW1 + t * 8192);
  u4v yr[4], yi[4];
  auto exchange = [&]() {
    yr[0] = pk8(re, 0); yr[1] = pk8(re, 1); yi[0] = pk8(im, 0); yi[1] = pk8(im, 1);
    char* mine = xch + t * 4096;
    sts128(mine, yr[0]); sts128(mine + 1024, yr[1]); sts128(mine + 2048, yi[0]); sts128(mine + 3072, yi[1]);
    lds_bar();
    const char* oth = xch + (t ^ 1) * 4096;
    yr[2] = lds128(oth); yr[3] = lds128(oth + 1024); yi[2] = lds128(oth + 2048); yi[3] = lds128(oth + 3072);
  };
  const char* a2 = tb + CT_A2 + t * 8192;
  const int ko = t * 4096;
  exchange();
  re = f16v{}; im = f16v{};
#pragma unroll
  for (int kk = 0; kk < 4; kk++) {
    u4v c = lds128(a2 + ((kk * 2048) ^ ko)), ns = lds128(a2 + ((kk * 2048) ^ ko) + 1024);
    re = mfma32(c, yr[kk], re); re = mfma32(ns, neg8(yi[kk]), re);
    im = mfma32(ns, yr[kk], im); im = mfma32(c, yi[kk], im);
  }
  const float wsc = real_in ? 0.5f : 1.f;
#pragma unroll
  for (int i = 0; i < 16; i += 2) {
    const f2v wr_ = f2v{bflo(wv[i]), bflo(wv[i + 1])} * wsc, wi_ = f2v{bfhi(wv[i]), bfhi(wv[i + 1])} * wsc;
    const f2v a = {re[i], re[i + 1]}, b = {im[i], im[i + 1]};
    const f2v nr = a * wr_ - b * wi_, ni = a * wi_ + b * wr_;
    re[i] = nr[0]; re[i + 1] = nr[1]; im[i] = ni[0]; im[i + 1] = ni[1];
  }
  lds_bar();
  exchange();
  re = f16v{}; im = f16v{};
#pragma unroll
  for (int kk = 0; kk < 4; kk++) {
    u4v c = lds128(a2 + ((kk * 2048) ^ ko)), ns = lds128(a2 + ((kk * 2048) ^ ko) + 1024);
    re = mfma32(yr[kk], c, re); re = mfma32(yi[kk], ns, re);
    im = mfma32(neg8(yr[kk]), ns, im); im = mfma32(yi[kk], c, im);
  }
  twiddle<1>(re, im, tb + CT_TW2 + t * 8192);
  u4v gr0 = pk8(re, 0), gr1 = pk8(re, 1), gi0 = pk8(im, 0), gi1 = pk8(im, 1);
  re = f16v{}; im = f16v{};
#pragma unroll
  for (int ks = 0; ks < 2; ks++) {
    u4v c = lds128(tb + CT_A1C + ks * 1024), ns = lds128(tb + CT_A1C + 2048 + ks * 1024);
    u4v gr = ks ? gr1 : gr0, gi = ks ? gi1 : gi0;
    re = mfma32(c, gr, re); re = mfma32(ns, gi, re);
    im = mfma32(ns, neg8(gr), im); im = mfma32(c, gi, im);
  }
  const u32 dst = WS_X2 + (cr * 2048 + (32 * t + r) * 32 + 8 * h) * 2;
#pragma unroll
  for (int g = 0; g < 4; g += 2) {
    st128c(rs, dst + 16 * g, swap_pack(pk2(re[4 * g], re[4 * g + 1]), pk2(re[4 * g + 2], re[4 * g + 3]),
                                       pk2(re[4 * g + 4], re[4 * g + 5]), pk2(re[4 * g + 6], re[4 * g + 7])));
    u4v vi = swap_pack(pk2(im[4 * g], im[4 * g + 1]), pk2(im[4 * g + 2], im[4 * g + 3]),
                       pk2(im[4 * g + 4], im[4 * g + 5]), pk2(im[4 * g + 6], im[4 * g + 7]));
    if (!real_in) st128c(rs, dst + 4096 + 16 * g, vi);
  }
}
DEVI void wot_job(const Params& P, char* img, int fr, int lane) {
  const int cb = fr / 20, ks = fr % 20;
  glds16((const char*)P.wo + (32 * ks * 640 + 16 * cb) * 2, (lane >> 1) * 1280 + (lane & 1) * 16, (u32)(uintptr_t)(LAS char*)img);
  wait_vm<0>();
  const char* a = img + (8 * (lane >> 4) + ((lane & 15) >> 2)) * 32 + (lane & 3) * 8;
  st128c(ws_rsrc(P.ws), WS_WOT + fr * 1024 + lane * 16, cat(ldstr(a), ldstr(a + 128)));
}
DEVI void b3_columns(const Params& P, char* smem, int i, u64 tag) {
  lds_bar();
  column_tables(smem);
  wait_flags<256>(P, FL_S2, tag);
  const int tid = fresh_tid(), w = uni(tid >> 6), lane = tid & 63;
  const int f = (w >> 1) == 0 ? i : 256 + i;
  if (w < 4 && f <= 320) column_job(P, smem, w >> 1, f, w & 1, lane);
  else {
    if (w >= 4 && i + 256 * (w - 4) < 800) wot_job(P, smem + CT_XCH + 16384 + (w - 4) * 1024, i + 256 * (w - 4), lane);
    lds_bar(); lds_bar(); lds_bar();
  }
}

DEVI void b45_out(const Params& P, char* smem, int mt, int nt, int wgi, u64 tag) {
  const u32 lds0 = (u32)(uintptr_t)(LAS char*)smem;
  const int p0 = mt * 64, n0 = nt * 80;
  constexpr int STG = 65536;
  const int tid = fresh_tid(), lane = tid & 63, w = uni(tid >> 6), l15 = lane & 15, g = lane >> 4;
  auto synth_off = [&] { const int o = w * 1024 + lane * 16, rho = o >> 7, s16 = (o >> 4) & 7, mbs = (s16 >> 1) ^ ((rho >> 1) & 3);
                         return (u32)((swap23(rho) * 2048 + p0 + mbs * 16 + (s16 & 1) * 8) * 2); };
  auto fetch_b = [&] {
    wait_flags<256>(P, FL_S3, tag);
    const u32 vo = synth_off();
#pragma unroll
    for (int t = 0; t < 10; t++) glds16c(P.ws + WS_X2 + t * (64 * 4096), vo, lds0 + t * 8192 + w * 1024);
    wait_flags<256>(P, FL_S1, tag);
    wait_flags<1>(P, FL_SB + wgi * 8, tag);
    wait_vm_bar<0>();
  };
  auto mk_co = [&](int (&ao)[4]) {
    const int rho0 = 16 * (g >> 1) + 4 * (g & 1) + (l15 >> 2);
#pragma unroll
    for (int mb = 0; mb < 4; mb++) ao[mb] = rho0 * 128 + ((mb ^ ((rho0 >> 1) & 3)) << 5) + (lane & 3) * 8;
  };
  const float sb = 2.f / (float)(S_ * D_);
  const int blk = w < 4 ? w : 4;
  auto stg = [&](int mb) { return smem + STG + (16 * mb + l15) * 176 + (16 * blk + 4 * g) * 2; };
  auto park = [&](int mb, const f4v& v) { sts64(stg(mb), u2v{pk2(v[0] * sb, v[1] * sb), pk2(v[2] * sb, v[3] * sb)}); };
  auto mix = [&](int mb, const f4v& v, float gg) {
    const float og = 1.f - gg;
    const u2v b = lds64(stg(mb));
    sts64(stg(mb), u2v{pk2(gg * v[0] + og * bflo(b[0]), gg * v[1] + og * bfhi(b[0])), pk2(gg * v[2] + og * bflo(b[1]), gg * v[3] + og * bfhi(b[1]))});
  };
  const rsrc_t rs = ws_rsrc(P.ws);
  if (w < 4) {
    u4v W[20], WO[20];
    f4v accb[4] = {}, acca[4] = {};
    int ao[4];
    mk_co(ao);
    const u4v* ti = (const u4v*)&gTI + (nt * 5 + w) * 1280 + lane;
#pragma unroll
    for (int i = 0; i < 20; i++) W[i] = ti[i * 64];
    fetch_b();
    __builtin_amdgcn_s_waitcnt(0x0F70);
    const u32 woff = WS_WOT + (nt * 5 + w) * 20480 + lane * 16;
    b_gemm_res<true>(smem, W, accb, ao, w, [&](auto J) { WO[J] = ld128c(rs, woff + J * 1024); });
    lds_bar();
#pragma unroll
    for (int mb = 0; mb < 4; mb++) park(mb, accb[mb]);
#pragma unroll
    for (int i = 14; i < 20; i++) WO[i] = ld128c(rs, woff + i * 1024);
    lds_bar();
    mk_ro(ao, l15, g);
    const u32 vo = rows_off(p0, false, w, lane);
    wait_flags<8, 1>(P, FL_S4 + mt * 64, tag);
    __builtin_amdgcn_s_waitcnt(0x0F70);
    float gate[4];
    b_gemm_w<1, true, true, 20, 4>(smem, WO, nullptr, 0, acca, ao, P.ws + WS_O, vo, 128, lds0, w, [&] {
#pragma unroll
      for (int mb = 0; mb < 4; mb++) gate[mb] = __builtin_bit_cast(float, ld32c(rs, WS_G + (p0 + 16 * mb + l15) * 4));
    });
#pragma unroll
    for (int mb = 0; mb < 4; mb++) mix(mb, acca[mb], gate[mb]);
    b_reduce<true>(smem, acca, w, lane);
  } else {
    const int kq = w & 3;
    u4v W[20];
    f4v acc[4] = {};
    int ao[4];
    mk_co(ao);
    const u4v* ti = (const u4v*)&gTI + (nt * 5 + 4) * 1280 + kq * 64 + lane;
#pragma unroll
    for (int i = 0; i < 5; i++) W[i] = ti[i * 256];
    fetch_b();
    __builtin_amdgcn_s_waitcnt(0x0F70);
    b_gemm_res<false>(smem, W, acc, ao, w, [] {});
    const u32 woff = WS_WOT + (nt * 5 + 4) * 20480 + kq * 1024 + lane * 16;
#pragma unroll
    for (int i = 0; i < 5; i++) W[i] = ld128c(rs, woff + i * 4096);
    const float gate = __builtin_bit_cast(float, ld32c(rs, WS_G + (p0 + 16 * kq + l15) * 4));
    park(kq, b_reduce<false>(smem, acc, w, lane));
#pragma unroll
    for (int mb = 0; mb < 4; mb++) acc[mb] = f4v{};
    mk_ro(ao, l15, g);
    const u32 vo = rows_off(p0, false, w, lane);
    wait_flags<8, 1>(P, FL_S4 + mt * 64, tag);
    __builtin_amdgcn_s_waitcnt(0x0F70);
    b_gemm_w<1, true, false, 20, 0>(smem, W, nullptr, 0, acc, ao, P.ws + WS_O, vo, 128, lds0, w, [] {});
    mix(kq, b_reduce<false>(smem, acc, w, lane), gate);
  }
  lds_bar();
  {
    const int t2 = fresh_tid();
    u4v v[2];
    u16* d[2];
#pragma unroll
    for (int k = 0; k < 2; k++) {
      const int ch = t2 + NTHR * k < 640 ? t2 + NTHR * k : t2, row = ch / 10, c = ch % 10;
      v[k] = lds128(smem + STG + row * 176 + c * 16);
      d[k] = P.out + s_of_p(p0 + row) * 640 + n0 + 8 * c;
    }
    *(u4v*)d[0] = v[0];
    if (t2 < 128) *(u4v*)d[1] = v[1];
  }
}

__global__ void __launch_bounds__(NTHR, 4) mega(Params P) {
  extern __shared__ __attribute__((aligned(16))) char smem[];
  asm volatile("" ::"s"(P.x), "s"(P.wq), "s"(P.wk), "s"(P.wv), "s"(P.wo), "s"(P.wr), "s"(P.wi), "s"(P.gw), "s"(P.out), "s"(P.ws));
  const int i = blockIdx.x & 255, xcd = i & 7, idx = i >> 3;
  const int mt = (xcd >> 1) * 8 + (idx >> 2), nt = (xcd & 1) * 4 + (idx & 3);
  if (blockIdx.x >= 256) {
    a_wg(P, smem, mt, nt, i);
    return;
  }
  __builtin_amdgcn_s_setprio(3);
  const u64 epoch = read_epoch(P);
  b1_kv(P, smem, (xcd >> 1) * 32 + idx, xcd & 1, idx);
  const u64 tag = tag_of(epoch);
  publish(P, FL_S1 + i * 8, tag);
  b2_fwd(P, smem, mt, nt, i, tag);
  publish(P, FL_S2 + i * 8, tag);
  __builtin_amdgcn_s_setprio(0);
  b3_columns(P, smem, i, tag);
  publish(P, FL_S3 + i * 8, tag);
  __builtin_amdgcn_s_setprio(2);
  if (i == 0 && uni(threadIdx.x >> 6) == 7) {
    spin_flags<256>(P, FL_SA, tag, threadIdx.x & 63);
    if ((threadIdx.x & 63) == 0) put_flag(P, FL_E, tag);
  }
  b45_out(P, smem, mt, nt, i, tag);
}

extern "C" void kernel_launch(void* const* d_in, const int* in_sizes, int n_in, void* d_out, int out_size, void* d_ws,
                              size_t ws_size, hipStream_t stream) {
  Params P;
  P.x = (const u16*)d_in[0];
  P.wq = (const u16*)d_in[1];
  P.wk = (const u16*)d_in[2];
  P.wv = (const u16*)d_in[3];
  P.wo = (const u16*)d_in[4];
  P.wr = (const u16*)d_in[6];
  P.wi = (const u16*)d_in[7];
  P.gw = (const u16*)d_in[8];
  P.out = (u16*)d_out;
  P.ws = (char*)d_ws;
  (void)hipFuncSetAttribute((const void*)mega, hipFuncAttributeMaxDynamicSharedMemorySize, LDS_BYTES);
  mega<<<512, NTHR, LDS_BYTES, stream>>>(P);
}
```
